```python
import torch

def _softmax(x, axis=-1):
    return torch.softmax(x, dim=axis)
def _gelu(x):
    return torch.nn.functional.gelu(x, approximate="tanh")
def _silu(x):
    return torch.nn.functional.silu(x)
def _sigmoid(x):
    return torch.sigmoid(x)
def _layernorm(x, axis=-1, eps=1e-5):
    m = x.mean(dim=axis, keepdim=True)
    v = x.var(dim=axis, keepdim=True, unbiased=False)
    return (x - m) / torch.sqrt(v + eps)
def _scatter_add(tgt, idx, src, axis=0):
    return tgt.clone().index_add_(axis, idx, src)
def _segment_sum(src, seg, n):
    out = torch.zeros((n,) + src.shape[1:], dtype=src.dtype)
    return out.index_add_(0, seg, src)
def _pad(x, pw):
    flat = tuple(v for pair in reversed(pw) for v in pair)
    return torch.nn.functional.pad(x, flat)

kernel_name = 'parallel_sum__compressed_kv_cla__dense_activation_gelu__decode'

def setup_inputs():

    _w = (lambda a: (lambda t: t.to(torch.bfloat16) if t.is_floating_point() else t)(torch.as_tensor(a)))
    def _A_setup():
        B, H, Sq, Sk, D, M = 2, 16, 1, 4096, 256, 448
        Dc = 4 * D
        return {
            "x": _w((lambda *s: torch.randn(*s, dtype=torch.bfloat16) * 0.05)(B, Sq, M)),
            "c_kv": _w((lambda *s: torch.randn(*s, dtype=torch.bfloat16) * 0.8)(B, Sk, Dc)),
            "w_q": _w((lambda *s: torch.randn(*s, dtype=torch.bfloat16) * 3.2)(M, H, D) * (M ** -0.5)),
            "w_uk": _w((lambda *s: torch.randn(*s, dtype=torch.bfloat16) * 3.2)(Dc, H, D) * (Dc ** -0.5)),
            "w_uv": _w((lambda *s: torch.randn(*s, dtype=torch.bfloat16) * 0.8)(Dc, H, D) * (Dc ** -0.5)),
            "w_o": _w((lambda *s: torch.randn(*s, dtype=torch.bfloat16) * 0.8)(H, D, M) * ((H * D) ** -0.5)),
        }
    _A_in = _A_setup()

    def _B_setup():
        B, S, D, F = 2, 1, 448, 1792
        return {
            "x": _w((lambda *s: torch.randn(*s, dtype=torch.bfloat16) * 0.05)(B, S, D)),
            "w1": _w((lambda *s: torch.randn(*s, dtype=torch.bfloat16) * 1.6)(D, F) * (D ** -0.5)),
            "b1": _w((lambda *s: torch.randn(*s, dtype=torch.bfloat16) * 1.6)(F) * 0.01),
            "w2": _w((lambda *s: torch.randn(*s, dtype=torch.bfloat16) * 0.2)(F, D) * (F ** -0.5)),
            "b2": _w((lambda *s: torch.randn(*s, dtype=torch.bfloat16) * 0.2)(D) * 0.01),
        }
    _B_in = _B_setup()

    return {
        "A_x": _A_in["x"],
        "A_c_kv": _A_in["c_kv"],
        "A_w_q": _A_in["w_q"],
        "A_w_uk": _A_in["w_uk"],
        "A_w_uv": _A_in["w_uv"],
        "A_w_o": _A_in["w_o"],
        "B_x": _B_in["x"],
        "B_w1": _B_in["w1"],
        "B_b1": _B_in["b1"],
        "B_w2": _B_in["w2"],
        "B_b2": _B_in["b2"],
    }

def reference(A_x, A_c_kv, A_w_q, A_w_uk, A_w_uv, A_w_o, B_x, B_w1, B_b1, B_w2, B_b2):
    def _A_ref(x, c_kv, w_q, w_uk, w_uv, w_o):
        B, Sq, M = x.shape[0], x.shape[1], x.shape[2]
        Sk = c_kv.shape[1]
        H, D = w_q.shape[1], w_q.shape[2]
        q = torch.einsum("bqm,mhd->bhqd", x, w_q)
        k = torch.einsum("bkc,chd->bhkd", c_kv, w_uk)
        v = torch.einsum("bkc,chd->bhkd", c_kv, w_uv)
        scores = torch.einsum("bhqd,bhkd->bhqk", q, k) * (D ** -0.5)
        off = Sk - Sq
        qi = torch.arange(Sq)[:, None]
        kj = torch.arange(Sk)[None, :]
        cmask = kj <= (qi + off)
        scores = torch.where(cmask[None, None], scores, -1e9)
        p = _softmax(scores, axis=-1)
        ctx = torch.einsum("bhqk,bhkd->bhqd", p, v)
        return torch.einsum("bhqd,hdm->bqm", ctx, w_o)

    def _B_ref(x, w1, b1, w2, b2):
        h = torch.einsum("bsd,df->bsf", x, w1) + b1
        h = _gelu(h)
        return torch.einsum("bsf,fd->bsd", h, w2) + b2

    x = A_x
    xn = _layernorm(x, axis=-1)
    return x + _A_ref(xn, A_c_kv, A_w_q, A_w_uk, A_w_uv, A_w_o) + _B_ref(xn, B_w1, B_b1, B_w2, B_b2)
```

```cpp
#include <hip/hip_runtime.h>
#include <hip/hip_bf16.h>
#include <stdint.h>

constexpr int B = 2, H = 16, SK = 4096, D = 256, M = 448, DC = 1024, F = 1792;
constexpr int NSPLIT = 128;
constexpr int KPS = SK / NSPLIT;

__device__ __forceinline__ float bf2f(unsigned short u) { return __uint_as_float(((unsigned)u) << 16); }
__device__ __forceinline__ float wave_sum(float v) {
    for (int o = 32; o > 0; o >>= 1) v += __shfl_xor(v, o);
    return v;
}
__device__ __forceinline__ float wave_max(float v) {
    for (int o = 32; o > 0; o >>= 1) v = fmaxf(v, __shfl_xor(v, o));
    return v;
}

__device__ __forceinline__ void compute_xn(const unsigned short* x, float* xn_lds  , int tid) {
    int wave = tid >> 6, lane = tid & 63;
    if (wave < 2) {
        float v[7]; float s = 0.f;
        for (int i = 0; i < 7; ++i) { v[i] = bf2f(x[wave * M + lane + 64 * i]); s += v[i]; }
        s = wave_sum(s); float mean = s / M;
        float ss = 0.f;
        for (int i = 0; i < 7; ++i) { float d = v[i] - mean; ss += d * d; }
        ss = wave_sum(ss); float var = ss / M;
        float r = 1.0f / sqrtf(var + 1e-5f);
        for (int i = 0; i < 7; ++i) xn_lds[wave * M + lane + 64 * i] = (v[i] - mean) * r;
    }
    __syncthreads();
}

__global__ void __launch_bounds__(256) k_qproj(const unsigned short* x, const unsigned short* wq, const unsigned short* w1,
                                               const unsigned short* b1, float* q, float* hmid) {
    __shared__ float xn[2 * M];
    __shared__ float red[2][8][32];
    int tid = threadIdx.x;
    compute_xn(x, xn, tid);
    int blk = blockIdx.x;
    const unsigned short* W; int ncols, c0; bool isq = blk < 128;
    if (isq) { W = wq; ncols = H * D; c0 = blk * 32; } else { W = w1; ncols = F; c0 = (blk - 128) * 32; }
    int col = tid & 31, rg = tid >> 5;
    float a0 = 0.f, a1 = 0.f;
    for (int m = rg; m < M; m += 8) {
        float w = bf2f(W[(size_t)m * ncols + c0 + col]);
        a0 += xn[m] * w; a1 += xn[M + m] * w;
    }
    red[0][rg][col] = a0; red[1][rg][col] = a1;
    __syncthreads();
    if (tid < 64) {
        int b = tid >> 5, c = tid & 31; float s = 0.f;
        for (int g = 0; g < 8; ++g) s += red[b][g][c];
        if (isq) q[b * H * D + c0 + c] = s;
        else {
            float hv = s + bf2f(b1[c0 + c]);
            float t = tanhf(0.7978845608028654f * (hv + 0.044715f * hv * hv * hv));
            hmid[b * F + c0 + c] = 0.5f * hv * (1.0f + t);
        }
    }
}

__global__ void __launch_bounds__(256) k_qabs(const float* q, const unsigned short* wuk, float* qabs) {
    int gw = (blockIdx.x * 256 + threadIdx.x) >> 6; int lane = threadIdx.x & 63;
    int nw = gridDim.x * 4;
    for (int r = gw; r < DC * H; r += nw) {
        int c = r / H, h = r % H;
        const unsigned short* w = wuk + (size_t)r * D + lane * 4;
        float a0 = 0.f, a1 = 0.f;
        for (int j = 0; j < 4; ++j) { float wv = bf2f(w[j]); a0 += wv * q[h * D + lane * 4 + j]; a1 += wv * q[H * D + h * D + lane * 4 + j]; }
        a0 = wave_sum(a0); a1 = wave_sum(a1);
        if (lane == 0) { qabs[(0 * H + h) * DC + c] = a0 * 0.0625f; qabs[(1 * H + h) * DC + c] = a1 * 0.0625f; }
    }
}

__global__ void __launch_bounds__(256) k_attn(const float* qabs, const unsigned short* ckv, float* pm, float* pl, float* pctx) {
    __shared__ float sc[H][KPS];
    int b = blockIdx.x / NSPLIT, sp = blockIdx.x % NSPLIT;
    int tid = threadIdx.x, wave = tid >> 6, lane = tid & 63;
    const unsigned short* kv = ckv + ((size_t)b * SK + sp * KPS) * DC;
    for (int kk = wave; kk < KPS; kk += 4) {
        float cv[16];
        for (int j = 0; j < 16; ++j) cv[j] = bf2f(kv[(size_t)kk * DC + lane * 16 + j]);
        for (int h = 0; h < H; ++h) {
            float a = 0.f;
            for (int j = 0; j < 16; ++j) a += cv[j] * qabs[(b * H + h) * DC + lane * 16 + j];
            a = wave_sum(a);
            if (lane == 0) sc[h][kk] = a;
        }
    }
    __syncthreads();
    if (tid < H) {
        float mx = -3.0e38f;
        for (int k = 0; k < KPS; ++k) mx = fmaxf(mx, sc[tid][k]);
        float l = 0.f;
        for (int k = 0; k < KPS; ++k) { float p = __expf(sc[tid][k] - mx); sc[tid][k] = p; l += p; }
        pm[(b * NSPLIT + sp) * H + tid] = mx; pl[(b * NSPLIT + sp) * H + tid] = l;
    }
    __syncthreads();
    float acc[H][4];
    for (int h = 0; h < H; ++h) for (int j = 0; j < 4; ++j) acc[h][j] = 0.f;
    for (int k = 0; k < KPS; ++k) {
        float cv[4];
        for (int j = 0; j < 4; ++j) cv[j] = bf2f(kv[(size_t)k * DC + tid * 4 + j]);
        for (int h = 0; h < H; ++h) { float p = sc[h][k]; for (int j = 0; j < 4; ++j) acc[h][j] += p * cv[j]; }
    }
    for (int h = 0; h < H; ++h) for (int j = 0; j < 4; ++j) pctx[(((size_t)b * NSPLIT + sp) * H + h) * DC + tid * 4 + j] = acc[h][j];
}

__global__ void __launch_bounds__(256) k_ctx(const float* pm, const float* pl, const float* pctx, const unsigned short* wuv, float* ctxp) {
    __shared__ float wgt[2][NSPLIT];
    __shared__ float cc_s[2][64];
    int h = blockIdx.x >> 4, cc = blockIdx.x & 15; int tid = threadIdx.x;
    if (tid < 128) {
        int b = tid >> 6, lane = tid & 63;
        float m0 = pm[(b * NSPLIT + lane) * H + h], m1 = pm[(b * NSPLIT + lane + 64) * H + h];
        float gm = wave_max(fmaxf(m0, m1));
        float e0 = __expf(m0 - gm), e1 = __expf(m1 - gm);
        float L = wave_sum(e0 * pl[(b * NSPLIT + lane) * H + h] + e1 * pl[(b * NSPLIT + lane + 64) * H + h]);
        wgt[b][lane] = e0 / L; wgt[b][lane + 64] = e1 / L;
    }
    __syncthreads();
    {
        int b = tid >> 7, half = (tid >> 6) & 1, c = tid & 63;
        float a = 0.f;
        for (int s = half * 64; s < half * 64 + 64; ++s) a += wgt[b][s] * pctx[(((size_t)b * NSPLIT + s) * H + h) * DC + cc * 64 + c];
        __shared__ float tmp[2][2][64];
        tmp[b][half][c] = a;
        __syncthreads();
        if (tid < 128) { int bb = tid >> 6; cc_s[bb][c] = tmp[bb][0][c] + tmp[bb][1][c]; }
        __syncthreads();
    }
    float a0 = 0.f, a1 = 0.f;
    for (int c = 0; c < 64; ++c) {
        float w = bf2f(wuv[((size_t)(cc * 64 + c) * H + h) * D + tid]);
        a0 += cc_s[0][c] * w; a1 += cc_s[1][c] * w;
    }
    ctxp[((size_t)cc * 2 + 0) * H * D + h * D + tid] = a0;
    ctxp[((size_t)cc * 2 + 1) * H * D + h * D + tid] = a1;
}

__global__ void __launch_bounds__(256) k_out(const unsigned short* x, const float* ctxp, const float* hmid, const unsigned short* wo,
                                             const unsigned short* w2, const unsigned short* b2, __hip_bfloat16* out) {
    __shared__ float red[2][16][16];
    int tid = threadIdx.x; int col = tid & 15, rg = tid >> 4; int m0 = blockIdx.x * 16;
    float a0 = 0.f, a1 = 0.f;
    for (int r = rg; r < H * D; r += 16) {
        float c0 = 0.f, c1 = 0.f;
        for (int p = 0; p < 16; ++p) { c0 += ctxp[((size_t)p * 2 + 0) * H * D + r]; c1 += ctxp[((size_t)p * 2 + 1) * H * D + r]; }
        float w = bf2f(wo[(size_t)r * M + m0 + col]);
        a0 += c0 * w; a1 += c1 * w;
    }
    for (int r = rg; r < F; r += 16) {
        float w = bf2f(w2[(size_t)r * M + m0 + col]);
        a0 += hmid[r] * w; a1 += hmid[F + r] * w;
    }
    red[0][rg][col] = a0; red[1][rg][col] = a1;
    __syncthreads();
    if (tid < 32) {
        int b = tid >> 4, c = tid & 15; float s = 0.f;
        for (int g = 0; g < 16; ++g) s += red[b][g][c];
        s += bf2f(b2[m0 + c]) + bf2f(x[b * M + m0 + c]);
        out[b * M + m0 + c] = __float2bfloat16(s);
    }
}

extern "C" void kernel_launch(void* const* d_in, const int* in_sizes, int n_in,
                              void* d_out, int out_size, void* d_ws, size_t ws_size,
                              hipStream_t stream) {
    const unsigned short* x = (const unsigned short*)d_in[0];
    const unsigned short* ckv = (const unsigned short*)d_in[1];
    const unsigned short* wq = (const unsigned short*)d_in[2];
    const unsigned short* wuk = (const unsigned short*)d_in[3];
    const unsigned short* wuv = (const unsigned short*)d_in[4];
    const unsigned short* wo = (const unsigned short*)d_in[5];
    const unsigned short* w1 = (const unsigned short*)d_in[7];
    const unsigned short* b1 = (const unsigned short*)d_in[8];
    const unsigned short* w2 = (const unsigned short*)d_in[9];
    const unsigned short* b2 = (const unsigned short*)d_in[10];
    float* ws = (float*)d_ws;
    float* q = ws;
    float* hmid = q + 2 * H * D;
    float* qabs = hmid + 2 * F;
    float* pm = qabs + 2 * H * DC;
    float* pl = pm + 2 * NSPLIT * H;
    float* ctxp = pl + 2 * NSPLIT * H;
    float* pctx = ctxp + 16 * 2 * H * D;
    k_qproj<<<128 + 56, 256, 0, stream>>>(x, wq, w1, b1, q, hmid);
    k_qabs<<<1024, 256, 0, stream>>>(q, wuk, qabs);
    k_attn<<<B * NSPLIT, 256, 0, stream>>>(qabs, ckv, pm, pl, pctx);
    k_ctx<<<H * 16, 256, 0, stream>>>(pm, pl, pctx, wuv, ctxp);
    k_out<<<M / 16, 256, 0, stream>>>(x, ctxp, hmid, wo, w2, b2, (__hip_bfloat16*)d_out);
}
```

```cpp
#include <hip/hip_runtime.h>
#include <hip/hip_bf16.h>
#include <stdint.h>

#define GAS __attribute__((address_space(1)))
#define LAS __attribute__((address_space(3)))
typedef unsigned v4u __attribute__((ext_vector_type(4)));
typedef unsigned v2u __attribute__((ext_vector_type(2)));
typedef float f32x4 __attribute__((ext_vector_type(4)));
typedef short bf16x8 __attribute__((ext_vector_type(8)));
typedef short s16x4 __attribute__((ext_vector_type(4)));
typedef GAS unsigned gu32;
typedef GAS unsigned long long gu64;
#define RLX_AGENT __ATOMIC_RELAXED, __HIP_MEMORY_SCOPE_AGENT

#define STAMPS 0

namespace {
constexpr int B = 2, H = 16, SK = 4096, D = 256, M = 448, DC = 1024, F = 1792;
constexpr int NCU = 256;
constexpr int NSPLIT = 128, KPS = 32;

constexpr size_t CTRL_OFF = 0, CTRL_BYTES = 32768;
constexpr size_t QP_OFF = CTRL_OFF + CTRL_BYTES;
constexpr size_t HP_OFF = QP_OFF + 4 * 2 * 4096 * 4;
constexpr size_t QABS_OFF = HP_OFF + 65536;
constexpr size_t ML_OFF = QABS_OFF + 65536;
constexpr size_t G4_OFF = ML_OFF + 32768;
constexpr size_t G4_BYTES = (size_t)256 * 512 * 8;
constexpr int NC5 = 112;
constexpr size_t G5_OFF = G4_OFF + G4_BYTES;
constexpr size_t G5_BYTES = (size_t)NC5 * 256 * 8 * 8;
constexpr size_t PCTX_OFF = G5_OFF + G5_BYTES;
constexpr size_t PCTX_BYTES = (size_t)2 * 128 * 16 * 1024 * 2;
constexpr size_t STAMP_OFF = PCTX_OFF + PCTX_BYTES;
constexpr size_t WS_NEED = STAMP_OFF + 256 * 16 * 8;

constexpr int CKV_STRIDE = 2064;
constexpr int L_CKV = 0;
constexpr int L_WUV = L_CKV + 32 * CKV_STRIDE;
constexpr int L_WO = L_WUV + 32768;
constexpr int L_W2 = L_WO + 14336;
constexpr int L_SC = L_W2 + 7168;
constexpr int L_P = L_SC + 16384;
constexpr int L_QS = L_P + 1024;
constexpr int L_HM = L_QS + 256;
constexpr int L_CC = L_HM + 64;
constexpr int L_X6 = L_CC + 512;
constexpr int L_CTX = L_X6 + 2048;
constexpr int LDS_BYTES = L_CTX + 128;
static_assert(LDS_BYTES <= 160 * 1024, "lds");

constexpr unsigned kSpinCap = 1u << 18;

__device__ __forceinline__ float bf2f(unsigned short u) { return __uint_as_float(((unsigned)u) << 16); }
__device__ __forceinline__ float bflo(unsigned u) { return __uint_as_float(u << 16); }
__device__ __forceinline__ float bfhi(unsigned u) { return __uint_as_float(u & 0xffff0000u); }
__device__ __forceinline__ unsigned f2bf(float f) { unsigned u = __float_as_uint(f); return (u + 0x7fffu + ((u >> 16) & 1u)) >> 16; }
__device__ __forceinline__ unsigned pack2(float a, float b) { return f2bf(a) | (f2bf(b) << 16); }
__device__ __forceinline__ float wsum(float v) { for (int o = 32; o > 0; o >>= 1) v += __shfl_xor(v, o); return v; }
__device__ __forceinline__ float wmax(float v) { for (int o = 32; o > 0; o >>= 1) v = fmaxf(v, __shfl_xor(v, o)); return v; }
__device__ __forceinline__ unsigned long long rt() { return __builtin_amdgcn_s_memrealtime(); }

__global__ void __launch_bounds__(256) k1(const unsigned short* __restrict__ x, const unsigned short* __restrict__ wq,
                                          const unsigned short* __restrict__ w1, unsigned char* __restrict__ ws) {
    __shared__ float xn[2][M];
    __shared__ float red[4][2][64];
    const int tid = threadIdx.x, wave = tid >> 6, lane = tid & 63;
    const int blk = blockIdx.x;
    const bool isq = blk < 256;
    const int idx = isq ? blk : blk - 256;
    const int mq = idx & 3, cg = idx >> 2;
    const int ncols = isq ? 4096 : F;
    const unsigned short* W = (isq ? wq : w1) + (size_t)(mq * 112) * ncols + cg * 64;
    const int r = tid >> 3, cch = tid & 7;
    v4u wv[4];
#pragma unroll
    for (int i = 0; i < 4; ++i) {
        const int row = r + 32 * i;
        if (i < 3 || r < 16) wv[i] = *(const v4u*)(W + (size_t)row * ncols + cch * 8);
        else wv[i] = (v4u){0u, 0u, 0u, 0u};
    }
    {
        const int nb = gridDim.x;
        v4u z = {0u, 0u, 0u, 0u};
        const size_t tot16 = (G4_BYTES + G5_BYTES) / 16;
        v4u* g = (v4u*)(ws + G4_OFF);
        for (size_t i = (size_t)blk * 256 + tid; i < tot16; i += (size_t)nb * 256) g[i] = z;
        if (blk == 0) { v4u* c = (v4u*)(ws + CTRL_OFF); for (int i = tid; i < (int)(CTRL_BYTES / 16); i += 256) c[i] = z; }
    }
    if (wave < 2) {
        float v[7]; float s = 0.f;
#pragma unroll
        for (int i = 0; i < 7; ++i) { v[i] = bf2f(x[wave * M + lane + 64 * i]); s += v[i]; }
        s = wsum(s); const float mean = s * (1.0f / M);
        float ss = 0.f;
#pragma unroll
        for (int i = 0; i < 7; ++i) { const float d = v[i] - mean; ss += d * d; }
        ss = wsum(ss);
        const float rs = 1.0f / sqrtf(ss * (1.0f / M) + 1e-5f);
#pragma unroll
        for (int i = 0; i < 7; ++i) xn[wave][lane + 64 * i] = (v[i] - mean) * rs;
    }
    __syncthreads();
    float a[2][8];
#pragma unroll
    for (int j = 0; j < 8; ++j) { a[0][j] = 0.f; a[1][j] = 0.f; }
#pragma unroll
    for (int i = 0; i < 4; ++i) {
        int row = mq * 112 + r + 32 * i; if (row >= M) row = M - 1;
        const float x0 = xn[0][row], x1 = xn[1][row];
#pragma unroll
        for (int j = 0; j < 4; ++j) {
            const float lo = bflo(wv[i][j]), hi = bfhi(wv[i][j]);
            a[0][2 * j] += x0 * lo; a[0][2 * j + 1] += x0 * hi;
            a[1][2 * j] += x1 * lo; a[1][2 * j + 1] += x1 * hi;
        }
    }
#pragma unroll
    for (int b = 0; b < 2; ++b)
#pragma unroll
        for (int j = 0; j < 8; ++j) { float v = a[b][j]; v += __shfl_xor(v, 8); v += __shfl_xor(v, 16); v += __shfl_xor(v, 32); a[b][j] = v; }
    if (lane < 8) {
#pragma unroll
        for (int b = 0; b < 2; ++b)
#pragma unroll
            for (int j = 0; j < 8; ++j) red[wave][b][lane * 8 + j] = a[b][j];
    }
    __syncthreads();
    if (tid < 128) {
        const int b = tid >> 6, c = tid & 63;
        const float s = red[0][b][c] + red[1][b][c] + red[2][b][c] + red[3][b][c];
        float* dst = (float*)(ws + (isq ? QP_OFF : HP_OFF));
        dst[((size_t)mq * 2 + b) * ncols + cg * 64 + c] = s;
    }
}

__device__ __forceinline__ bool spin_fail(unsigned& spins, gu32* tmo) {
    __builtin_amdgcn_s_sleep(1);
    if (++spins > kSpinCap) { __hip_atomic_store(tmo, 1u, RLX_AGENT); return true; }
    return (spins & 1023u) == 0u && __builtin_amdgcn_readfirstlane(__hip_atomic_load(tmo, RLX_AGENT));
}
__device__ __forceinline__ void wait_cnt(gu32* word, unsigned want, gu32* tmo) {
    for (unsigned spins = 0;;) {
        if ((unsigned)__builtin_amdgcn_readfirstlane(__hip_atomic_load(word, RLX_AGENT)) >= want) return;
        if (spin_fail(spins, tmo)) return;
    }
}

#define GLDS(src, dst) __builtin_amdgcn_global_load_lds((const unsigned*)(src), (LAS unsigned*)(dst), 16, 0, 0)
#define BAR() __builtin_amdgcn_s_barrier()
#define LGKM0() asm volatile("s_waitcnt lgkmcnt(0)" ::: "memory")
#define VM0() asm volatile("s_waitcnt vmcnt(0)" ::: "memory")
#define STAMP(i) do {} while (0)

__global__ void __launch_bounds__(576, 1) k2(const unsigned short* __restrict__ x, const unsigned short* __restrict__ ckv,
                                             const unsigned short* __restrict__ wuk, const unsigned short* __restrict__ wuv,
                                             const unsigned short* __restrict__ wo, const unsigned short* __restrict__ b1,
                                             const unsigned short* __restrict__ w2, const unsigned short* __restrict__ b2,
                                             unsigned char* __restrict__ ws, unsigned short* __restrict__ out) {
    extern __shared__ __attribute__((aligned(16))) unsigned char lds_raw[];
    LAS unsigned char* L = (LAS unsigned char*)lds_raw;
    const int tid = threadIdx.x, wave = __builtin_amdgcn_readfirstlane(tid >> 6), lane = tid & 63;
    const int cu = blockIdx.x;
    const int hh = cu >> 4, sl = cu & 15;
    const int bb = cu >> 7, sp = cu & 127;
    gu32* ctrl = (gu32*)(ws + CTRL_OFF);
    gu32* tmo = ctrl + 4096;

    if (wave == 8) {
        const unsigned short* src = ckv + ((size_t)bb * SK + (size_t)sp * KPS) * DC;
#pragma unroll 4
        for (int i = 0; i < 64; ++i) GLDS(src + (size_t)i * 512 + lane * 8, L + L_CKV + (i >> 1) * CKV_STRIDE + (i & 1) * 1024);
        BAR();
        BAR();
        {
#pragma unroll 4
            for (int i = 0; i < 32; ++i) {
                const int c = 64 * sl + 2 * i + (lane >> 5);
                GLDS(wuv + ((size_t)c * H + hh) * D + (lane & 31) * 8, L + L_WUV + i * 1024);
            }
            const unsigned short* so = wo + ((size_t)hh * D + sl * 16) * M;
#pragma unroll 2
            for (int i = 0; i < 14; ++i) GLDS(so + (size_t)i * 512 + lane * 8, L + L_WO + i * 1024);
            const unsigned short* s2 = w2 + (size_t)cu * 7 * M;
#pragma unroll 2
            for (int i = 0; i < 6; ++i) GLDS(s2 + (size_t)i * 512 + lane * 8, L + L_W2 + i * 1024);
            if (lane < 8) GLDS(s2 + (size_t)6 * 512 + lane * 8, L + L_W2 + 6 * 1024);
        }
        asm volatile("s_waitcnt vmcnt(53)" ::: "memory");
        BAR();
        BAR();
        BAR();
        BAR();
        VM0();
        BAR();
        BAR();
        BAR();
        BAR();
        BAR();
        BAR();
        if (cu < NC5) BAR();
        return;
    }

    STAMP(0);
    const __amdgpu_buffer_rsrc_t rs_ws = __builtin_amdgcn_make_buffer_rsrc(ws, 0, (int)WS_NEED, 0x00020000);
    v4u wk[4];
#pragma unroll
    for (int i = 0; i < 4; ++i) {
        const int c = 64 * sl + 8 * wave + 2 * i + (lane >> 5);
        wk[i] = *(const v4u*)(wuk + ((size_t)c * H + hh) * D + (lane & 31) * 8);
    }
    float qv[2][8];
    {
        const float* qp = (const float*)(ws + QP_OFF);
#pragma unroll
        for (int b = 0; b < 2; ++b) {
            f32x4 s0 = {0.f, 0.f, 0.f, 0.f}, s1 = {0.f, 0.f, 0.f, 0.f};
#pragma unroll
            for (int p = 0; p < 4; ++p) {
                const float* a = qp + ((size_t)p * 2 + b) * 4096 + hh * D + (lane & 31) * 8;
                s0 += *(const f32x4*)a; s1 += *(const f32x4*)(a + 4);
            }
#pragma unroll
            for (int j = 0; j < 4; ++j) { qv[b][j] = s0[j]; qv[b][4 + j] = s1[j]; }
        }
    }
    if (wave == 1 && lane < 14) {
        const int b = lane / 7, r = lane % 7, f = cu * 7 + r;
        const float* hp = (const float*)(ws + HP_OFF);
        float hv = bf2f(b1[f]);
#pragma unroll
        for (int p = 0; p < 4; ++p) hv += hp[((size_t)p * 2 + b) * F + f];
        const float t = tanhf(0.7978845608028654f * (hv + 0.044715f * hv * hv * hv));
        ((LAS float*)(L + L_HM))[b * 8 + r] = 0.5f * hv * (1.0f + t);
    }
    {
        const float QS = 0.0625f * 1.4426950408889634f;
#pragma unroll
        for (int i = 0; i < 4; ++i) {
            float a0 = 0.f, a1 = 0.f;
#pragma unroll
            for (int j = 0; j < 4; ++j) {
                const float lo = bflo(wk[i][j]), hi = bfhi(wk[i][j]);
                a0 += lo * qv[0][2 * j] + hi * qv[0][2 * j + 1];
                a1 += lo * qv[1][2 * j] + hi * qv[1][2 * j + 1];
            }
#pragma unroll
            for (int o = 1; o < 32; o <<= 1) { a0 += __shfl_xor(a0, o); a1 += __shfl_xor(a1, o); }
            if ((lane & 31) == 0) {
                const int r = 8 * wave + 2 * i + (lane >> 5);
                ((LAS unsigned short*)(L + L_QS))[r] = (unsigned short)f2bf(a0 * QS);
                ((LAS unsigned short*)(L + L_QS))[64 + r] = (unsigned short)f2bf(a1 * QS);
            }
        }
    }
    STAMP(1);
    LGKM0();
    BAR();
    if (wave == 0) {
        if (lane < 16) {
            const int b = lane >> 3, ch = lane & 7;
            const v4u v = *(const LAS v4u*)(L + L_QS + b * 128 + ch * 16);
            __builtin_amdgcn_raw_buffer_store_b128(v, rs_ws, (int)(QABS_OFF + (((size_t)b * H + hh) * DC + 64 * sl + ch * 8) * 2), 0, 16);
        }
        VM0();
        if (lane < 32) __hip_atomic_fetch_add(ctrl + lane * 64, 1u, RLX_AGENT);
        wait_cnt(ctrl + (cu & 31) * 64, NCU, tmo);
    }
    STAMP(2);
    BAR();
    bf16x8 qa[4];
#pragma unroll
    for (int i = 0; i < 4; ++i) {
        const v4u v = __builtin_amdgcn_raw_buffer_load_b128(rs_ws, (int)(QABS_OFF + (((size_t)bb * H + (lane & 15)) * DC + 128 * wave + 32 * i + 8 * (lane >> 4)) * 2), 0, 16);
        qa[i] = __builtin_bit_cast(bf16x8, v);
    }
    VM0();
    STAMP(3);
    BAR();
    {
        f32x4 acc[2] = {{0.f, 0.f, 0.f, 0.f}, {0.f, 0.f, 0.f, 0.f}};
#pragma unroll
        for (int nt = 0; nt < 2; ++nt)
#pragma unroll
            for (int i = 0; i < 4; ++i) {
                const v4u v = *(const LAS v4u*)(L + L_CKV + (16 * nt + (lane & 15)) * CKV_STRIDE + (128 * wave + 32 * i + 8 * (lane >> 4)) * 2);
                acc[nt] = __builtin_amdgcn_mfma_f32_16x16x32_bf16(qa[i], __builtin_bit_cast(bf16x8, v), acc[nt], 0, 0, 0);
            }
        LAS float* sc = (LAS float*)(L + L_SC);
#pragma unroll
        for (int nt = 0; nt < 2; ++nt)
#pragma unroll
            for (int r = 0; r < 4; ++r) sc[(wave * 16 + 4 * (lane >> 4) + r) * 32 + 16 * nt + (lane & 15)] = acc[nt][r];
    }
    LGKM0();
    BAR();
    {
        const int h = tid >> 5, key = tid & 31;
        const LAS float* sc = (const LAS float*)(L + L_SC);
        float s = 0.f;
#pragma unroll
        for (int w = 0; w < 8; ++w) s += sc[(w * 16 + h) * 32 + key];
        float mx = s;
#pragma unroll
        for (int o = 1; o < 32; o <<= 1) mx = fmaxf(mx, __shfl_xor(mx, o));
        const unsigned pb = f2bf(__builtin_amdgcn_exp2f(s - mx));
        float l = __uint_as_float(pb << 16);
#pragma unroll
        for (int o = 1; o < 32; o <<= 1) l += __shfl_xor(l, o);
        ((LAS unsigned short*)(L + L_P))[h * 32 + key] = (unsigned short)pb;
        if (key == 0) {
            const unsigned long long v = (unsigned long long)__float_as_uint(mx) | ((unsigned long long)__float_as_uint(l) << 32);
            __hip_atomic_store((gu64*)(ws + ML_OFF) + ((size_t)h * 2 + bb) * 128 + sp, v, RLX_AGENT);
        }
    }
    LGKM0();
    BAR();
    {
        const bf16x8 pf = __builtin_bit_cast(bf16x8, *(const LAS v4u*)(L + L_P + (lane & 15) * 64 + (lane >> 4) * 16));
        const int g = lane >> 4, q = (lane & 15) >> 2, p = lane & 3;
        gu64* dst = (gu64*)(ws + PCTX_OFF + ((((size_t)bb * NSPLIT + sp) * H + (lane & 15)) * DC + 128 * wave + 4 * g) * 2);
#pragma unroll
        for (int t = 0; t < 8; ++t) {
            const int c0 = 128 * wave + 16 * t;
            const LAS s16x4* a0 = (const LAS s16x4*)(L + L_CKV + (8 * g + q) * CKV_STRIDE + c0 * 2 + 8 * p);
            const LAS s16x4* a1 = (const LAS s16x4*)(L + L_CKV + (8 * g + 4 + q) * CKV_STRIDE + c0 * 2 + 8 * p);
            const s16x4 lo = __builtin_amdgcn_ds_read_tr16_b64_v4i16((LAS s16x4*)a0);
            const s16x4 hi = __builtin_amdgcn_ds_read_tr16_b64_v4i16((LAS s16x4*)a1);
            bf16x8 af;
            af[0] = lo[0]; af[1] = lo[1]; af[2] = lo[2]; af[3] = lo[3]; af[4] = hi[0]; af[5] = hi[1]; af[6] = hi[2]; af[7] = hi[3];
            f32x4 o = {0.f, 0.f, 0.f, 0.f};
            o = __builtin_amdgcn_mfma_f32_16x16x32_bf16(af, pf, o, 0, 0, 0);
            const unsigned long long v = (unsigned long long)pack2(o[0], o[1]) | ((unsigned long long)pack2(o[2], o[3]) << 32);
            __hip_atomic_store(dst + t * 4, v, RLX_AGENT);
        }
    }
    VM0();
    STAMP(4);
    BAR();
    if (wave == 0) {
        if (lane < 32) __hip_atomic_fetch_add(ctrl + 2048 + lane * 64, 1u, RLX_AGENT);
        wait_cnt(ctrl + 2048 + (cu & 31) * 64, NCU, tmo);
    }
    STAMP(5);
    BAR();
    {
        v4u ml[2], pc[4];
#pragma unroll
        for (int b = 0; b < 2; ++b) ml[b] = __builtin_amdgcn_raw_buffer_load_b128(rs_ws, (int)(ML_OFF + (((size_t)hh * 2 + b) * 128 + 2 * lane) * 8), 0, 16);
        const int j = lane >> 3, ch = lane & 7;
#pragma unroll
        for (int i = 0; i < 4; ++i) {
            const int b = i >> 1, s = 16 * wave + (i & 1) * 8 + j;
            pc[i] = __builtin_amdgcn_raw_buffer_load_b128(rs_ws, (int)(PCTX_OFF + ((((size_t)b * NSPLIT + s) * H + hh) * DC + 64 * sl + ch * 8) * 2), 0, 16);
        }
        float wg[2][2];
#pragma unroll
        for (int b = 0; b < 2; ++b) {
            const float m0 = __uint_as_float(ml[b][0]), l0 = __uint_as_float(ml[b][1]), m1 = __uint_as_float(ml[b][2]), l1 = __uint_as_float(ml[b][3]);
            const float gm = wmax(fmaxf(m0, m1));
            const float e0 = __builtin_amdgcn_exp2f(m0 - gm), e1 = __builtin_amdgcn_exp2f(m1 - gm);
            const float Ls = wsum(e0 * l0 + e1 * l1);
            const float inv = 1.0f / Ls;
            wg[b][0] = e0 * inv; wg[b][1] = e1 * inv;
        }
        float acc[2][8];
#pragma unroll
        for (int k = 0; k < 8; ++k) { acc[0][k] = 0.f; acc[1][k] = 0.f; }
#pragma unroll
        for (int i = 0; i < 4; ++i) {
            const int b = i >> 1, s = 16 * wave + (i & 1) * 8 + j;
            const float w0 = __shfl(wg[b][0], s >> 1), w1 = __shfl(wg[b][1], s >> 1);
            const float w = (s & 1) ? w1 : w0;
#pragma unroll
            for (int k = 0; k < 4; ++k) { acc[b][2 * k] += w * bflo(pc[i][k]); acc[b][2 * k + 1] += w * bfhi(pc[i][k]); }
        }
#pragma unroll
        for (int b = 0; b < 2; ++b)
#pragma unroll
            for (int k = 0; k < 8; ++k) { float v = acc[b][k]; v += __shfl_xor(v, 8); v += __shfl_xor(v, 16); v += __shfl_xor(v, 32); acc[b][k] = v; }
        LAS float* red = (LAS float*)(L + L_SC);
        if (lane < 8) {
#pragma unroll
            for (int b = 0; b < 2; ++b)
#pragma unroll
                for (int k = 0; k < 8; ++k) red[(wave * 2 + b) * 64 + lane * 8 + k] = acc[b][k];
        }
    }
    LGKM0();
    BAR();
    if (tid < 128) {
        const LAS float* red = (const LAS float*)(L + L_SC);
        float s = 0.f;
#pragma unroll
        for (int w = 0; w < 8; ++w) s += red[w * 128 + tid];
        ((LAS float*)(L + L_CC))[tid] = s;
    }
    LGKM0();
    BAR();
    {
        const int dp = tid & 127, cq = tid >> 7;
        const LAS float* cc = (const LAS float*)(L + L_CC);
        const LAS unsigned* wv = (const LAS unsigned*)(L + L_WUV);
        float a00 = 0.f, a01 = 0.f, a10 = 0.f, a11 = 0.f;
#pragma unroll
        for (int c = 16 * cq; c < 16 * cq + 16; ++c) {
            const unsigned w = wv[c * 128 + dp];
            const float lo = bflo(w), hi = bfhi(w), c0 = cc[c], c1 = cc[64 + c];
            a00 += c0 * lo; a01 += c0 * hi; a10 += c1 * lo; a11 += c1 * hi;
        }
        LAS float* red = (LAS float*)(L + L_SC + 8192);
        red[(cq * 2 + 0) * 256 + 2 * dp] = a00; red[(cq * 2 + 0) * 256 + 2 * dp + 1] = a01;
        red[(cq * 2 + 1) * 256 + 2 * dp] = a10; red[(cq * 2 + 1) * 256 + 2 * dp + 1] = a11;
    }
    LGKM0();
    BAR();
    {
        const LAS float* red = (const LAS float*)(L + L_SC + 8192);
        const float s = red[tid] + red[512 + tid] + red[1024 + tid] + red[1536 + tid];
        const int b = tid >> 8, d = tid & 255;
        const unsigned long long v = (1ull << 32) | (unsigned long long)__float_as_uint(s);
        __hip_atomic_store((gu64*)(ws + G4_OFF) + (((size_t)hh * 16 + (d >> 4)) * 16 + sl) * 32 + b * 16 + (d & 15), v, RLX_AGENT);
    }
    STAMP(6);
    {
        gu64* g = (gu64*)(ws + G4_OFF) + ((size_t)cu * 16 + 2 * wave) * 32 + lane;
        unsigned long long v;
        for (unsigned spins = 0;;) {
            v = __hip_atomic_load(g, RLX_AGENT);
            if (__all((unsigned)(v >> 32) == 1u)) break;
            if (spin_fail(spins, tmo)) break;
        }
        ((LAS float*)(L + L_X6))[2 * wave * 32 + lane] = __uint_as_float((unsigned)v);
    }
    STAMP(7);
    LGKM0();
    BAR();
    if (tid < 32) {
        const LAS float* x6 = (const LAS float*)(L + L_X6);
        float s = 0.f;
#pragma unroll
        for (int c = 0; c < 16; ++c) s += x6[c * 32 + tid];
        ((LAS float*)(L + L_CTX))[tid] = s;
    }
    LGKM0();
    BAR();
    if (tid < M) {
        const LAS float* cx = (const LAS float*)(L + L_CTX);
        const LAS float* hm = (const LAS float*)(L + L_HM);
        const LAS unsigned short* wol = (const LAS unsigned short*)(L + L_WO);
        const LAS unsigned short* w2l = (const LAS unsigned short*)(L + L_W2);
        float a0 = 0.f, a1 = 0.f;
#pragma unroll
        for (int dd = 0; dd < 16; ++dd) { const float w = bf2f(wol[dd * M + tid]); a0 += cx[dd] * w; a1 += cx[16 + dd] * w; }
#pragma unroll
        for (int r = 0; r < 7; ++r) { const float w = bf2f(w2l[r * M + tid]); a0 += hm[r] * w; a1 += hm[8 + r] * w; }
        gu64* g = (gu64*)(ws + G5_OFF) + ((size_t)(tid >> 2) * 256 + cu) * 8 + (tid & 3);
        __hip_atomic_store(g, (1ull << 32) | (unsigned long long)__float_as_uint(a0), RLX_AGENT);
        __hip_atomic_store(g + 4, (1ull << 32) | (unsigned long long)__float_as_uint(a1), RLX_AGENT);
    }
    STAMP(8);
    if (cu < NC5) {
        gu64* g = (gu64*)(ws + G5_OFF) + (size_t)cu * 2048 + tid;
        unsigned long long v[4];
        for (unsigned spins = 0;;) {
            bool ok = true;
#pragma unroll
            for (int i = 0; i < 4; ++i) { v[i] = __hip_atomic_load(g + 512 * i, RLX_AGENT); }
#pragma unroll
            for (int i = 0; i < 4; ++i) ok &= ((unsigned)(v[i] >> 32) == 1u);
            if (__all(ok)) break;
            if (spin_fail(spins, tmo)) break;
        }
        float s = (__uint_as_float((unsigned)v[0]) + __uint_as_float((unsigned)v[1])) + (__uint_as_float((unsigned)v[2]) + __uint_as_float((unsigned)v[3]));
        s += __shfl_xor(s, 8); s += __shfl_xor(s, 16); s += __shfl_xor(s, 32);
        LAS float* red = (LAS float*)(L + L_SC);
        if (lane < 8) red[wave * 8 + lane] = s;
        LGKM0();
        BAR();
        if (tid < 8) {
            float t = 0.f;
#pragma unroll
            for (int w = 0; w < 8; ++w) t += red[w * 8 + tid];
            const int b = tid >> 2, m = cu * 4 + (tid & 3);
            t += bf2f(x[b * M + m]) + bf2f(b2[m]);
            out[b * M + m] = (unsigned short)f2bf(t);
        }
        STAMP(9);
    }
}
}

extern "C" void kernel_launch(void* const* d_in, const int* in_sizes, int n_in,
                              void* d_out, int out_size, void* d_ws, size_t ws_size,
                              hipStream_t stream) {
    const unsigned short* x = (const unsigned short*)d_in[0];
    const unsigned short* ckv = (const unsigned short*)d_in[1];
    const unsigned short* wq = (const unsigned short*)d_in[2];
    const unsigned short* wuk = (const unsigned short*)d_in[3];
    const unsigned short* wuv = (const unsigned short*)d_in[4];
    const unsigned short* wo = (const unsigned short*)d_in[5];
    const unsigned short* w1 = (const unsigned short*)d_in[7];
    const unsigned short* b1 = (const unsigned short*)d_in[8];
    const unsigned short* w2 = (const unsigned short*)d_in[9];
    const unsigned short* b2 = (const unsigned short*)d_in[10];
    unsigned char* ws = (unsigned char*)d_ws;
    (void)hipFuncSetAttribute((const void*)k2, hipFuncAttributeMaxDynamicSharedMemorySize, LDS_BYTES);
    k1<<<368, 256, 0, stream>>>(x, wq, w1, ws);
    k2<<<NCU, 576, LDS_BYTES, stream>>>(x, ckv, wuk, wuv, wo, b1, w2, b2, ws, (unsigned short*)d_out);
}
```

```cpp
#include <hip/hip_runtime.h>
#include <hip/hip_bf16.h>
#include <stdint.h>

#define GAS __attribute__((address_space(1)))
#define LAS __attribute__((address_space(3)))
typedef unsigned v4u __attribute__((ext_vector_type(4)));
typedef unsigned v2u __attribute__((ext_vector_type(2)));
typedef float f32x4 __attribute__((ext_vector_type(4)));
typedef short bf16x8 __attribute__((ext_vector_type(8)));
typedef short s16x4 __attribute__((ext_vector_type(4)));
typedef GAS unsigned gu32;
typedef GAS unsigned long long gu64;
#define RLX_AGENT __ATOMIC_RELAXED, __HIP_MEMORY_SCOPE_AGENT

#define STAMPS 0

namespace {
constexpr int B = 2, H = 16, SK = 4096, D = 256, M = 448, DC = 1024, F = 1792;
constexpr int NCU = 256;
constexpr int NSPLIT = 128, KPS = 32;

constexpr size_t CTRL_OFF = 0, CTRL_BYTES = 32768;
constexpr size_t QP_OFF = CTRL_OFF + CTRL_BYTES;
constexpr size_t HP_OFF = QP_OFF + 4 * 2 * 4096 * 4;
constexpr size_t QABS_OFF = HP_OFF + 65536;
constexpr size_t ML_OFF = QABS_OFF + 65536;
constexpr size_t G4_OFF = ML_OFF + 32768;
constexpr size_t G4_BYTES = (size_t)256 * 512 * 4;
constexpr int NC5 = 112;
constexpr size_t G5_OFF = G4_OFF + G4_BYTES;
constexpr size_t G5_BYTES = (size_t)NC5 * 256 * 8 * 4;
constexpr size_t PCTX_OFF = G5_OFF + G5_BYTES;
constexpr size_t PCTX_BYTES = (size_t)2 * 128 * 16 * 1024 * 2;
constexpr size_t STAMP_OFF = PCTX_OFF + PCTX_BYTES;
constexpr size_t WS_NEED = STAMP_OFF + 256 * 16 * 8;

constexpr int CKV_STRIDE = 2064;
constexpr int L_CKV = 0;
constexpr int L_WUV = L_CKV + 32 * CKV_STRIDE;
constexpr int L_WO = L_WUV + 32768;
constexpr int L_W2 = L_WO + 14336;
constexpr int L_SC = L_W2 + 7168;
constexpr int L_P = L_SC + 16384;
constexpr int L_QS = L_P + 1024;
constexpr int L_HM = L_QS + 256;
constexpr int L_CC = L_HM + 64;
constexpr int L_X6 = L_CC + 512;
constexpr int L_CTX = L_X6 + 2048;
constexpr int L_ST = L_CKV;
constexpr int LDS_BYTES = L_CTX + 128;
static_assert(LDS_BYTES <= 160 * 1024, "lds");

constexpr unsigned kSpinCap = 1u << 18;
constexpr unsigned SENT = 0xFFFFFFFFu;

__device__ __forceinline__ float bf2f(unsigned short u) { return __uint_as_float(((unsigned)u) << 16); }
__device__ __forceinline__ float bflo(unsigned u) { return __uint_as_float(u << 16); }
__device__ __forceinline__ float bfhi(unsigned u) { return __uint_as_float(u & 0xffff0000u); }
__device__ __forceinline__ unsigned f2bf(float f) { unsigned u = __float_as_uint(f); return (u + 0x7fffu + ((u >> 16) & 1u)) >> 16; }
__device__ __forceinline__ unsigned pack2(float a, float b) { return f2bf(a) | (f2bf(b) << 16); }
__device__ __forceinline__ float wsum(float v) { for (int o = 32; o > 0; o >>= 1) v += __shfl_xor(v, o); return v; }
__device__ __forceinline__ float wmax(float v) { for (int o = 32; o > 0; o >>= 1) v = fmaxf(v, __shfl_xor(v, o)); return v; }
__device__ __forceinline__ unsigned long long rt() { return __builtin_amdgcn_s_memrealtime(); }

__global__ void __launch_bounds__(256) k1(const unsigned short* __restrict__ x, const unsigned short* __restrict__ wq,
                                          const unsigned short* __restrict__ w1, unsigned char* __restrict__ ws) {
    __shared__ float xn[2][M];
    __shared__ float red[4][2][64];
    const int tid = threadIdx.x, wave = tid >> 6, lane = tid & 63;
    const int blk = blockIdx.x;
    const bool isq = blk < 256;
    const int idx = isq ? blk : blk - 256;
    const int mq = idx & 3, cg = idx >> 2;
    const int ncols = isq ? 4096 : F;
    const unsigned short* W = (isq ? wq : w1) + (size_t)(mq * 112) * ncols + cg * 64;
    const int r = tid >> 3, cch = tid & 7;
    v4u wv[4];
#pragma unroll
    for (int i = 0; i < 4; ++i) {
        const int row = r + 32 * i;
        if (i < 3 || r < 16) wv[i] = __builtin_nontemporal_load((const v4u*)(W + (size_t)row * ncols + cch * 8));
        else wv[i] = (v4u){0u, 0u, 0u, 0u};
    }
    {
        const int nb = gridDim.x;
        v4u z = {0u, 0u, 0u, 0u};
        v4u sn = {SENT, SENT, SENT, SENT};
        const size_t tot16 = (G5_OFF + G5_BYTES - QABS_OFF) / 16;
        v4u* g = (v4u*)(ws + QABS_OFF);
        for (size_t i = (size_t)blk * 256 + tid; i < tot16; i += (size_t)nb * 256) g[i] = sn;
        if (blk == 0) { v4u* c = (v4u*)(ws + CTRL_OFF); for (int i = tid; i < (int)(CTRL_BYTES / 16); i += 256) c[i] = z; }
    }
    if (wave < 2) {
        float v[7]; float s = 0.f;
#pragma unroll
        for (int i = 0; i < 7; ++i) { v[i] = bf2f(x[wave * M + lane + 64 * i]); s += v[i]; }
        s = wsum(s); const float mean = s * (1.0f / M);
        float ss = 0.f;
#pragma unroll
        for (int i = 0; i < 7; ++i) { const float d = v[i] - mean; ss += d * d; }
        ss = wsum(ss);
        const float rs = 1.0f / sqrtf(ss * (1.0f / M) + 1e-5f);
#pragma unroll
        for (int i = 0; i < 7; ++i) xn[wave][lane + 64 * i] = (v[i] - mean) * rs;
    }
    __syncthreads();
    float a[2][8];
#pragma unroll
    for (int j = 0; j < 8; ++j) { a[0][j] = 0.f; a[1][j] = 0.f; }
#pragma unroll
    for (int i = 0; i < 4; ++i) {
        int row = mq * 112 + r + 32 * i; if (row >= M) row = M - 1;
        const float x0 = xn[0][row], x1 = xn[1][row];
#pragma unroll
        for (int j = 0; j < 4; ++j) {
            const float lo = bflo(wv[i][j]), hi = bfhi(wv[i][j]);
            a[0][2 * j] += x0 * lo; a[0][2 * j + 1] += x0 * hi;
            a[1][2 * j] += x1 * lo; a[1][2 * j + 1] += x1 * hi;
        }
    }
#pragma unroll
    for (int b = 0; b < 2; ++b)
#pragma unroll
        for (int j = 0; j < 8; ++j) { float v = a[b][j]; v += __shfl_xor(v, 8); v += __shfl_xor(v, 16); v += __shfl_xor(v, 32); a[b][j] = v; }
    if (lane < 8) {
#pragma unroll
        for (int b = 0; b < 2; ++b)
#pragma unroll
            for (int j = 0; j < 8; ++j) red[wave][b][lane * 8 + j] = a[b][j];
    }
    __syncthreads();
    if (tid < 128) {
        const int b = tid >> 6, c = tid & 63;
        const float s = red[0][b][c] + red[1][b][c] + red[2][b][c] + red[3][b][c];
        float* dst = (float*)(ws + (isq ? QP_OFF : HP_OFF));
        dst[((size_t)mq * 2 + b) * ncols + cg * 64 + c] = s;
    }
}

__device__ __forceinline__ bool spin_fail(unsigned& spins, gu32* tmo) {
    __builtin_amdgcn_s_sleep(1);
    if (++spins > kSpinCap) { __hip_atomic_store(tmo, 1u, RLX_AGENT); return true; }
    return (spins & 1023u) == 0u && __builtin_amdgcn_readfirstlane(__hip_atomic_load(tmo, RLX_AGENT));
}
__device__ __forceinline__ bool has_sent(v4u v) { return v[0] == SENT || v[1] == SENT || v[2] == SENT || v[3] == SENT; }
__device__ __forceinline__ unsigned canon(unsigned u) { return u == SENT ? 0x7FC00000u : u; }
__device__ __forceinline__ unsigned canonb(unsigned u) { return u == SENT ? 0xFFC0FFC0u : u; }

#define GLDS(src, dst) __builtin_amdgcn_global_load_lds((const unsigned*)(src), (LAS unsigned*)(dst), 16, 0, 2)
#define BAR() __builtin_amdgcn_s_barrier()
#define LGKM0() asm volatile("s_waitcnt lgkmcnt(0)" ::: "memory")
#define VM0() asm volatile("s_waitcnt vmcnt(0)" ::: "memory")
#define STAMP(i) do {} while (0)

__device__ __forceinline__ void wait_flags(const __amdgpu_buffer_rsrc_t& rs, int off, int lane, gu32* tmo) {
    for (unsigned spins = 0;;) {
        const v4u v = __builtin_amdgcn_raw_buffer_load_b128(rs, off + lane * 16, 0, 16);
        if (__all(v[0] != 0u && v[1] != 0u && v[2] != 0u && v[3] != 0u)) return;
        if (spin_fail(spins, tmo)) return;
    }
}

__global__ void __launch_bounds__(576, 1) k2(const unsigned short* __restrict__ x, const unsigned short* __restrict__ ckv,
                                             const unsigned short* __restrict__ wuk, const unsigned short* __restrict__ wuv,
                                             const unsigned short* __restrict__ wo, const unsigned short* __restrict__ b1,
                                             const unsigned short* __restrict__ w2, const unsigned short* __restrict__ b2,
                                             unsigned char* __restrict__ ws, unsigned short* __restrict__ out) {
    extern __shared__ __attribute__((aligned(16))) unsigned char lds_raw[];
    LAS unsigned char* L = (LAS unsigned char*)lds_raw;
    const int tid = threadIdx.x, wave = __builtin_amdgcn_readfirstlane(tid >> 6), lane = tid & 63;
    const int cu = blockIdx.x;
    const int hh = cu >> 4, sl = cu & 15;
    const int bb = cu >> 7, sp = cu & 127;
    gu32* ctrl = (gu32*)(ws + CTRL_OFF);
    gu32* tmo = ctrl + 4096;

    if (wave == 8) {
        BAR();
        const unsigned short* src = ckv + ((size_t)bb * SK + (size_t)sp * KPS) * DC;
#pragma unroll 4
        for (int i = 0; i < 64; ++i) GLDS(src + (size_t)i * 512 + lane * 8, L + L_CKV + (i >> 1) * CKV_STRIDE + (i & 1) * 1024);
        BAR();
        asm volatile("s_waitcnt vmcnt(8)" ::: "memory");
        {
#pragma unroll 4
            for (int i = 0; i < 32; ++i) {
                const int c = 64 * sl + 2 * i + (lane >> 5);
                GLDS(wuv + ((size_t)c * H + hh) * D + (lane & 31) * 8, L + L_WUV + i * 1024);
            }
            const unsigned short* so = wo + ((size_t)hh * D + sl * 16) * M;
#pragma unroll 2
            for (int i = 0; i < 14; ++i) GLDS(so + (size_t)i * 512 + lane * 8, L + L_WO + i * 1024);
            const unsigned short* s2 = w2 + (size_t)cu * 7 * M;
#pragma unroll 2
            for (int i = 0; i < 6; ++i) GLDS(s2 + (size_t)i * 512 + lane * 8, L + L_W2 + i * 1024);
            if (lane < 8) GLDS(s2 + (size_t)6 * 512 + lane * 8, L + L_W2 + 6 * 1024);
        }
        asm volatile("s_waitcnt vmcnt(53)" ::: "memory");
        BAR();
        BAR();
        BAR();
        BAR();
        BAR();
        VM0();
        BAR();
        BAR();
        BAR();
        BAR();
        BAR();
        BAR();
        if (cu < NC5) BAR();
        return;
    }

    STAMP(0);
    const __amdgpu_buffer_rsrc_t rs_ws = __builtin_amdgcn_make_buffer_rsrc(ws, 0, (int)WS_NEED, 0x00020000);
    v4u wk[4];
#pragma unroll
    for (int i = 0; i < 4; ++i) {
        const int c = 64 * sl + 8 * wave + 2 * i + (lane >> 5);
        wk[i] = __builtin_nontemporal_load((const v4u*)(wuk + ((size_t)c * H + hh) * D + (lane & 31) * 8));
    }
    float qv[2][8];
    {
        const float* qp = (const float*)(ws + QP_OFF);
#pragma unroll
        for (int b = 0; b < 2; ++b) {
            f32x4 s0 = {0.f, 0.f, 0.f, 0.f}, s1 = {0.f, 0.f, 0.f, 0.f};
#pragma unroll
            for (int p = 0; p < 4; ++p) {
                const float* a = qp + ((size_t)p * 2 + b) * 4096 + hh * D + (lane & 31) * 8;
                s0 += *(const f32x4*)a; s1 += *(const f32x4*)(a + 4);
            }
#pragma unroll
            for (int j = 0; j < 4; ++j) { qv[b][j] = s0[j]; qv[b][4 + j] = s1[j]; }
        }
    }
    if (wave == 1 && lane < 14) {
        const int b = lane / 7, r = lane % 7, f = cu * 7 + r;
        const float* hp = (const float*)(ws + HP_OFF);
        float hv = bf2f(b1[f]);
#pragma unroll
        for (int p = 0; p < 4; ++p) hv += hp[((size_t)p * 2 + b) * F + f];
        const float t = tanhf(0.7978845608028654f * (hv + 0.044715f * hv * hv * hv));
        ((LAS float*)(L + L_HM))[b * 8 + r] = 0.5f * hv * (1.0f + t);
    }
    {
        const float QS = 0.0625f * 1.4426950408889634f;
#pragma unroll
        for (int i = 0; i < 4; ++i) {
            float a0 = 0.f, a1 = 0.f;
#pragma unroll
            for (int j = 0; j < 4; ++j) {
                const float lo = bflo(wk[i][j]), hi = bfhi(wk[i][j]);
                a0 += lo * qv[0][2 * j] + hi * qv[0][2 * j + 1];
                a1 += lo * qv[1][2 * j] + hi * qv[1][2 * j + 1];
            }
#pragma unroll
            for (int o = 1; o < 32; o <<= 1) { a0 += __shfl_xor(a0, o); a1 += __shfl_xor(a1, o); }
            if ((lane & 31) == 0) {
                const int r = 8 * wave + 2 * i + (lane >> 5);
                ((LAS unsigned short*)(L + L_QS))[r] = (unsigned short)f2bf(a0 * QS);
                ((LAS unsigned short*)(L + L_QS))[64 + r] = (unsigned short)f2bf(a1 * QS);
            }
        }
    }
    STAMP(1);
    LGKM0();
    BAR();
    if (wave == 0) {
        if (lane < 16) {
            const int b = lane >> 3, ch = lane & 7;
            v4u v = *(const LAS v4u*)(L + L_QS + b * 128 + ch * 16);
            v[0] = canonb(v[0]); v[1] = canonb(v[1]); v[2] = canonb(v[2]); v[3] = canonb(v[3]);
            __builtin_amdgcn_raw_buffer_store_b128(v, rs_ws, (int)(QABS_OFF + (((size_t)b * H + hh) * DC + 64 * sl + ch * 8) * 2), 0, 16);
        }
        if (lane == 0) __hip_atomic_store(ctrl + cu, 1u, RLX_AGENT);
        wait_flags(rs_ws, (int)CTRL_OFF, lane, tmo);
    }
    STAMP(2);
    BAR();
    bf16x8 qa[4];
    for (unsigned spins = 0;;) {
        v4u v[4]; bool bad = false;
#pragma unroll
        for (int i = 0; i < 4; ++i)
            v[i] = __builtin_amdgcn_raw_buffer_load_b128(rs_ws, (int)(QABS_OFF + (((size_t)bb * H + (lane & 15)) * DC + 128 * wave + 32 * i + 8 * (lane >> 4)) * 2), 0, 16);
#pragma unroll
        for (int i = 0; i < 4; ++i) { bad |= has_sent(v[i]); qa[i] = __builtin_bit_cast(bf16x8, v[i]); }
        if (!__any(bad)) break;
        if (spin_fail(spins, tmo)) break;
    }
    STAMP(3);
    BAR();
    STAMP(10);
    {
        f32x4 acc[2] = {{0.f, 0.f, 0.f, 0.f}, {0.f, 0.f, 0.f, 0.f}};
#pragma unroll
        for (int nt = 0; nt < 2; ++nt)
#pragma unroll
            for (int i = 0; i < 4; ++i) {
                const v4u v = *(const LAS v4u*)(L + L_CKV + (16 * nt + (lane & 15)) * CKV_STRIDE + (128 * wave + 32 * i + 8 * (lane >> 4)) * 2);
                acc[nt] = __builtin_amdgcn_mfma_f32_16x16x32_bf16(qa[i], __builtin_bit_cast(bf16x8, v), acc[nt], 0, 0, 0);
            }
        LAS float* sc = (LAS float*)(L + L_SC);
#pragma unroll
        for (int nt = 0; nt < 2; ++nt)
#pragma unroll
            for (int r = 0; r < 4; ++r) sc[(wave * 16 + 4 * (lane >> 4) + r) * 32 + 16 * nt + (lane & 15)] = acc[nt][r];
    }
    LGKM0();
    BAR();
    {
        const int h = tid >> 5, key = tid & 31;
        const LAS float* sc = (const LAS float*)(L + L_SC);
        float s = 0.f;
#pragma unroll
        for (int w = 0; w < 8; ++w) s += sc[(w * 16 + h) * 32 + key];
        float mx = s;
#pragma unroll
        for (int o = 1; o < 32; o <<= 1) mx = fmaxf(mx, __shfl_xor(mx, o));
        const unsigned pb = f2bf(__builtin_amdgcn_exp2f(s - mx));
        float l = __uint_as_float(pb << 16);
#pragma unroll
        for (int o = 1; o < 32; o <<= 1) l += __shfl_xor(l, o);
        ((LAS unsigned short*)(L + L_P))[h * 32 + key] = (unsigned short)pb;
        if (key == 0) {
            const unsigned long long v = (unsigned long long)__float_as_uint(mx) | ((unsigned long long)__float_as_uint(l) << 32);
            __hip_atomic_store((gu64*)(ws + ML_OFF) + ((size_t)h * 2 + bb) * 128 + sp, v, RLX_AGENT);
        }
    }
    LGKM0();
    BAR();
    {
        const bf16x8 pf = __builtin_bit_cast(bf16x8, *(const LAS v4u*)(L + L_P + (lane & 15) * 64 + (lane >> 4) * 16));
        const int g = lane >> 4, q = (lane & 15) >> 2, p = lane & 3;
#pragma unroll
        for (int t = 0; t < 8; ++t) {
            const int c0 = 128 * wave + 16 * t;
            const LAS s16x4* a0 = (const LAS s16x4*)(L + L_CKV + (8 * g + q) * CKV_STRIDE + c0 * 2 + 8 * p);
            const LAS s16x4* a1 = (const LAS s16x4*)(L + L_CKV + (8 * g + 4 + q) * CKV_STRIDE + c0 * 2 + 8 * p);
            const s16x4 lo = __builtin_amdgcn_ds_read_tr16_b64_v4i16((LAS s16x4*)a0);
            const s16x4 hi = __builtin_amdgcn_ds_read_tr16_b64_v4i16((LAS s16x4*)a1);
            bf16x8 af;
            af[0] = lo[0]; af[1] = lo[1]; af[2] = lo[2]; af[3] = lo[3]; af[4] = hi[0]; af[5] = hi[1]; af[6] = hi[2]; af[7] = hi[3];
            f32x4 o = {0.f, 0.f, 0.f, 0.f};
            o = __builtin_amdgcn_mfma_f32_16x16x32_bf16(af, pf, o, 0, 0, 0);
            v2u v; v[0] = pack2(o[0], o[1]); v[1] = pack2(o[2], o[3]);
            *(LAS v2u*)(L + L_ST + (lane & 15) * CKV_STRIDE + (c0 + 4 * g) * 2) = v;
        }
    }
    LGKM0();
    BAR();
    {
#pragma unroll
        for (int i = 0; i < 4; ++i) {
            const int h = 2 * wave + (i >> 1);
            const v4u v = *(const LAS v4u*)(L + L_ST + h * CKV_STRIDE + (i & 1) * 1024 + lane * 16);
            __builtin_amdgcn_raw_buffer_store_b128(v, rs_ws, (int)(PCTX_OFF + ((((size_t)bb * NSPLIT + sp) * H + h) * DC) * 2 + (i & 1) * 1024 + lane * 16), 0, 16);
        }
    }
    VM0();
    STAMP(4);
    BAR();
    if (wave == 0) {
        if (lane == 0) __hip_atomic_store(ctrl + 256 + cu, 1u, RLX_AGENT);
        wait_flags(rs_ws, (int)CTRL_OFF + 1024, lane, tmo);
    }
    STAMP(5);
    BAR();
    {
        v4u ml[2], pc[4];
#pragma unroll
        for (int b = 0; b < 2; ++b) ml[b] = __builtin_amdgcn_raw_buffer_load_b128(rs_ws, (int)(ML_OFF + (((size_t)hh * 2 + b) * 128 + 2 * lane) * 8), 0, 16);
        const int j = lane >> 3, ch = lane & 7;
#pragma unroll
        for (int i = 0; i < 4; ++i) {
            const int b = i >> 1, s = 16 * wave + (i & 1) * 8 + j;
            pc[i] = __builtin_amdgcn_raw_buffer_load_b128(rs_ws, (int)(PCTX_OFF + ((((size_t)b * NSPLIT + s) * H + hh) * DC + 64 * sl + ch * 8) * 2), 0, 16);
        }
        float wg[2][2];
#pragma unroll
        for (int b = 0; b < 2; ++b) {
            const float m0 = __uint_as_float(ml[b][0]), l0 = __uint_as_float(ml[b][1]), m1 = __uint_as_float(ml[b][2]), l1 = __uint_as_float(ml[b][3]);
            const float gm = wmax(fmaxf(m0, m1));
            const float e0 = __builtin_amdgcn_exp2f(m0 - gm), e1 = __builtin_amdgcn_exp2f(m1 - gm);
            const float Ls = wsum(e0 * l0 + e1 * l1);
            const float inv = 1.0f / Ls;
            wg[b][0] = e0 * inv; wg[b][1] = e1 * inv;
        }
        float acc[2][8];
#pragma unroll
        for (int k = 0; k < 8; ++k) { acc[0][k] = 0.f; acc[1][k] = 0.f; }
#pragma unroll
        for (int i = 0; i < 4; ++i) {
            const int b = i >> 1, s = 16 * wave + (i & 1) * 8 + j;
            const float w0 = __shfl(wg[b][0], s >> 1), w1 = __shfl(wg[b][1], s >> 1);
            const float w = (s & 1) ? w1 : w0;
#pragma unroll
            for (int k = 0; k < 4; ++k) { acc[b][2 * k] += w * bflo(pc[i][k]); acc[b][2 * k + 1] += w * bfhi(pc[i][k]); }
        }
#pragma unroll
        for (int b = 0; b < 2; ++b)
#pragma unroll
            for (int k = 0; k < 8; ++k) { float v = acc[b][k]; v += __shfl_xor(v, 8); v += __shfl_xor(v, 16); v += __shfl_xor(v, 32); acc[b][k] = v; }
        LAS float* red = (LAS float*)(L + L_SC);
        if (lane < 8) {
#pragma unroll
            for (int b = 0; b < 2; ++b)
#pragma unroll
                for (int k = 0; k < 8; ++k) red[(wave * 2 + b) * 64 + lane * 8 + k] = acc[b][k];
        }
    }
    STAMP(11);
    LGKM0();
    BAR();
    if (tid < 128) {
        const LAS float* red = (const LAS float*)(L + L_SC);
        float s = 0.f;
#pragma unroll
        for (int w = 0; w < 8; ++w) s += red[w * 128 + tid];
        ((LAS float*)(L + L_CC))[tid] = s;
    }
    LGKM0();
    BAR();
    {
        const int dp = tid & 127, cq = tid >> 7;
        const LAS float* cc = (const LAS float*)(L + L_CC);
        const LAS unsigned* wv = (const LAS unsigned*)(L + L_WUV);
        float a00 = 0.f, a01 = 0.f, a10 = 0.f, a11 = 0.f;
#pragma unroll
        for (int c = 16 * cq; c < 16 * cq + 16; ++c) {
            const unsigned w = wv[c * 128 + dp];
            const float lo = bflo(w), hi = bfhi(w), c0 = cc[c], c1 = cc[64 + c];
            a00 += c0 * lo; a01 += c0 * hi; a10 += c1 * lo; a11 += c1 * hi;
        }
        LAS float* red = (LAS float*)(L + L_SC + 8192);
        red[(cq * 2 + 0) * 256 + 2 * dp] = a00; red[(cq * 2 + 0) * 256 + 2 * dp + 1] = a01;
        red[(cq * 2 + 1) * 256 + 2 * dp] = a10; red[(cq * 2 + 1) * 256 + 2 * dp + 1] = a11;
    }
    LGKM0();
    BAR();
    if (tid < 128) {
        const LAS f32x4* red = (const LAS f32x4*)(L + L_SC + 8192);
        const f32x4 s = red[tid] + red[128 + tid] + red[256 + tid] + red[384 + tid];
        const int b = tid >> 6, d = (tid & 63) * 4;
        v4u v; v[0] = canon(__float_as_uint(s[0])); v[1] = canon(__float_as_uint(s[1])); v[2] = canon(__float_as_uint(s[2])); v[3] = canon(__float_as_uint(s[3]));
        __builtin_amdgcn_raw_buffer_store_b128(v, rs_ws, (int)(G4_OFF + ((((size_t)hh * 16 + (d >> 4)) * 16 + sl) * 32 + b * 16 + (d & 15)) * 4), 0, 16);
    }
    STAMP(6);
    if (wave < 2) {
        v4u v;
        for (unsigned spins = 0;;) {
            v = __builtin_amdgcn_raw_buffer_load_b128(rs_ws, (int)(G4_OFF + (size_t)cu * 2048 + tid * 16), 0, 16);
            if (!__any(has_sent(v))) break;
            if (spin_fail(spins, tmo)) break;
        }
        *(LAS v4u*)(L + L_X6 + tid * 16) = v;
    }
    STAMP(7);
    LGKM0();
    BAR();
    if (tid < 32) {
        const LAS float* x6 = (const LAS float*)(L + L_X6);
        float s = 0.f;
#pragma unroll
        for (int c = 0; c < 16; ++c) s += x6[c * 32 + tid];
        ((LAS float*)(L + L_CTX))[tid] = s;
    }
    LGKM0();
    BAR();
    if (tid < 224) {
        const int j = tid >> 1, b = tid & 1;
        const LAS float* cx = (const LAS float*)(L + L_CTX) + b * 16;
        const LAS float* hm = (const LAS float*)(L + L_HM) + b * 8;
        f32x4 a = {0.f, 0.f, 0.f, 0.f};
#pragma unroll
        for (int dd = 0; dd < 16; ++dd) {
            const v2u w = *(const LAS v2u*)(L + L_WO + dd * (M * 2) + j * 8);
            const float c = cx[dd];
            a[0] += c * bflo(w[0]); a[1] += c * bfhi(w[0]); a[2] += c * bflo(w[1]); a[3] += c * bfhi(w[1]);
        }
#pragma unroll
        for (int r = 0; r < 7; ++r) {
            const v2u w = *(const LAS v2u*)(L + L_W2 + r * (M * 2) + j * 8);
            const float c = hm[r];
            a[0] += c * bflo(w[0]); a[1] += c * bfhi(w[0]); a[2] += c * bflo(w[1]); a[3] += c * bfhi(w[1]);
        }
        v4u v; v[0] = canon(__float_as_uint(a[0])); v[1] = canon(__float_as_uint(a[1])); v[2] = canon(__float_as_uint(a[2])); v[3] = canon(__float_as_uint(a[3]));
        __builtin_amdgcn_raw_buffer_store_b128(v, rs_ws, (int)(G5_OFF + (((size_t)j * 256 + cu) * 8 + b * 4) * 4), 0, 16);
    }
    STAMP(8);
    if (cu < NC5) {
        v4u v;
        for (unsigned spins = 0;;) {
            v = __builtin_amdgcn_raw_buffer_load_b128(rs_ws, (int)(G5_OFF + (size_t)cu * 8192 + tid * 16), 0, 16);
            if (!__any(has_sent(v))) break;
            if (spin_fail(spins, tmo)) break;
        }
        f32x4 s; s[0] = __uint_as_float(v[0]); s[1] = __uint_as_float(v[1]); s[2] = __uint_as_float(v[2]); s[3] = __uint_as_float(v[3]);
#pragma unroll
        for (int o = 2; o < 64; o <<= 1) { s[0] += __shfl_xor(s[0], o); s[1] += __shfl_xor(s[1], o); s[2] += __shfl_xor(s[2], o); s[3] += __shfl_xor(s[3], o); }
        LAS float* red = (LAS float*)(L + L_SC);
        if (lane < 2) { red[wave * 8 + lane * 4 + 0] = s[0]; red[wave * 8 + lane * 4 + 1] = s[1]; red[wave * 8 + lane * 4 + 2] = s[2]; red[wave * 8 + lane * 4 + 3] = s[3]; }
        LGKM0();
        BAR();
        if (tid < 8) {
            float t = 0.f;
#pragma unroll
            for (int w = 0; w < 8; ++w) t += red[w * 8 + tid];
            const int b = tid >> 2, m = cu * 4 + (tid & 3);
            t += bf2f(x[b * M + m]) + bf2f(b2[m]);
            out[b * M + m] = (unsigned short)f2bf(t);
        }
        STAMP(9);
    }
}
}

extern "C" void kernel_launch(void* const* d_in, const int* in_sizes, int n_in,
                              void* d_out, int out_size, void* d_ws, size_t ws_size,
                              hipStream_t stream) {
    const unsigned short* x = (const unsigned short*)d_in[0];
    const unsigned short* ckv = (const unsigned short*)d_in[1];
    const unsigned short* wq = (const unsigned short*)d_in[2];
    const unsigned short* wuk = (const unsigned short*)d_in[3];
    const unsigned short* wuv = (const unsigned short*)d_in[4];
    const unsigned short* wo = (const unsigned short*)d_in[5];
    const unsigned short* w1 = (const unsigned short*)d_in[7];
    const unsigned short* b1 = (const unsigned short*)d_in[8];
    const unsigned short* w2 = (const unsigned short*)d_in[9];
    const unsigned short* b2 = (const unsigned short*)d_in[10];
    unsigned char* ws = (unsigned char*)d_ws;
    (void)hipFuncSetAttribute((const void*)k2, hipFuncAttributeMaxDynamicSharedMemorySize, LDS_BYTES);
    k1<<<368, 256, 0, stream>>>(x, wq, w1, ws);
    k2<<<NCU, 576, LDS_BYTES, stream>>>(x, ckv, wuk, wuv, wo, b1, w2, b2, ws, (unsigned short*)d_out);
}
```

```cpp
#include <hip/hip_runtime.h>
#include <stdint.h>

#define GAS __attribute__((address_space(1)))
#define LAS __attribute__((address_space(3)))
typedef unsigned v4u __attribute__((ext_vector_type(4)));
typedef unsigned v2u __attribute__((ext_vector_type(2)));
typedef float f32x4 __attribute__((ext_vector_type(4)));
typedef float f32x2 __attribute__((ext_vector_type(2)));
typedef short bf16x8 __attribute__((ext_vector_type(8)));
typedef short s16x4 __attribute__((ext_vector_type(4)));
typedef GAS unsigned gu32;
typedef GAS unsigned long long gu64;
#define RLX_AGENT __ATOMIC_RELAXED, __HIP_MEMORY_SCOPE_AGENT

#define STAMPS 0

namespace {
constexpr int H = 16, SK = 4096, D = 256, M = 448, DC = 1024, F = 1792;
constexpr int NCU = 256;
constexpr int NSPLIT = 128, KPS = 32;
constexpr int NC5 = 112;
constexpr int MLP0 = 144;

constexpr size_t FLG_OFF = 0;
constexpr size_t TMO_OFF = 8192;
constexpr size_t QABS_OFF = 16384;
constexpr size_t G5_OFF = QABS_OFF + 131072;
constexpr size_t G5_BYTES = (size_t)NC5 * 256 * 8 * 4;
constexpr size_t FILL_BYTES = G5_OFF + G5_BYTES;
constexpr size_t ML_OFF = FILL_BYTES;
constexpr size_t PCTX_OFF = ML_OFF + 32768;
constexpr size_t PCTX_BYTES = (size_t)2 * 128 * 16 * 1024 * 2;
constexpr size_t STAMP_OFF = PCTX_OFF + PCTX_BYTES;
constexpr size_t WS_NEED = STAMP_OFF + 256 * 16 * 8;
static_assert(FILL_BYTES % 16 == 0, "fill");

constexpr int CKV_STRIDE = 2064;
constexpr int L_CKV = 0;
constexpr int L_WUV = L_CKV + 32 * CKV_STRIDE;
constexpr int LDS_DMA_BYTES = L_WUV + 32768;
constexpr int L_SC = 0;
constexpr int L_XN = L_SC + 20480;
constexpr int L_QA = L_XN + 3584;
constexpr int L_QS = L_QA + 2048;
constexpr int L_P = L_QS + 512;
constexpr int L_STAT = L_P + 1024;
constexpr int L_CC = L_STAT + 128;
constexpr int L_RM = L_CC + 512;
constexpr int L_HM = L_RM + 1024;
constexpr int L_ST = L_CKV;
constexpr int LDS_SC_BYTES = L_HM + 128;
static_assert(LDS_DMA_BYTES + LDS_SC_BYTES <= 160 * 1024, "lds");

constexpr unsigned kSpinCap = 1u << 18;
constexpr unsigned SENT = 0xFFFFFFFFu;

__device__ __forceinline__ float bf2f(unsigned short u) { return __uint_as_float(((unsigned)u) << 16); }
__device__ __forceinline__ float bflo(unsigned u) { return __uint_as_float(u << 16); }
__device__ __forceinline__ float bfhi(unsigned u) { return __uint_as_float(u & 0xffff0000u); }
__device__ __forceinline__ unsigned f2bf(float f) { unsigned u = __float_as_uint(f); return (u + 0x7fffu + ((u >> 16) & 1u)) >> 16; }
__device__ __forceinline__ unsigned pack2(float a, float b) { return f2bf(a) | (f2bf(b) << 16); }
__device__ __forceinline__ float wsum(float v) { for (int o = 32; o > 0; o >>= 1) v += __shfl_xor(v, o); return v; }
__device__ __forceinline__ unsigned long long rt() { return __builtin_amdgcn_s_memrealtime(); }

__global__ void __launch_bounds__(256) k1(unsigned char* __restrict__ ws) {
    const v4u sn = {SENT, SENT, SENT, SENT};
    v4u* g = (v4u*)ws;
    const unsigned tot16 = (unsigned)(FILL_BYTES / 16);
    for (unsigned i = blockIdx.x * 256 + threadIdx.x; i < tot16; i += gridDim.x * 256) g[i] = sn;
}

__device__ __forceinline__ bool spin_fail(unsigned& spins, gu32* tmo) {
    __builtin_amdgcn_s_sleep(1);
    if (++spins > kSpinCap) { __hip_atomic_store(tmo, 1u, RLX_AGENT); return true; }
    return (spins & 1023u) == 0u && __builtin_amdgcn_readfirstlane(__hip_atomic_load(tmo, RLX_AGENT)) == 1u;
}
__device__ __forceinline__ bool has_sent(v4u v) { return v[0] == SENT || v[1] == SENT || v[2] == SENT || v[3] == SENT; }
__device__ __forceinline__ unsigned canon(unsigned u) { return u == SENT ? 0x7FC00000u : u; }
__device__ __forceinline__ unsigned canonb(unsigned u) { return u == SENT ? 0xFFC0FFC0u : u; }

#define GLDS_NT(src, dst) __builtin_amdgcn_global_load_lds((const unsigned*)(src), (LAS unsigned*)(dst), 16, 0, 2)
#define BAR() __builtin_amdgcn_s_barrier()
#define LGKM0() asm volatile("s_waitcnt lgkmcnt(0)" ::: "memory")
#define VM0() asm volatile("s_waitcnt vmcnt(0)" ::: "memory")
#define PIN() __builtin_amdgcn_sched_barrier(0)
#define STAMP(i) do {} while (0)

__global__ void __launch_bounds__(512, 1) k2(const unsigned short* __restrict__ x, const unsigned short* __restrict__ ckv,
                                             const unsigned short* __restrict__ wq, const unsigned short* __restrict__ wuk,
                                             const unsigned short* __restrict__ wuv, const unsigned short* __restrict__ wo,
                                             const unsigned short* __restrict__ w1, const unsigned short* __restrict__ b1,
                                             const unsigned short* __restrict__ w2, const unsigned short* __restrict__ b2,
                                             unsigned char* __restrict__ ws, unsigned short* __restrict__ out) {
    __shared__ __attribute__((aligned(16))) unsigned char lds_dma[LDS_DMA_BYTES];
    __shared__ __attribute__((aligned(16))) unsigned char lds_sc[LDS_SC_BYTES];
    LAS unsigned char* L = (LAS unsigned char*)lds_dma;
    LAS unsigned char* S = (LAS unsigned char*)lds_sc;
    const int tid = threadIdx.x, wave = __builtin_amdgcn_readfirstlane(tid >> 6), lane = tid & 63;
    const int cu = blockIdx.x;
    const int hh = 2 * (cu & 7) + ((cu >> 3) & 1), cs = cu >> 4;
    const int bb = cu >> 7, sp = cu & 127;
    const bool is_mlp = cu >= MLP0;
    const int mj = cu - MLP0;
    gu32* tmo = (gu32*)(ws + TMO_OFF);
    const __amdgpu_buffer_rsrc_t rs_ws = __builtin_amdgcn_make_buffer_rsrc(ws, 0, (int)WS_NEED, 0x00020000);
    STAMP(0);

    unsigned xr[7];
    {
        const unsigned short* p = x + (wave & 1) * M + lane;
#pragma unroll
        for (int i = 0; i < 7; ++i) asm volatile("global_load_ushort %0, %1, off" : "=v"(xr[i]) : "v"(p + 64 * i));
    }
    v4u wqv[28];
    {
        const unsigned short* p = wq + ((size_t)(tid >> 5) * H + hh) * D + (tid & 31) * 8;
#pragma unroll
        for (int r = 0; r < 28; ++r) asm volatile("global_load_dwordx4 %0, %1, off" : "=v"(wqv[r]) : "v"(p + (size_t)r * 16 * H * D));
    }
    v4u wk[8];
    {
        const unsigned short* src = ckv + ((size_t)bb * SK + (size_t)sp * KPS + 4 * wave) * DC;
        if (wave < 4) {
            const unsigned short* p = wuk + ((size_t)(64 * cs + 16 * wave + (lane & 15)) * H + hh) * D + 8 * (lane >> 4);
#pragma unroll
            for (int ks = 0; ks < 8; ++ks) asm volatile("global_load_dwordx4 %0, %1, off nt" : "=v"(wk[ks]) : "v"(p + 32 * ks));
#pragma unroll
            for (int i = 0; i < 8; ++i) GLDS_NT(src + (size_t)i * 512 + lane * 8, L + L_CKV + (4 * wave + (i >> 1)) * CKV_STRIDE + (i & 1) * 1024);
        } else {
#pragma unroll
            for (int i = 0; i < 8; ++i) GLDS_NT(src + (size_t)i * 512 + lane * 8, L + L_CKV + (4 * wave + (i >> 1)) * CKV_STRIDE + (i & 1) * 1024);
#pragma unroll
            for (int i = 0; i < 8; ++i) {
                const int cl = 16 * (wave - 4) + 2 * i;
                GLDS_NT(wuv + ((size_t)(64 * cs + cl + (lane >> 5)) * H + hh) * D + (lane & 31) * 8, L + L_WUV + cl * 512);
            }
        }
    }
    PIN();
    asm volatile("s_waitcnt vmcnt(44)" : "+v"(xr[0]), "+v"(xr[1]), "+v"(xr[2]), "+v"(xr[3]), "+v"(xr[4]), "+v"(xr[5]), "+v"(xr[6]));
    if (wave < 2) {
        float xv[7];
        float s = 0.f;
#pragma unroll
        for (int i = 0; i < 7; ++i) { xv[i] = __uint_as_float(xr[i] << 16); s += xv[i]; }
        s = wsum(s); const float mean = s * (1.0f / M);
        float ss = 0.f;
#pragma unroll
        for (int i = 0; i < 7; ++i) { const float d = xv[i] - mean; ss += d * d; }
        ss = wsum(ss);
        const float rs = 1.0f / sqrtf(ss * (1.0f / M) + 1e-5f);
#pragma unroll
        for (int i = 0; i < 7; ++i) ((LAS float*)(S + L_XN))[2 * (lane + 64 * i) + wave] = (xv[i] - mean) * rs;
    }
    LGKM0();
    BAR();
    {
        f32x2 a[8];
#pragma unroll
        for (int j = 0; j < 8; ++j) a[j] = (f32x2){0.f, 0.f};
        const LAS f32x2* xn = (const LAS f32x2*)(S + L_XN) + (tid >> 5);
#define QGRP(g, n) \
        asm volatile("s_waitcnt vmcnt(" #n ")" : "+v"(wqv[4 * g]), "+v"(wqv[4 * g + 1]), "+v"(wqv[4 * g + 2]), "+v"(wqv[4 * g + 3])); \
        _Pragma("unroll") for (int r = 4 * g; r < 4 * g + 4; ++r) { \
            const f32x2 xx = xn[16 * r]; \
            _Pragma("unroll") for (int j = 0; j < 4; ++j) { \
                const float lo = bflo(wqv[r][j]), hi = bfhi(wqv[r][j]); \
                a[2 * j] += xx * lo; a[2 * j + 1] += xx * hi; \
            } \
        }
        QGRP(0, 40) QGRP(1, 36) QGRP(2, 32) QGRP(3, 28) QGRP(4, 24) QGRP(5, 20) QGRP(6, 16)
#undef QGRP
#pragma unroll
        for (int j = 0; j < 8; ++j) { a[j][0] += __shfl_xor(a[j][0], 32); a[j][1] += __shfl_xor(a[j][1], 32); }
        if (lane < 32) {
            LAS f32x4* red = (LAS f32x4*)(S + L_SC);
#pragma unroll
            for (int b = 0; b < 2; ++b) {
                red[((wave * 2 + b) * 256 + lane * 8) / 4] = (f32x4){a[0][b], a[1][b], a[2][b], a[3][b]};
                red[((wave * 2 + b) * 256 + lane * 8) / 4 + 1] = (f32x4){a[4][b], a[5][b], a[6][b], a[7][b]};
            }
        }
    }
    LGKM0();
    BAR();
    {
        const LAS float* red = (const LAS float*)(S + L_SC);
        float s = 0.f;
#pragma unroll
        for (int w = 0; w < 8; ++w) s += red[w * 512 + tid];
        s *= 0.0625f * 1.4426950408889634f;
        const unsigned hi = f2bf(s);
        const unsigned lo = f2bf(s - __uint_as_float(hi << 16));
        const int b = tid >> 8, d = tid & 255;
        ((LAS unsigned short*)(S + L_QA))[(2 * b) * 256 + d] = (unsigned short)hi;
        ((LAS unsigned short*)(S + L_QA))[(2 * b + 1) * 256 + d] = (unsigned short)lo;
    }
    LGKM0();
    BAR();
    STAMP(1);
    if (wave < 4) {
        asm volatile("s_waitcnt vmcnt(8)" : "+v"(wk[0]), "+v"(wk[1]), "+v"(wk[2]), "+v"(wk[3]), "+v"(wk[4]), "+v"(wk[5]), "+v"(wk[6]), "+v"(wk[7]));
        f32x4 acc = {0.f, 0.f, 0.f, 0.f};
#pragma unroll
        for (int ks = 0; ks < 8; ++ks) {
            v4u av = {0u, 0u, 0u, 0u};
            if ((lane & 15) < 4) av = *(const LAS v4u*)(S + L_QA + (lane & 15) * 512 + (32 * ks + 8 * (lane >> 4)) * 2);
            acc = __builtin_amdgcn_mfma_f32_16x16x32_bf16(__builtin_bit_cast(bf16x8, av), __builtin_bit_cast(bf16x8, wk[ks]), acc, 0, 0, 0);
        }
        LAS unsigned short* qs = (LAS unsigned short*)(S + L_QS) + wave * 64;
        if (lane < 16) {
#pragma unroll
            for (int b = 0; b < 2; ++b) {
                const float s = acc[2 * b] + acc[2 * b + 1];
                const unsigned hi = f2bf(s);
                const unsigned lo = f2bf(s - __uint_as_float(hi << 16));
                qs[(b * 2) * 16 + lane] = (unsigned short)hi;
                qs[(b * 2 + 1) * 16 + lane] = (unsigned short)lo;
            }
        }
        LGKM0();
        if (lane < 8) {
            const int bh = lane >> 1, half = lane & 1;
            v4u v = *(const LAS v4u*)(S + L_QS + wave * 128 + bh * 32 + half * 16);
            v[0] = canonb(v[0]); v[1] = canonb(v[1]); v[2] = canonb(v[2]); v[3] = canonb(v[3]);
            __builtin_amdgcn_raw_buffer_store_b128(v, rs_ws, (int)(QABS_OFF + (((size_t)bh * H + hh) * DC + 64 * cs + 16 * wave + 8 * half) * 2), 0, 16);
        }
    }
    STAMP(2);
    bf16x8 qa[8];
    for (unsigned spins = 0;;) {
        v4u v[8]; bool bad = false;
#pragma unroll
        for (int i = 0; i < 8; ++i)
            v[i] = __builtin_amdgcn_raw_buffer_load_b128(rs_ws, (int)(QABS_OFF + ((((size_t)bb * 2 + (i >> 2)) * H + (lane & 15)) * DC + 128 * wave + 32 * (i & 3) + 8 * (lane >> 4)) * 2), 0, 16);
#pragma unroll
        for (int i = 0; i < 8; ++i) { bad |= has_sent(v[i]); qa[i] = __builtin_bit_cast(bf16x8, v[i]); }
        if (!__any(bad)) break;
        if (spin_fail(spins, tmo)) break;
    }
    VM0();
    STAMP(3);
    PIN();
    v4u wov[32];
    {
        const unsigned short* p = wo + ((size_t)hh * D + 32 * wave) * M + (lane < 56 ? lane : 55) * 8;
#pragma unroll
        for (int r = 0; r < 32; ++r) wov[r] = *(const v4u*)(p + (size_t)r * M);
    }
    v4u w1v[2], w2v[2];
    if (is_mlp) {
#pragma unroll
        for (int i = 0; i < 2; ++i) {
            int row = (tid >> 1) + 256 * i; if (row >= M) row = M - 1;
            w1v[i] = __builtin_nontemporal_load((const v4u*)(w1 + (size_t)row * F + 16 * mj + (tid & 1) * 8));
            w2v[i] = __builtin_nontemporal_load((const v4u*)(w2 + (size_t)(16 * mj + 2 * wave + i) * M + (lane < 56 ? lane : 55) * 8));
        }
    }
    PIN();
    BAR();
    {
        f32x4 acc[2] = {{0.f, 0.f, 0.f, 0.f}, {0.f, 0.f, 0.f, 0.f}};
#pragma unroll
        for (int nt = 0; nt < 2; ++nt)
#pragma unroll
            for (int i = 0; i < 4; ++i) {
                const v4u v = *(const LAS v4u*)(L + L_CKV + (16 * nt + (lane & 15)) * CKV_STRIDE + (128 * wave + 32 * i + 8 * (lane >> 4)) * 2);
                acc[nt] = __builtin_amdgcn_mfma_f32_16x16x32_bf16(qa[i], __builtin_bit_cast(bf16x8, v), acc[nt], 0, 0, 0);
                acc[nt] = __builtin_amdgcn_mfma_f32_16x16x32_bf16(qa[4 + i], __builtin_bit_cast(bf16x8, v), acc[nt], 0, 0, 0);
            }
        LAS float* sc = (LAS float*)(S + L_SC);
#pragma unroll
        for (int nt = 0; nt < 2; ++nt)
#pragma unroll
            for (int r = 0; r < 4; ++r) sc[(wave * 16 + 4 * (lane >> 4) + r) * 32 + 16 * nt + (lane & 15)] = acc[nt][r];
    }
    LGKM0();
    BAR();
    {
        const int h = tid >> 5, key = tid & 31;
        const LAS float* sc = (const LAS float*)(S + L_SC);
        float s = 0.f;
#pragma unroll
        for (int w = 0; w < 8; ++w) s += sc[(w * 16 + h) * 32 + key];
        float mx = s;
#pragma unroll
        for (int o = 1; o < 32; o <<= 1) mx = fmaxf(mx, __shfl_xor(mx, o));
        const unsigned pb = f2bf(__builtin_amdgcn_exp2f(s - mx));
        float l = __uint_as_float(pb << 16);
#pragma unroll
        for (int o = 1; o < 32; o <<= 1) l += __shfl_xor(l, o);
        ((LAS unsigned short*)(S + L_P))[h * 32 + key] = (unsigned short)pb;
        if (key == 0) {
            const unsigned long long v = (unsigned long long)__float_as_uint(mx) | ((unsigned long long)__float_as_uint(l) << 32);
            __hip_atomic_store((gu64*)(ws + ML_OFF) + ((size_t)h * 2 + bb) * 128 + sp, v, RLX_AGENT);
        }
    }
    LGKM0();
    BAR();
    {
        const bf16x8 pf = __builtin_bit_cast(bf16x8, *(const LAS v4u*)(S + L_P + (lane & 15) * 64 + (lane >> 4) * 16));
        const int g = lane >> 4, q = (lane & 15) >> 2, p = lane & 3;
        v2u o2[8];
#pragma unroll
        for (int t = 0; t < 8; ++t) {
            const int c0 = 128 * wave + 16 * t;
            const LAS s16x4* a0 = (const LAS s16x4*)(L + L_CKV + (8 * g + q) * CKV_STRIDE + c0 * 2 + 8 * p);
            const LAS s16x4* a1 = (const LAS s16x4*)(L + L_CKV + (8 * g + 4 + q) * CKV_STRIDE + c0 * 2 + 8 * p);
            const s16x4 lo = __builtin_amdgcn_ds_read_tr16_b64_v4i16((LAS s16x4*)a0);
            const s16x4 hi = __builtin_amdgcn_ds_read_tr16_b64_v4i16((LAS s16x4*)a1);
            bf16x8 af;
            af[0] = lo[0]; af[1] = lo[1]; af[2] = lo[2]; af[3] = lo[3]; af[4] = hi[0]; af[5] = hi[1]; af[6] = hi[2]; af[7] = hi[3];
            f32x4 o = {0.f, 0.f, 0.f, 0.f};
            o = __builtin_amdgcn_mfma_f32_16x16x32_bf16(af, pf, o, 0, 0, 0);
            o2[t][0] = pack2(o[0], o[1]); o2[t][1] = pack2(o[2], o[3]);
        }
        LGKM0();
#pragma unroll
        for (int t = 0; t < 8; ++t) *(LAS v2u*)(L + L_ST + (lane & 15) * CKV_STRIDE + (128 * wave + 16 * t + 4 * g) * 2) = o2[t];
    }
    LGKM0();
    BAR();
    {
#pragma unroll
        for (int i = 0; i < 4; ++i) {
            const int h = 2 * wave + (i >> 1);
            const v4u v = *(const LAS v4u*)(L + L_ST + h * CKV_STRIDE + (i & 1) * 1024 + lane * 16);
            __builtin_amdgcn_raw_buffer_store_b128(v, rs_ws, (int)(PCTX_OFF + ((((size_t)bb * NSPLIT + sp) * H + h) * DC) * 2 + (i & 1) * 1024 + lane * 16), 0, 16);
        }
        VM0();
        if (lane == 0) __hip_atomic_store((gu32*)(ws + FLG_OFF) + wave * 256 + cu, 1u, RLX_AGENT);
    }
    STAMP(4);
    if (is_mlp) {
        f32x2 a[8];
#pragma unroll
        for (int j = 0; j < 8; ++j) a[j] = (f32x2){0.f, 0.f};
#pragma unroll
        for (int i = 0; i < 2; ++i) {
            const int row = (tid >> 1) + 256 * i;
            if (row < M) {
                const v4u w = w1v[i];
                const f32x2 xx = ((const LAS f32x2*)(S + L_XN))[row];
#pragma unroll
                for (int j = 0; j < 4; ++j) { a[2 * j] += xx * bflo(w[j]); a[2 * j + 1] += xx * bfhi(w[j]); }
            }
        }
#pragma unroll
        for (int j = 0; j < 8; ++j)
#pragma unroll
            for (int o = 2; o < 64; o <<= 1) { a[j][0] += __shfl_xor(a[j][0], o); a[j][1] += __shfl_xor(a[j][1], o); }
        if (lane < 2) {
            LAS float* rm = (LAS float*)(S + L_RM);
#pragma unroll
            for (int j = 0; j < 8; ++j) { rm[(wave * 2 + 0) * 16 + lane * 8 + j] = a[j][0]; rm[(wave * 2 + 1) * 16 + lane * 8 + j] = a[j][1]; }
        }
    }
    {
        const int fo = (int)FLG_OFF + ((hh >> 1) * 256 + ((lane >> 4) & 1) * 128 + 16 * wave + (lane & 15)) * 4;
        for (unsigned spins = 0;;) {
            const unsigned f = __builtin_amdgcn_raw_buffer_load_b32(rs_ws, fo, 0, 16);
            if (__all(f == 1u)) break;
            if (spin_fail(spins, tmo)) break;
        }
    }
    STAMP(5);
    {
        const int j = lane >> 3, ch = lane & 7;
        v4u pc[4]; v2u ml[4];
#pragma unroll
        for (int i = 0; i < 4; ++i) {
            const int b = i >> 1, s = 16 * wave + (i & 1) * 8 + j;
            ml[i] = __builtin_amdgcn_raw_buffer_load_b64(rs_ws, (int)(ML_OFF + (((size_t)hh * 2 + b) * 128 + s) * 8), 0, 16);
            pc[i] = __builtin_amdgcn_raw_buffer_load_b128(rs_ws, (int)(PCTX_OFF + ((((size_t)b * NSPLIT + s) * H + hh) * DC + 64 * cs + ch * 8) * 2), 0, 16);
        }
        LAS float* red = (LAS float*)(S + L_SC);
#pragma unroll
        for (int b = 0; b < 2; ++b) {
            const float m0 = __uint_as_float(ml[2 * b][0]), l0 = __uint_as_float(ml[2 * b][1]);
            const float m1 = __uint_as_float(ml[2 * b + 1][0]), l1 = __uint_as_float(ml[2 * b + 1][1]);
            float gm = fmaxf(m0, m1);
            gm = fmaxf(gm, __shfl_xor(gm, 8)); gm = fmaxf(gm, __shfl_xor(gm, 16)); gm = fmaxf(gm, __shfl_xor(gm, 32));
            const float e0 = __builtin_amdgcn_exp2f(m0 - gm), e1 = __builtin_amdgcn_exp2f(m1 - gm);
            float ls = e0 * l0 + e1 * l1;
            ls += __shfl_xor(ls, 8); ls += __shfl_xor(ls, 16); ls += __shfl_xor(ls, 32);
            float acc[8];
#pragma unroll
            for (int k = 0; k < 4; ++k) {
                acc[2 * k] = e0 * bflo(pc[2 * b][k]) + e1 * bflo(pc[2 * b + 1][k]);
                acc[2 * k + 1] = e0 * bfhi(pc[2 * b][k]) + e1 * bfhi(pc[2 * b + 1][k]);
            }
#pragma unroll
            for (int k = 0; k < 8; ++k) { float v = acc[k]; v += __shfl_xor(v, 8); v += __shfl_xor(v, 16); v += __shfl_xor(v, 32); acc[k] = v; }
            if (lane < 8) {
                *(LAS f32x4*)(red + (wave * 2 + b) * 64 + lane * 8) = (f32x4){acc[0], acc[1], acc[2], acc[3]};
                *(LAS f32x4*)(red + (wave * 2 + b) * 64 + lane * 8 + 4) = (f32x4){acc[4], acc[5], acc[6], acc[7]};
            }
            if (lane == 0) ((LAS f32x2*)(S + L_STAT))[wave * 2 + b] = (f32x2){gm, ls};
        }
    }
    STAMP(11);
    LGKM0();
    BAR();
    if (tid < 128) {
        const int b = tid >> 6;
        const LAS float* red = (const LAS float*)(S + L_SC);
        const LAS f32x2* st = (const LAS f32x2*)(S + L_STAT);
        float gm = st[b][0];
#pragma unroll
        for (int w = 1; w < 8; ++w) gm = fmaxf(gm, st[w * 2 + b][0]);
        float s = 0.f, ls = 0.f;
#pragma unroll
        for (int w = 0; w < 8; ++w) {
            const f32x2 t = st[w * 2 + b];
            const float e = __builtin_amdgcn_exp2f(t[0] - gm);
            ls += e * t[1]; s += e * red[w * 128 + tid];
        }
        ((LAS float*)(S + L_CC))[tid] = s / ls;
    } else if (tid < 160 && is_mlp) {
        const int b = (tid >> 4) & 1, f = tid & 15;
        const LAS float* rm = (const LAS float*)(S + L_RM);
        float hv = bf2f(b1[16 * mj + f]);
#pragma unroll
        for (int w = 0; w < 8; ++w) hv += rm[(w * 2 + b) * 16 + f];
        const float t = tanhf(0.7978845608028654f * (hv + 0.044715f * hv * hv * hv));
        ((LAS float*)(S + L_HM))[b * 16 + f] = 0.5f * hv * (1.0f + t);
    }
    LGKM0();
    BAR();
    {
        const LAS float* cc = (const LAS float*)(S + L_CC);
        f32x2 a[8];
#pragma unroll
        for (int j = 0; j < 8; ++j) a[j] = (f32x2){0.f, 0.f};
#pragma unroll
        for (int i = 0; i < 4; ++i) {
            const int c = 8 * wave + 2 * i + (lane >> 5);
            const v4u w = *(const LAS v4u*)(L + L_WUV + c * 512 + (lane & 31) * 16);
            const f32x2 cv = {cc[c], cc[64 + c]};
#pragma unroll
            for (int j = 0; j < 4; ++j) { a[2 * j] += cv * bflo(w[j]); a[2 * j + 1] += cv * bfhi(w[j]); }
        }
#pragma unroll
        for (int j = 0; j < 8; ++j) { a[j][0] += __shfl_xor(a[j][0], 32); a[j][1] += __shfl_xor(a[j][1], 32); }
        if (lane < 32) {
            LAS f32x4* red = (LAS f32x4*)(S + L_SC + 4096);
#pragma unroll
            for (int b = 0; b < 2; ++b) {
                red[((wave * 2 + b) * 256 + lane * 8) / 4] = (f32x4){a[0][b], a[1][b], a[2][b], a[3][b]};
                red[((wave * 2 + b) * 256 + lane * 8) / 4 + 1] = (f32x4){a[4][b], a[5][b], a[6][b], a[7][b]};
            }
        }
    }
    LGKM0();
    BAR();
    STAMP(6);
    {
        const LAS float* red = (const LAS float*)(S + L_SC + 4096);
        float cv = 0.f;
#pragma unroll
        for (int w = 0; w < 8; ++w) cv += red[(w * 2 + (lane >> 5)) * 256 + 32 * wave + (lane & 31)];
        f32x2 a0[4], a1[4];
#pragma unroll
        for (int j = 0; j < 4; ++j) { a0[j] = (f32x2){0.f, 0.f}; a1[j] = (f32x2){0.f, 0.f}; }
#pragma unroll
        for (int r = 0; r < 32; ++r) {
            const float c0 = __builtin_bit_cast(float, __builtin_amdgcn_readlane(__builtin_bit_cast(int, cv), r));
            const float c1 = __builtin_bit_cast(float, __builtin_amdgcn_readlane(__builtin_bit_cast(int, cv), 32 + r));
#pragma unroll
            for (int j = 0; j < 4; ++j) {
                const f32x2 w = {bflo(wov[r][j]), bfhi(wov[r][j])};
                a0[j] += w * c0; a1[j] += w * c1;
            }
        }
        if (is_mlp) {
            const LAS float* hm = (const LAS float*)(S + L_HM);
#pragma unroll
            for (int i = 0; i < 2; ++i) {
                const int f = 2 * wave + i;
                const v4u wv = w2v[i];
                const float c0 = hm[f], c1 = hm[16 + f];
#pragma unroll
                for (int j = 0; j < 4; ++j) {
                    const f32x2 w = {bflo(wv[j]), bfhi(wv[j])};
                    a0[j] += w * c0; a1[j] += w * c1;
                }
            }
        }
        if (lane < 56) {
            LAS f32x4* r3 = (LAS f32x4*)(L + L_CKV);
            r3[((wave * 2 + 0) * M + lane * 8) / 4] = (f32x4){a0[0][0], a0[0][1], a0[1][0], a0[1][1]};
            r3[((wave * 2 + 0) * M + lane * 8) / 4 + 1] = (f32x4){a0[2][0], a0[2][1], a0[3][0], a0[3][1]};
            r3[((wave * 2 + 1) * M + lane * 8) / 4] = (f32x4){a1[0][0], a1[0][1], a1[1][0], a1[1][1]};
            r3[((wave * 2 + 1) * M + lane * 8) / 4 + 1] = (f32x4){a1[2][0], a1[2][1], a1[3][0], a1[3][1]};
        }
    }
    LGKM0();
    BAR();
    if (tid < 224) {
        const int j = tid >> 1, b = tid & 1;
        const LAS f32x4* r3 = (const LAS f32x4*)(L + L_CKV);
        f32x4 a = r3[(b * M) / 4 + j];
#pragma unroll
        for (int w = 1; w < 8; ++w) a += r3[((w * 2 + b) * M) / 4 + j];
        v4u v; v[0] = canon(__float_as_uint(a[0])); v[1] = canon(__float_as_uint(a[1])); v[2] = canon(__float_as_uint(a[2])); v[3] = canon(__float_as_uint(a[3]));
        __builtin_amdgcn_raw_buffer_store_b128(v, rs_ws, (int)(G5_OFF + (((size_t)j * 256 + cu) * 8 + b * 4) * 4), 0, 16);
    }
    STAMP(8);
    if (cu < NC5) {
        v4u v;
        for (unsigned spins = 0;;) {
            v = __builtin_amdgcn_raw_buffer_load_b128(rs_ws, (int)(G5_OFF + (size_t)cu * 8192 + tid * 16), 0, 16);
            if (!__any(has_sent(v))) break;
            if (spin_fail(spins, tmo)) break;
        }
        f32x4 s; s[0] = __uint_as_float(v[0]); s[1] = __uint_as_float(v[1]); s[2] = __uint_as_float(v[2]); s[3] = __uint_as_float(v[3]);
#pragma unroll
        for (int o = 2; o < 64; o <<= 1) { s[0] += __shfl_xor(s[0], o); s[1] += __shfl_xor(s[1], o); s[2] += __shfl_xor(s[2], o); s[3] += __shfl_xor(s[3], o); }
        LAS float* red = (LAS float*)(S + L_CC);
        if (lane < 2) { red[wave * 8 + lane * 4 + 0] = s[0]; red[wave * 8 + lane * 4 + 1] = s[1]; red[wave * 8 + lane * 4 + 2] = s[2]; red[wave * 8 + lane * 4 + 3] = s[3]; }
        LGKM0();
        BAR();
        if (tid < 8) {
            float t = 0.f;
#pragma unroll
            for (int w = 0; w < 8; ++w) t += red[w * 8 + tid];
            const int b = tid >> 2, m = cu * 4 + (tid & 3);
            t += bf2f(x[b * M + m]) + bf2f(b2[m]);
            out[b * M + m] = (unsigned short)f2bf(t);
        }
        STAMP(9);
    }
}
}

extern "C" void kernel_launch(void* const* d_in, const int* in_sizes, int n_in,
                              void* d_out, int out_size, void* d_ws, size_t ws_size,
                              hipStream_t stream) {
    const unsigned short* x = (const unsigned short*)d_in[0];
    const unsigned short* ckv = (const unsigned short*)d_in[1];
    const unsigned short* wq = (const unsigned short*)d_in[2];
    const unsigned short* wuk = (const unsigned short*)d_in[3];
    const unsigned short* wuv = (const unsigned short*)d_in[4];
    const unsigned short* wo = (const unsigned short*)d_in[5];
    const unsigned short* w1 = (const unsigned short*)d_in[7];
    const unsigned short* b1 = (const unsigned short*)d_in[8];
    const unsigned short* w2 = (const unsigned short*)d_in[9];
    const unsigned short* b2 = (const unsigned short*)d_in[10];
    unsigned char* ws = (unsigned char*)d_ws;
    k1<<<128, 256, 0, stream>>>(ws);
    k2<<<NCU, 512, 0, stream>>>(x, ckv, wq, wuk, wuv, wo, w1, b1, w2, b2, ws, (unsigned short*)d_out);
}
```

```cpp
#include <hip/hip_runtime.h>
#include <stdint.h>

#define GAS __attribute__((address_space(1)))
#define LAS __attribute__((address_space(3)))
typedef unsigned v4u __attribute__((ext_vector_type(4)));
typedef unsigned v2u __attribute__((ext_vector_type(2)));
typedef float f32x4 __attribute__((ext_vector_type(4)));
typedef float f32x2 __attribute__((ext_vector_type(2)));
typedef short bf16x8 __attribute__((ext_vector_type(8)));
typedef short s16x4 __attribute__((ext_vector_type(4)));
typedef GAS unsigned gu32;
typedef GAS unsigned long long gu64;
#define RLX_AGENT __ATOMIC_RELAXED, __HIP_MEMORY_SCOPE_AGENT

#define STAMPS 0

namespace {
constexpr int H = 16, SK = 4096, D = 256, M = 448, DC = 1024, F = 1792;
constexpr int NCU = 256;
constexpr int NSPLIT = 128, KPS = 32;
constexpr int NC5 = 112;

constexpr size_t FLG_OFF = 0;
constexpr size_t TMO_OFF = 131072;
constexpr size_t QABS_OFF = TMO_OFF + 4096;
constexpr size_t G4_OFF = QABS_OFF + 65536;
constexpr size_t G4_BYTES = (size_t)256 * 512 * 4;
constexpr size_t G5_OFF = G4_OFF + G4_BYTES;
constexpr size_t G5_BYTES = (size_t)NC5 * 256 * 8 * 4;
constexpr size_t FILL_BYTES = G5_OFF + G5_BYTES;
constexpr size_t QP_OFF = FILL_BYTES;
constexpr size_t HP_OFF = QP_OFF + 4 * 2 * 4096 * 4;
constexpr size_t PCTX_OFF = HP_OFF + 65536;
constexpr size_t PCTX_BYTES = (size_t)2 * 128 * 16 * 1024 * 2;
constexpr size_t STAMP_OFF = PCTX_OFF + PCTX_BYTES;
constexpr size_t WS_NEED = STAMP_OFF + 256 * 16 * 8;
static_assert(FILL_BYTES % 16 == 0, "fill");

constexpr int CKV_STRIDE = 2064;
constexpr int L_CKV = 0;
constexpr int LDS_DMA_BYTES = 32 * CKV_STRIDE;
constexpr int L_SC = 0;
constexpr int L_WO = L_SC + 20480;
constexpr int L_W2 = L_WO + 14336;
constexpr int L_QA = L_W2 + 7168;
constexpr int L_QS = L_QA + 2048;
constexpr int L_P = L_QS + 512;
constexpr int L_STAT = L_P + 1024;
constexpr int L_CC = L_STAT + 128;
constexpr int L_HM = L_CC + 512;
constexpr int L_X6 = L_HM + 128;
constexpr int L_ST = L_CKV;
constexpr int LDS_SC_BYTES = L_X6 + 256;
static_assert(LDS_DMA_BYTES + LDS_SC_BYTES <= 160 * 1024, "lds");

constexpr unsigned kSpinCap = 1u << 18;
constexpr unsigned SENT = 0xFFFFFFFFu;

__device__ __forceinline__ float bf2f(unsigned short u) { return __uint_as_float(((unsigned)u) << 16); }
__device__ __forceinline__ float bflo(unsigned u) { return __uint_as_float(u << 16); }
__device__ __forceinline__ float bfhi(unsigned u) { return __uint_as_float(u & 0xffff0000u); }
__device__ __forceinline__ unsigned f2bf(float f) { unsigned u = __float_as_uint(f); return (u + 0x7fffu + ((u >> 16) & 1u)) >> 16; }
__device__ __forceinline__ unsigned pack2(float a, float b) { return f2bf(a) | (f2bf(b) << 16); }
__device__ __forceinline__ float wsum(float v) { for (int o = 32; o > 0; o >>= 1) v += __shfl_xor(v, o); return v; }
__device__ __forceinline__ unsigned long long rt() { return __builtin_amdgcn_s_memrealtime(); }

__global__ void __launch_bounds__(256) k1(const unsigned short* __restrict__ x, const unsigned short* __restrict__ wq,
                                          const unsigned short* __restrict__ w1, unsigned char* __restrict__ ws) {
    __shared__ float xn[2][M];
    __shared__ float red[4][2][64];
    const int tid = threadIdx.x, wave = tid >> 6, lane = tid & 63;
    const int blk = blockIdx.x;
    const bool isq = blk < 256;
    const int idx = isq ? blk : blk - 256;
    const int mq = idx & 3, cg = idx >> 2;
    const int ncols = isq ? 4096 : F;
    const unsigned short* W = (isq ? wq : w1) + (size_t)(mq * 112) * ncols + cg * 64;
    const int r = tid >> 3, cch = tid & 7;
    v4u wv[4];
#pragma unroll
    for (int i = 0; i < 4; ++i) {
        const int row = r + 32 * i;
        if (i < 3 || r < 16) wv[i] = __builtin_nontemporal_load((const v4u*)(W + (size_t)row * ncols + cch * 8));
        else wv[i] = (v4u){0u, 0u, 0u, 0u};
    }
    {
        const v4u sn = {SENT, SENT, SENT, SENT};
        v4u* g = (v4u*)ws;
        const unsigned tot16 = (unsigned)(FILL_BYTES / 16);
        for (unsigned i = blk * 256 + tid; i < tot16; i += gridDim.x * 256) g[i] = sn;
    }
    if (wave < 2) {
        float v[7]; float s = 0.f;
#pragma unroll
        for (int i = 0; i < 7; ++i) { v[i] = bf2f(x[wave * M + lane + 64 * i]); s += v[i]; }
        s = wsum(s); const float mean = s * (1.0f / M);
        float ss = 0.f;
#pragma unroll
        for (int i = 0; i < 7; ++i) { const float d = v[i] - mean; ss += d * d; }
        ss = wsum(ss);
        const float rs = 1.0f / sqrtf(ss * (1.0f / M) + 1e-5f);
#pragma unroll
        for (int i = 0; i < 7; ++i) xn[wave][lane + 64 * i] = (v[i] - mean) * rs;
    }
    __syncthreads();
    float a[2][8];
#pragma unroll
    for (int j = 0; j < 8; ++j) { a[0][j] = 0.f; a[1][j] = 0.f; }
#pragma unroll
    for (int i = 0; i < 4; ++i) {
        int row = mq * 112 + r + 32 * i; if (row >= M) row = M - 1;
        const float x0 = xn[0][row], x1 = xn[1][row];
#pragma unroll
        for (int j = 0; j < 4; ++j) {
            const float lo = bflo(wv[i][j]), hi = bfhi(wv[i][j]);
            a[0][2 * j] += x0 * lo; a[0][2 * j + 1] += x0 * hi;
            a[1][2 * j] += x1 * lo; a[1][2 * j + 1] += x1 * hi;
        }
    }
#pragma unroll
    for (int b = 0; b < 2; ++b)
#pragma unroll
        for (int j = 0; j < 8; ++j) { float v = a[b][j]; v += __shfl_xor(v, 8); v += __shfl_xor(v, 16); v += __shfl_xor(v, 32); a[b][j] = v; }
    if (lane < 8) {
#pragma unroll
        for (int b = 0; b < 2; ++b)
#pragma unroll
            for (int j = 0; j < 8; ++j) red[wave][b][lane * 8 + j] = a[b][j];
    }
    __syncthreads();
    if (tid < 128) {
        const int b = tid >> 6, c = tid & 63;
        const float s = red[0][b][c] + red[1][b][c] + red[2][b][c] + red[3][b][c];
        float* dst = (float*)(ws + (isq ? QP_OFF : HP_OFF));
        dst[((size_t)mq * 2 + b) * ncols + cg * 64 + c] = s;
    }
}

__device__ __forceinline__ bool spin_fail(unsigned& spins, gu32* tmo) {
    __builtin_amdgcn_s_sleep(1);
    if (++spins > kSpinCap) { __hip_atomic_store(tmo, 1u, RLX_AGENT); return true; }
    return (spins & 1023u) == 0u && __builtin_amdgcn_readfirstlane(__hip_atomic_load(tmo, RLX_AGENT)) == 1u;
}
__device__ __forceinline__ bool has_sent(v4u v) { return v[0] == SENT || v[1] == SENT || v[2] == SENT || v[3] == SENT; }
__device__ __forceinline__ unsigned canon(unsigned u) { return u == SENT ? 0x7FC00000u : u; }
__device__ __forceinline__ unsigned canonb(unsigned u) { return u == SENT ? 0xFFC0FFC0u : u; }

#define GLDS_NT(src, dst) __builtin_amdgcn_global_load_lds((const unsigned*)(src), (LAS unsigned*)(dst), 16, 0, 2)
#define BAR() __builtin_amdgcn_s_barrier()
#define LGKM0() asm volatile("s_waitcnt lgkmcnt(0)" ::: "memory")
#define VM0() asm volatile("s_waitcnt vmcnt(0)" ::: "memory")
#define PIN() __builtin_amdgcn_sched_barrier(0)
#define STAMP(i) do {} while (0)
#define STAMPW(i, w) do {} while (0)

__global__ void __launch_bounds__(512, 1) k2(const unsigned short* __restrict__ x, const unsigned short* __restrict__ ckv,
                                             const unsigned short* __restrict__ wuk, const unsigned short* __restrict__ wuv,
                                             const unsigned short* __restrict__ wo, const unsigned short* __restrict__ b1,
                                             const unsigned short* __restrict__ w2, const unsigned short* __restrict__ b2,
                                             unsigned char* __restrict__ ws, unsigned short* __restrict__ out) {
    __shared__ __attribute__((aligned(16))) unsigned char lds_dma[LDS_DMA_BYTES];
    __shared__ __attribute__((aligned(16))) unsigned char lds_sc[LDS_SC_BYTES];
    LAS unsigned char* L = (LAS unsigned char*)lds_dma;
    LAS unsigned char* S = (LAS unsigned char*)lds_sc;
    const int tid = threadIdx.x, wave = __builtin_amdgcn_readfirstlane(tid >> 6), lane = tid & 63;
    const int cu = blockIdx.x;
    const int hh = cu >> 4, cs = cu & 15;
    const int bb = cu >> 7, sp = cu & 127;
    gu32* tmo = (gu32*)(ws + TMO_OFF);
    const __amdgpu_buffer_rsrc_t rs_ws = __builtin_amdgcn_make_buffer_rsrc(ws, 0, (int)WS_NEED, 0x00020000);
    STAMP(0);

    float qp[4];
    {
        const float* p = (const float*)(ws + QP_OFF) + (size_t)(tid >> 8) * 4096 + hh * D + (tid & 255);
#pragma unroll
        for (int i = 0; i < 4; ++i) asm volatile("global_load_dword %0, %1, off" : "=v"(qp[i]) : "v"(p + (size_t)i * 8192));
    }
    v4u wk[8];
    {
        const unsigned short* src = ckv + ((size_t)bb * SK + (size_t)sp * KPS + 4 * wave) * DC;
        if (wave < 4) {
            const unsigned short* p = wuk + ((size_t)(64 * cs + 16 * wave + (lane & 15)) * H + hh) * D + 8 * (lane >> 4);
#pragma unroll
            for (int ks = 0; ks < 8; ++ks) asm volatile("global_load_dwordx4 %0, %1, off nt" : "=v"(wk[ks]) : "v"(p + 32 * ks));
        }
#pragma unroll
        for (int i = 0; i < 8; ++i) GLDS_NT(src + (size_t)i * 512 + lane * 8, L + L_CKV + (4 * wave + (i >> 1)) * CKV_STRIDE + (i & 1) * 1024);
    }
    PIN();
    if (wave < 4) asm volatile("s_waitcnt vmcnt(16)" ::: "memory");
    else asm volatile("s_waitcnt vmcnt(8)" ::: "memory");
    asm volatile("" : "+v"(qp[0]), "+v"(qp[1]), "+v"(qp[2]), "+v"(qp[3]));
    {
        float s = (qp[0] + qp[1]) + (qp[2] + qp[3]);
        s *= 0.0625f * 1.4426950408889634f;
        const unsigned hi = f2bf(s);
        const unsigned lo = f2bf(s - __uint_as_float(hi << 16));
        const int b = tid >> 8, d = tid & 255;
        ((LAS unsigned short*)(S + L_QA))[(2 * b) * 256 + d] = (unsigned short)hi;
        ((LAS unsigned short*)(S + L_QA))[(2 * b + 1) * 256 + d] = (unsigned short)lo;
    }
    LGKM0();
    BAR();
    STAMP(1);
    if (wave < 4) {
        v4u av[8];
#pragma unroll
        for (int ks = 0; ks < 8; ++ks) {
            av[ks] = *(const LAS v4u*)(S + L_QA + (lane & 3) * 512 + (32 * ks + 8 * (lane >> 4)) * 2);
        }
        const bool keep = (lane & 15) < 4;
        asm volatile("s_waitcnt vmcnt(8)" : "+v"(wk[0]), "+v"(wk[1]), "+v"(wk[2]), "+v"(wk[3]), "+v"(wk[4]), "+v"(wk[5]), "+v"(wk[6]), "+v"(wk[7]));
        f32x4 acc = {0.f, 0.f, 0.f, 0.f};
#pragma unroll
        for (int ks = 0; ks < 8; ++ks) {
            v4u a = av[ks];
            a[0] = keep ? a[0] : 0u; a[1] = keep ? a[1] : 0u; a[2] = keep ? a[2] : 0u; a[3] = keep ? a[3] : 0u;
            acc = __builtin_amdgcn_mfma_f32_16x16x32_bf16(__builtin_bit_cast(bf16x8, a), __builtin_bit_cast(bf16x8, wk[ks]), acc, 0, 0, 0);
        }
        LAS unsigned short* qs = (LAS unsigned short*)(S + L_QS) + wave * 64;
        if (lane < 16) {
            qs[lane] = (unsigned short)f2bf(acc[0] + acc[1]);
            qs[16 + lane] = (unsigned short)f2bf(acc[2] + acc[3]);
        }
        LGKM0();
        if (lane < 4) {
            const int b = lane >> 1, half = lane & 1;
            v4u v = *(const LAS v4u*)(S + L_QS + wave * 128 + b * 32 + half * 16);
            v[0] = canonb(v[0]); v[1] = canonb(v[1]); v[2] = canonb(v[2]); v[3] = canonb(v[3]);
            __builtin_amdgcn_raw_buffer_store_b128(v, rs_ws, (int)(QABS_OFF + (((size_t)b * H + hh) * DC + 64 * cs + 16 * wave + 8 * half) * 2), 0, 16);
        }
    } else if (wave == 4 && lane < 14) {
        const int b = lane / 7, r = lane % 7, f = cu * 7 + r;
        const float* hp = (const float*)(ws + HP_OFF);
        float hv = bf2f(b1[f]);
#pragma unroll
        for (int p = 0; p < 4; ++p) hv += hp[((size_t)p * 2 + b) * F + f];
        const float t = tanhf(0.7978845608028654f * (hv + 0.044715f * hv * hv * hv));
        ((LAS float*)(S + L_HM))[b * 8 + r] = 0.5f * hv * (1.0f + t);
    }
    STAMP(2);
    PIN();
    v4u wuvv[4], wov[2], w2v;
    {
#pragma unroll
        for (int i = 0; i < 4; ++i) {
            const int c = 64 * cs + 8 * wave + 2 * i + (lane >> 5);
            wuvv[i] = __builtin_nontemporal_load((const v4u*)(wuv + ((size_t)c * H + hh) * D + (lane & 31) * 8));
        }
        const unsigned short* so = wo + ((size_t)hh * D + cs * 16) * M;
        const int t2 = tid < 448 ? tid : 447;
        wov[0] = __builtin_nontemporal_load((const v4u*)(so + (size_t)t2 * 8));
        wov[1] = __builtin_nontemporal_load((const v4u*)(so + (size_t)(448 + t2) * 8));
        w2v = __builtin_nontemporal_load((const v4u*)(w2 + (size_t)cu * 7 * M + (size_t)(tid < 392 ? tid : 391) * 8));
    }
    PIN();
    const int qbase = (int)(QABS_OFF + (((size_t)bb * H + (lane & 15)) * DC + 128 * wave) * 2);
    for (unsigned spins = 0;;) {
        const v4u v = __builtin_amdgcn_raw_buffer_load_b128(rs_ws, qbase + (64 * ((lane >> 4) & 1) + 48 * (lane >> 5)) * 2, 0, 16);
        if (!__any(has_sent(v))) break;
        if (spin_fail(spins, tmo)) break;
    }
    bf16x8 qa[4];
    for (unsigned spins = 0;;) {
        v4u v[4]; bool bad = false;
#pragma unroll
        for (int i = 0; i < 4; ++i)
            v[i] = __builtin_amdgcn_raw_buffer_load_b128(rs_ws, qbase + (32 * i + 8 * (lane >> 4)) * 2, 0, 16);
#pragma unroll
        for (int i = 0; i < 4; ++i) { bad |= has_sent(v[i]); qa[i] = __builtin_bit_cast(bf16x8, v[i]); }
        if (!__any(bad)) break;
        if (spin_fail(spins, tmo)) break;
    }
    VM0();
    STAMP(3);
    STAMPW(14, 7);
    BAR();
    STAMP(10);
    {
        f32x4 acc[2] = {{0.f, 0.f, 0.f, 0.f}, {0.f, 0.f, 0.f, 0.f}};
#pragma unroll
        for (int nt = 0; nt < 2; ++nt)
#pragma unroll
            for (int i = 0; i < 4; ++i) {
                const v4u v = *(const LAS v4u*)(L + L_CKV + (16 * nt + (lane & 15)) * CKV_STRIDE + (128 * wave + 32 * i + 8 * (lane >> 4)) * 2);
                acc[nt] = __builtin_amdgcn_mfma_f32_16x16x32_bf16(qa[i], __builtin_bit_cast(bf16x8, v), acc[nt], 0, 0, 0);
            }
        LAS float* sc = (LAS float*)(S + L_SC);
#pragma unroll
        for (int nt = 0; nt < 2; ++nt)
#pragma unroll
            for (int r = 0; r < 4; ++r) sc[(wave * 16 + 4 * (lane >> 4) + r) * 32 + 16 * nt + (lane & 15)] = acc[nt][r];
    }
    LGKM0();
    BAR();
    float st_m, st_l;
    {
        const int h = tid >> 5, key = tid & 31;
        const LAS float* sc = (const LAS float*)(S + L_SC);
        float s = 0.f;
#pragma unroll
        for (int w = 0; w < 8; ++w) s += sc[(w * 16 + h) * 32 + key];
        float mx = s;
#pragma unroll
        for (int o = 1; o < 32; o <<= 1) mx = fmaxf(mx, __shfl_xor(mx, o));
        const unsigned pb = f2bf(__builtin_amdgcn_exp2f(s - mx));
        float l = __uint_as_float(pb << 16);
#pragma unroll
        for (int o = 1; o < 32; o <<= 1) l += __shfl_xor(l, o);
        ((LAS unsigned short*)(S + L_P))[h * 32 + key] = (unsigned short)pb;
        st_m = mx; st_l = l;
    }
    LGKM0();
    BAR();
    {
        const bf16x8 pf = __builtin_bit_cast(bf16x8, *(const LAS v4u*)(S + L_P + (lane & 15) * 64 + (lane >> 4) * 16));
        const int g = lane >> 4, q = (lane & 15) >> 2, p = lane & 3;
        v2u o2[8];
#pragma unroll
        for (int t = 0; t < 8; ++t) {
            const int c0 = 128 * wave + 16 * t;
            const LAS s16x4* a0 = (const LAS s16x4*)(L + L_CKV + (8 * g + q) * CKV_STRIDE + c0 * 2 + 8 * p);
            const LAS s16x4* a1 = (const LAS s16x4*)(L + L_CKV + (8 * g + 4 + q) * CKV_STRIDE + c0 * 2 + 8 * p);
            const s16x4 lo = __builtin_amdgcn_ds_read_tr16_b64_v4i16((LAS s16x4*)a0);
            const s16x4 hi = __builtin_amdgcn_ds_read_tr16_b64_v4i16((LAS s16x4*)a1);
            bf16x8 af;
            af[0] = lo[0]; af[1] = lo[1]; af[2] = lo[2]; af[3] = lo[3]; af[4] = hi[0]; af[5] = hi[1]; af[6] = hi[2]; af[7] = hi[3];
            f32x4 o = {0.f, 0.f, 0.f, 0.f};
            o = __builtin_amdgcn_mfma_f32_16x16x32_bf16(af, pf, o, 0, 0, 0);
            o2[t][0] = pack2(o[0], o[1]); o2[t][1] = pack2(o[2], o[3]);
        }
        LGKM0();
#pragma unroll
        for (int t = 0; t < 8; ++t) *(LAS v2u*)(L + L_ST + (lane & 15) * CKV_STRIDE + (128 * wave + 16 * t + 4 * g) * 2) = o2[t];
    }
    LGKM0();
    BAR();
    {
#pragma unroll
        for (int i = 0; i < 4; ++i) {
            const int h = 2 * wave + (i >> 1);
            const v4u v = *(const LAS v4u*)(L + L_ST + h * CKV_STRIDE + (i & 1) * 1024 + lane * 16);
            __builtin_amdgcn_raw_buffer_store_b128(v, rs_ws, (int)(PCTX_OFF + ((((size_t)bb * NSPLIT + sp) * H + h) * DC) * 2 + (i & 1) * 1024 + lane * 16), 0, 16);
        }
        v4u fv;
        fv[0] = canon(__float_as_uint(st_m)); fv[1] = canon(__float_as_uint(st_l));
        fv[2] = canon(__float_as_uint(__shfl(st_m, 32))); fv[3] = canon(__float_as_uint(__shfl(st_l, 32)));
        if (lane != 0) fv = (v4u){0u, 0u, 0u, 0u};
        VM0();
        if (lane < 4) __builtin_amdgcn_raw_buffer_store_b128(fv, rs_ws, (int)(FLG_OFF + ((size_t)cu * 8 + wave) * 64 + lane * 16), 0, 16);
    }
    STAMP(4);
    if (tid < 448) { *(LAS v4u*)(S + L_WO + tid * 16) = wov[0]; *(LAS v4u*)(S + L_WO + (448 + tid) * 16) = wov[1]; }
    if (tid < 392) *(LAS v4u*)(S + L_W2 + tid * 16) = w2v;
    {
        const int j = lane >> 3, ch = lane & 7;
        v4u pc[4]; v2u ml[4];
        for (unsigned spins = 0;;) {
            bool bad = false;
#pragma unroll
            for (int i = 0; i < 4; ++i) {
                const int b = i >> 1, s = 16 * wave + (i & 1) * 8 + j;
                ml[i] = __builtin_amdgcn_raw_buffer_load_b64(rs_ws, (int)(FLG_OFF + (((size_t)b * 128 + s) * 8 + (hh >> 1)) * 64 + (hh & 1) * 8), 0, 16);
            }
#pragma unroll
            for (int i = 0; i < 4; ++i) bad |= (ml[i][0] == SENT) || (ml[i][1] == SENT);
            if (!__any(bad)) break;
            if (spin_fail(spins, tmo)) break;
        }
        STAMP(5);
#pragma unroll
        for (int i = 0; i < 4; ++i) {
            const int b = i >> 1, s = 16 * wave + (i & 1) * 8 + j;
            pc[i] = __builtin_amdgcn_raw_buffer_load_b128(rs_ws, (int)(PCTX_OFF + ((((size_t)b * NSPLIT + s) * H + hh) * DC + 64 * cs + ch * 8) * 2), 0, 16);
        }
        LAS float* red = (LAS float*)(S + L_SC);
#pragma unroll
        for (int b = 0; b < 2; ++b) {
            const float m0 = __uint_as_float(ml[2 * b][0]), l0 = __uint_as_float(ml[2 * b][1]);
            const float m1 = __uint_as_float(ml[2 * b + 1][0]), l1 = __uint_as_float(ml[2 * b + 1][1]);
            float gm = fmaxf(m0, m1);
            gm = fmaxf(gm, __shfl_xor(gm, 8)); gm = fmaxf(gm, __shfl_xor(gm, 16)); gm = fmaxf(gm, __shfl_xor(gm, 32));
            const float e0 = __builtin_amdgcn_exp2f(m0 - gm), e1 = __builtin_amdgcn_exp2f(m1 - gm);
            float ls = e0 * l0 + e1 * l1;
            ls += __shfl_xor(ls, 8); ls += __shfl_xor(ls, 16); ls += __shfl_xor(ls, 32);
            float acc[8];
#pragma unroll
            for (int k = 0; k < 4; ++k) {
                acc[2 * k] = e0 * bflo(pc[2 * b][k]) + e1 * bflo(pc[2 * b + 1][k]);
                acc[2 * k + 1] = e0 * bfhi(pc[2 * b][k]) + e1 * bfhi(pc[2 * b + 1][k]);
            }
#pragma unroll
            for (int k = 0; k < 8; ++k) { float v = acc[k]; v += __shfl_xor(v, 8); v += __shfl_xor(v, 16); v += __shfl_xor(v, 32); acc[k] = v; }
            if (lane < 8) {
                *(LAS f32x4*)(red + (wave * 2 + b) * 64 + lane * 8) = (f32x4){acc[0], acc[1], acc[2], acc[3]};
                *(LAS f32x4*)(red + (wave * 2 + b) * 64 + lane * 8 + 4) = (f32x4){acc[4], acc[5], acc[6], acc[7]};
            }
            if (lane == 0) ((LAS f32x2*)(S + L_STAT))[wave * 2 + b] = (f32x2){gm, ls};
        }
    }
    STAMP(11);
    LGKM0();
    BAR();
    STAMP(12);
    if (tid < 128) {
        const int b = tid >> 6;
        const LAS float* red = (const LAS float*)(S + L_SC);
        const LAS f32x2* st = (const LAS f32x2*)(S + L_STAT);
        float gm = st[b][0];
#pragma unroll
        for (int w = 1; w < 8; ++w) gm = fmaxf(gm, st[w * 2 + b][0]);
        float s = 0.f, ls = 0.f;
#pragma unroll
        for (int w = 0; w < 8; ++w) {
            const f32x2 t = st[w * 2 + b];
            const float e = __builtin_amdgcn_exp2f(t[0] - gm);
            ls += e * t[1]; s += e * red[w * 128 + tid];
        }
        ((LAS float*)(S + L_CC))[tid] = s / ls;
    }
    LGKM0();
    BAR();
    {
        const LAS float* cc = (const LAS float*)(S + L_CC);
        f32x2 a[8];
#pragma unroll
        for (int j = 0; j < 8; ++j) a[j] = (f32x2){0.f, 0.f};
#pragma unroll
        for (int i = 0; i < 4; ++i) {
            const int c = 8 * wave + 2 * i + (lane >> 5);
            const v4u w = wuvv[i];
            const f32x2 cv = {cc[c], cc[64 + c]};
#pragma unroll
            for (int j = 0; j < 4; ++j) { a[2 * j] += cv * bflo(w[j]); a[2 * j + 1] += cv * bfhi(w[j]); }
        }
#pragma unroll
        for (int j = 0; j < 8; ++j) { a[j][0] += __shfl_xor(a[j][0], 32); a[j][1] += __shfl_xor(a[j][1], 32); }
        if (lane < 32) {
            LAS f32x4* red = (LAS f32x4*)(S + L_SC + 4096);
#pragma unroll
            for (int b = 0; b < 2; ++b) {
                red[((wave * 2 + b) * 256 + lane * 8) / 4] = (f32x4){a[0][b], a[1][b], a[2][b], a[3][b]};
                red[((wave * 2 + b) * 256 + lane * 8) / 4 + 1] = (f32x4){a[4][b], a[5][b], a[6][b], a[7][b]};
            }
        }
    }
    LGKM0();
    BAR();
    if (tid < 128) {
        const LAS f32x4* red = (const LAS f32x4*)(S + L_SC + 4096);
        f32x4 s = red[tid];
#pragma unroll
        for (int w = 1; w < 8; ++w) s += red[w * 128 + tid];
        const int b = tid >> 6, d = (tid & 63) * 4;
        v4u v; v[0] = canon(__float_as_uint(s[0])); v[1] = canon(__float_as_uint(s[1])); v[2] = canon(__float_as_uint(s[2])); v[3] = canon(__float_as_uint(s[3]));
        __builtin_amdgcn_raw_buffer_store_b128(v, rs_ws, (int)(G4_OFF + ((((size_t)hh * 16 + (d >> 4)) * 16 + cs) * 32 + b * 16 + (d & 15)) * 4), 0, 16);
        STAMP(6);
        v4u g;
        for (unsigned spins = 0;;) {
            g = __builtin_amdgcn_raw_buffer_load_b128(rs_ws, (int)(G4_OFF + (size_t)cu * 2048 + tid * 16), 0, 16);
            if (!__any(has_sent(g))) break;
            if (spin_fail(spins, tmo)) break;
        }
        f32x4 t; t[0] = __uint_as_float(g[0]); t[1] = __uint_as_float(g[1]); t[2] = __uint_as_float(g[2]); t[3] = __uint_as_float(g[3]);
#pragma unroll
        for (int o = 8; o < 64; o <<= 1) { t[0] += __shfl_xor(t[0], o); t[1] += __shfl_xor(t[1], o); t[2] += __shfl_xor(t[2], o); t[3] += __shfl_xor(t[3], o); }
        if (lane < 8) *(LAS f32x4*)(S + L_X6 + wave * 128 + lane * 16) = t;
        STAMP(7);
    }
    LGKM0();
    BAR();
    if (tid < 224) {
        const int j = tid >> 1, b = tid & 1;
        const LAS f32x4* x6 = (const LAS f32x4*)(S + L_X6);
        const LAS float* hm = (const LAS float*)(S + L_HM) + b * 8;
        f32x4 a = {0.f, 0.f, 0.f, 0.f};
#pragma unroll
        for (int q4 = 0; q4 < 4; ++q4) {
            const f32x4 cx = x6[b * 4 + q4] + x6[8 + b * 4 + q4];
#pragma unroll
            for (int k = 0; k < 4; ++k) {
                const v2u w = *(const LAS v2u*)(S + L_WO + (q4 * 4 + k) * (M * 2) + j * 8);
                const float c = cx[k];
                a[0] += c * bflo(w[0]); a[1] += c * bfhi(w[0]); a[2] += c * bflo(w[1]); a[3] += c * bfhi(w[1]);
            }
        }
#pragma unroll
        for (int r = 0; r < 7; ++r) {
            const v2u w = *(const LAS v2u*)(S + L_W2 + r * (M * 2) + j * 8);
            const float c = hm[r];
            a[0] += c * bflo(w[0]); a[1] += c * bfhi(w[0]); a[2] += c * bflo(w[1]); a[3] += c * bfhi(w[1]);
        }
        v4u v; v[0] = canon(__float_as_uint(a[0])); v[1] = canon(__float_as_uint(a[1])); v[2] = canon(__float_as_uint(a[2])); v[3] = canon(__float_as_uint(a[3]));
        __builtin_amdgcn_raw_buffer_store_b128(v, rs_ws, (int)(G5_OFF + (((size_t)j * 256 + cu) * 8 + b * 4) * 4), 0, 16);
    }
    STAMP(8);
    if (cu < NC5) {
        v4u v;
        for (unsigned spins = 0;;) {
            v = __builtin_amdgcn_raw_buffer_load_b128(rs_ws, (int)(G5_OFF + (size_t)cu * 8192 + tid * 16), 0, 16);
            if (!__any(has_sent(v))) break;
            if (spin_fail(spins, tmo)) break;
        }
        f32x4 s; s[0] = __uint_as_float(v[0]); s[1] = __uint_as_float(v[1]); s[2] = __uint_as_float(v[2]); s[3] = __uint_as_float(v[3]);
#pragma unroll
        for (int o = 2; o < 64; o <<= 1) { s[0] += __shfl_xor(s[0], o); s[1] += __shfl_xor(s[1], o); s[2] += __shfl_xor(s[2], o); s[3] += __shfl_xor(s[3], o); }
        LAS float* red = (LAS float*)(S + L_CC);
        if (lane < 2) { red[wave * 8 + lane * 4 + 0] = s[0]; red[wave * 8 + lane * 4 + 1] = s[1]; red[wave * 8 + lane * 4 + 2] = s[2]; red[wave * 8 + lane * 4 + 3] = s[3]; }
        LGKM0();
        BAR();
        if (tid < 8) {
            float t = 0.f;
#pragma unroll
            for (int w = 0; w < 8; ++w) t += red[w * 8 + tid];
            const int b = tid >> 2, m = cu * 4 + (tid & 3);
            t += bf2f(x[b * M + m]) + bf2f(b2[m]);
            out[b * M + m] = (unsigned short)f2bf(t);
        }
        STAMP(9);
    }
}
}

extern "C" void kernel_launch(void* const* d_in, const int* in_sizes, int n_in,
                              void* d_out, int out_size, void* d_ws, size_t ws_size,
                              hipStream_t stream) {
    const unsigned short* x = (const unsigned short*)d_in[0];
    const unsigned short* ckv = (const unsigned short*)d_in[1];
    const unsigned short* wq = (const unsigned short*)d_in[2];
    const unsigned short* wuk = (const unsigned short*)d_in[3];
    const unsigned short* wuv = (const unsigned short*)d_in[4];
    const unsigned short* wo = (const unsigned short*)d_in[5];
    const unsigned short* w1 = (const unsigned short*)d_in[7];
    const unsigned short* b1 = (const unsigned short*)d_in[8];
    const unsigned short* w2 = (const unsigned short*)d_in[9];
    const unsigned short* b2 = (const unsigned short*)d_in[10];
    unsigned char* ws = (unsigned char*)d_ws;
    k1<<<368, 256, 0, stream>>>(x, wq, w1, ws);
    k2<<<NCU, 512, 0, stream>>>(x, ckv, wuk, wuv, wo, b1, w2, b2, ws, (unsigned short*)d_out);
}
```

```cpp
#include <hip/hip_runtime.h>
#include <stdint.h>

#define GAS __attribute__((address_space(1)))
#define LAS __attribute__((address_space(3)))
typedef unsigned v4u __attribute__((ext_vector_type(4)));
typedef unsigned v2u __attribute__((ext_vector_type(2)));
typedef float f32x4 __attribute__((ext_vector_type(4)));
typedef float f32x2 __attribute__((ext_vector_type(2)));
typedef short bf16x8 __attribute__((ext_vector_type(8)));
typedef short s16x4 __attribute__((ext_vector_type(4)));
typedef GAS unsigned gu32;
typedef GAS unsigned long long gu64;
#define RLX_AGENT __ATOMIC_RELAXED, __HIP_MEMORY_SCOPE_AGENT

#define STAMPS 0
#define FILLMODE 0
#define S2LATE 0
#define PREF 0
#define PFSRC wuk
#define CODEPF 0
#define TOUCH 1
#define DLY 0
#define TLBW 0
#define WUKMAP 0
#define E4L 1
#define E2L 1
#define E3L 1
#define SLP 1
#define QREP 1
#define E2MODE 1
#define E3MODE 0

namespace {
constexpr int H = 16, SK = 4096, D = 256, M = 448, DC = 1024, F = 1792;
constexpr int NCU = 256;
constexpr int NSPLIT = 128, KPS = 32;
#define E5W 1
constexpr int NC5 = E5W == 2 ? 28 : E5W ? 56 : 112;

constexpr size_t FLG_OFF = 0;
constexpr size_t FLGS = E3L ? 128 : 64;
constexpr size_t TMO_OFF = 256 * 8 * FLGS;
constexpr size_t QABS_OFF = TMO_OFF + 4096;
constexpr size_t G4_OFF = QABS_OFF + 65536 * QREP;
constexpr size_t G4_BYTES = (size_t)256 * 512 * 4;
constexpr size_t G5_OFF = G4_OFF + G4_BYTES;
constexpr size_t G5_BYTES = (size_t)112 * 256 * 8 * 4;
constexpr size_t PCTX_OFF = G5_OFF + G5_BYTES;
constexpr size_t PCTX_BYTES = (size_t)2 * 128 * 16 * 1024 * 2;
constexpr size_t FILL_BYTES = E3MODE ? PCTX_OFF + PCTX_BYTES : PCTX_OFF;
constexpr size_t QP_OFF = PCTX_OFF + PCTX_BYTES;
constexpr size_t HP_OFF = QP_OFF + 4 * 2 * 4096 * 4;
constexpr size_t STAMP_OFF = HP_OFF + 65536;
constexpr size_t K1ST_OFF = STAMP_OFF + 256 * 16 * 8;
constexpr size_t WS_NEED = K1ST_OFF + 512 * 2 * 8;
static_assert(FILL_BYTES % 16 == 0, "fill");

constexpr int CKV_STRIDE = 2064;
constexpr int L_CKV = 0;
constexpr int LDS_DMA_BYTES = 32 * CKV_STRIDE;
constexpr int L_SC = 0;
constexpr int L_WO = L_SC + 20480;
constexpr int L_W2 = L_WO + 14336;
constexpr int L_QA = L_W2 + 7168;
constexpr int L_QS = L_QA + 2048;
constexpr int L_P = L_QS + 512;
constexpr int L_STAT = L_P + 1024;
constexpr int L_CC = L_STAT + 128;
constexpr int L_HM = L_CC + 512;
constexpr int L_X6 = L_HM + 128;
constexpr int L_ST = L_CKV;
constexpr int LDS_SC_BYTES = L_X6 + 256;
static_assert(LDS_DMA_BYTES + LDS_SC_BYTES <= 160 * 1024, "lds");

constexpr unsigned kSpinCap = 1u << 18;
constexpr unsigned SENT = 0xFFFFFFFFu;

__device__ __forceinline__ float bf2f(unsigned short u) { return __uint_as_float(((unsigned)u) << 16); }
__device__ __forceinline__ float bflo(unsigned u) { return __uint_as_float(u << 16); }
__device__ __forceinline__ float bfhi(unsigned u) { return __uint_as_float(u & 0xffff0000u); }
__device__ __forceinline__ unsigned f2bf(float f) { unsigned u = __float_as_uint(f); return (u + 0x7fffu + ((u >> 16) & 1u)) >> 16; }
__device__ __forceinline__ unsigned pack2(float a, float b) { return f2bf(a) | (f2bf(b) << 16); }
__device__ __forceinline__ float wsum(float v) { for (int o = 32; o > 0; o >>= 1) v += __shfl_xor(v, o); return v; }
__device__ __forceinline__ unsigned long long rt() { return __builtin_amdgcn_s_memrealtime(); }

__global__ void __launch_bounds__(512, 1) k2(const unsigned short* __restrict__ x, const unsigned short* __restrict__ ckv,
                                             const unsigned short* __restrict__ wuk, const unsigned short* __restrict__ wuv,
                                             const unsigned short* __restrict__ wo, const unsigned short* __restrict__ b1,
                                             const unsigned short* __restrict__ w2, const unsigned short* __restrict__ b2,
                                             unsigned char* __restrict__ ws, unsigned short* __restrict__ out);
__global__ void __launch_bounds__(256) k1(const unsigned short* __restrict__ x, const unsigned short* __restrict__ wq,
                                          const unsigned short* __restrict__ w1, unsigned char* __restrict__ ws, const unsigned short* __restrict__ pf,
                                          const unsigned char* __restrict__ t_ckv, const unsigned char* __restrict__ t_wuk, const unsigned char* __restrict__ t_wuv,
                                          const unsigned char* __restrict__ t_wo, const unsigned char* __restrict__ t_w2) {
    __shared__ float xn[2][M];
    __shared__ float red[4][2][64];
    const int tid = threadIdx.x, wave = tid >> 6, lane = tid & 63;
    const int blk = blockIdx.x;
    const bool isq = blk < 256;
    const int idx = isq ? blk : blk - 256;
    const int mq = idx & 3, cg = idx >> 2;
    const int ncols = isq ? 4096 : F;
    const unsigned short* W = (isq ? wq : w1) + (size_t)(mq * 112) * ncols + cg * 64;
    const int r = tid >> 3, cch = tid & 7;
    v4u wv[4];
#pragma unroll
    for (int i = 0; i < 4; ++i) {
        const int row = r + 32 * i;
        if (i < 3 || r < 16) wv[i] = __builtin_nontemporal_load((const v4u*)(W + (size_t)row * ncols + cch * 8));
        else wv[i] = (v4u){0u, 0u, 0u, 0u};
    }
    {
        const v4u sn = {SENT, SENT, SENT, SENT};
        v4u* g = (v4u*)ws;
        const unsigned tot16 = (unsigned)(FILL_BYTES / 16);
        for (unsigned i = blk * 256 + tid; i < tot16; i += gridDim.x * 256) g[i] = sn;
    }
    if (wave < 2) {
        float v[7]; float s = 0.f;
#pragma unroll
        for (int i = 0; i < 7; ++i) { v[i] = bf2f(x[wave * M + lane + 64 * i]); s += v[i]; }
        s = wsum(s); const float mean = s * (1.0f / M);
        float ss = 0.f;
#pragma unroll
        for (int i = 0; i < 7; ++i) { const float d = v[i] - mean; ss += d * d; }
        ss = wsum(ss);
        const float rs = 1.0f / sqrtf(ss * (1.0f / M) + 1e-5f);
#pragma unroll
        for (int i = 0; i < 7; ++i) xn[wave][lane + 64 * i] = (v[i] - mean) * rs;
    }
    __syncthreads();
    float a[2][8];
#pragma unroll
    for (int j = 0; j < 8; ++j) { a[0][j] = 0.f; a[1][j] = 0.f; }
#pragma unroll
    for (int i = 0; i < 4; ++i) {
        int row = mq * 112 + r + 32 * i; if (row >= M) row = M - 1;
        const float x0 = xn[0][row], x1 = xn[1][row];
#pragma unroll
        for (int j = 0; j < 4; ++j) {
            const float lo = bflo(wv[i][j]), hi = bfhi(wv[i][j]);
            a[0][2 * j] += x0 * lo; a[0][2 * j + 1] += x0 * hi;
            a[1][2 * j] += x1 * lo; a[1][2 * j + 1] += x1 * hi;
        }
    }
#pragma unroll
    for (int b = 0; b < 2; ++b)
#pragma unroll
        for (int j = 0; j < 8; ++j) { float v = a[b][j]; v += __shfl_xor(v, 8); v += __shfl_xor(v, 16); v += __shfl_xor(v, 32); a[b][j] = v; }
    if (lane < 8) {
#pragma unroll
        for (int b = 0; b < 2; ++b)
#pragma unroll
            for (int j = 0; j < 8; ++j) red[wave][b][lane * 8 + j] = a[b][j];
    }
    __syncthreads();
    if (tid < 128) {
        const int b = tid >> 6, c = tid & 63;
        const float s = red[0][b][c] + red[1][b][c] + red[2][b][c] + red[3][b][c];
        float* dst = (float*)(ws + (isq ? QP_OFF : HP_OFF));
        dst[((size_t)mq * 2 + b) * ncols + cg * 64 + c] = s;
    }
}

__device__ __forceinline__ bool spin_fail(unsigned& spins, gu32* tmo) {
    __builtin_amdgcn_s_sleep(SLP);
    if (++spins > kSpinCap) { __hip_atomic_store(tmo, 1u, RLX_AGENT); return true; }
    return (spins & 1023u) == 0u && __builtin_amdgcn_readfirstlane(__hip_atomic_load(tmo, RLX_AGENT)) == 1u;
}
__device__ __forceinline__ bool has_sent(v4u v) { return v[0] == SENT || v[1] == SENT || v[2] == SENT || v[3] == SENT; }
__device__ __forceinline__ unsigned canon(unsigned u) { return u == SENT ? 0x7FC00000u : u; }
__device__ __forceinline__ unsigned canonb(unsigned u) { return u == SENT ? 0xFFC0FFC0u : u; }

#define GLDS_NT(src, dst) __builtin_amdgcn_global_load_lds((const unsigned*)(src), (LAS unsigned*)(dst), 16, 0, 2)
#define BAR() __builtin_amdgcn_s_barrier()
#define LGKM0() asm volatile("s_waitcnt lgkmcnt(0)" ::: "memory")
#define VM0() asm volatile("s_waitcnt vmcnt(0)" ::: "memory")
#define PIN() __builtin_amdgcn_sched_barrier(0)
#define STAMP(i) do {} while (0)
#define STAMPW(i, w) do {} while (0)

__global__ void __launch_bounds__(512, 1) k2(const unsigned short* __restrict__ x, const unsigned short* __restrict__ ckv,
                                             const unsigned short* __restrict__ wuk, const unsigned short* __restrict__ wuv,
                                             const unsigned short* __restrict__ wo, const unsigned short* __restrict__ b1,
                                             const unsigned short* __restrict__ w2, const unsigned short* __restrict__ b2,
                                             unsigned char* __restrict__ ws, unsigned short* __restrict__ out) {
    __shared__ __attribute__((aligned(16))) unsigned char lds_dma[LDS_DMA_BYTES];
    __shared__ __attribute__((aligned(16))) unsigned char lds_sc[LDS_SC_BYTES];
    LAS unsigned char* L = (LAS unsigned char*)lds_dma;
    LAS unsigned char* S = (LAS unsigned char*)lds_sc;
    const int tid = threadIdx.x, wave = __builtin_amdgcn_readfirstlane(tid >> 6), lane = tid & 63;
    const int cu = blockIdx.x;
#define CSROT 0
    const int hh = cu >> 4, cs = (cu + CSROT) & 15;
    const int bb = cu >> 7, sp = cu & 127;
    gu32* tmo = (gu32*)(ws + TMO_OFF);
    const __amdgpu_buffer_rsrc_t rs_ws = __builtin_amdgcn_make_buffer_rsrc(ws, 0, (int)WS_NEED, 0x00020000);
    STAMP(0);

    float qp[4];
    {
        const float* p = (const float*)(ws + QP_OFF) + (size_t)(tid >> 8) * 4096 + hh * D + (tid & 255);
#pragma unroll
        for (int i = 0; i < 4; ++i) asm volatile("global_load_dword %0, %1, off" : "=v"(qp[i]) : "v"(p + (size_t)i * 8192));
    }
    v4u wk[8];
    {
        const unsigned short* src = ckv + ((size_t)bb * SK + (size_t)sp * KPS + 4 * wave) * DC;
        if (wave < 4) {
            const unsigned short* p = wuk + ((size_t)(64 * cs + 16 * wave + (lane & 15)) * H + hh) * D + 8 * (lane >> 4);
#pragma unroll
            for (int ks = 0; ks < 8; ++ks) asm volatile("global_load_dwordx4 %0, %1, off nt" : "=v"(wk[ks]) : "v"(p + 32 * ks));
        }
#pragma unroll
        for (int i = 0; i < 8; ++i) GLDS_NT(src + (size_t)i * 512 + lane * 8, L + L_CKV + (4 * wave + (i >> 1)) * CKV_STRIDE + (i & 1) * 1024);
    }
    PIN();
    if (wave < 4) asm volatile("s_waitcnt vmcnt(16)" ::: "memory");
    else asm volatile("s_waitcnt vmcnt(8)" ::: "memory");
    asm volatile("" : "+v"(qp[0]), "+v"(qp[1]), "+v"(qp[2]), "+v"(qp[3]));
    {
        float s = (qp[0] + qp[1]) + (qp[2] + qp[3]);
        s *= 0.0625f * 1.4426950408889634f;
        const unsigned hi = f2bf(s);
        const unsigned lo = f2bf(s - __uint_as_float(hi << 16));
        const int b = tid >> 8, d = tid & 255;
        ((LAS unsigned short*)(S + L_QA))[(2 * b) * 256 + d] = (unsigned short)hi;
        ((LAS unsigned short*)(S + L_QA))[(2 * b + 1) * 256 + d] = (unsigned short)lo;
    }
    LGKM0();
    BAR();
    STAMP(1);
    if (wave < 4) {
        v4u av[8];
#pragma unroll
        for (int ks = 0; ks < 8; ++ks) {
            av[ks] = *(const LAS v4u*)(S + L_QA + (lane & 3) * 512 + (32 * ks + 8 * (lane >> 4)) * 2);
        }
        const bool keep = (lane & 15) < 4;
        asm volatile("s_waitcnt vmcnt(8)" : "+v"(wk[0]), "+v"(wk[1]), "+v"(wk[2]), "+v"(wk[3]), "+v"(wk[4]), "+v"(wk[5]), "+v"(wk[6]), "+v"(wk[7]));
        f32x4 acc = {0.f, 0.f, 0.f, 0.f};
#pragma unroll
        for (int ks = 0; ks < 8; ++ks) {
            v4u a = av[ks];
            a[0] = keep ? a[0] : 0u; a[1] = keep ? a[1] : 0u; a[2] = keep ? a[2] : 0u; a[3] = keep ? a[3] : 0u;
            acc = __builtin_amdgcn_mfma_f32_16x16x32_bf16(__builtin_bit_cast(bf16x8, a), __builtin_bit_cast(bf16x8, wk[ks]), acc, 0, 0, 0);
        }
        LAS unsigned short* qs = (LAS unsigned short*)(S + L_QS) + wave * 16;
        if (lane < 16) {
            qs[lane] = (unsigned short)f2bf(acc[0] + acc[1]);
            qs[64 + lane] = (unsigned short)f2bf(acc[2] + acc[3]);
        }
        LGKM0();
    }
    BAR();
    if (tid < 16 * QREP) {
        v4u v = *(const LAS v4u*)(S + L_QS + (lane & 15) * 16);
        v[0] = canonb(v[0]); v[1] = canonb(v[1]); v[2] = canonb(v[2]); v[3] = canonb(v[3]);
        __builtin_amdgcn_raw_buffer_store_b128(v, rs_ws, (int)(QABS_OFF + (size_t)(tid >> 4) * 65536 + (((size_t)((lane >> 3) & 1) * H + hh) * DC + 64 * cs + 8 * (lane & 7)) * 2), 0, 16);
    } else if (wave == 4 && lane < 14) {
        const int b = lane / 7, r = lane % 7, f = cu * 7 + r;
        const float* hp = (const float*)(ws + HP_OFF);
        float hv = bf2f(b1[f]);
#pragma unroll
        for (int p = 0; p < 4; ++p) hv += hp[((size_t)p * 2 + b) * F + f];
        const float t = tanhf(0.7978845608028654f * (hv + 0.044715f * hv * hv * hv));
        ((LAS float*)(S + L_HM))[b * 8 + r] = 0.5f * hv * (1.0f + t);
    }
    STAMP(2);
    v4u wuvv[4], wov[2], w2v;
    PIN();
    {
#pragma unroll
        for (int i = 0; i < 4; ++i) {
            const int c = 64 * cs + 8 * wave + 2 * i + (lane >> 5);
            wuvv[i] = __builtin_nontemporal_load((const v4u*)(wuv + ((size_t)c * H + hh) * D + (lane & 31) * 8));
        }
        const unsigned short* so = wo + ((size_t)hh * D + cs * 16) * M;
        const int t2 = tid < 448 ? tid : 447;
        wov[0] = __builtin_nontemporal_load((const v4u*)(so + (size_t)t2 * 8));
        wov[1] = __builtin_nontemporal_load((const v4u*)(so + (size_t)(448 + t2) * 8));
        w2v = __builtin_nontemporal_load((const v4u*)(w2 + (size_t)cu * 7 * M + (size_t)(tid < 392 ? tid : 391) * 8));
    }
    PIN();
    float xb = 0.f;
    {
        const int b = (tid >> 3) & 1, m = (cu < NC5 ? cu : 0) * 8 + (tid & 7);
        xb = bf2f(x[b * M + m]) + bf2f(b2[m]);
    }
    const int qbase = (int)(QABS_OFF + (size_t)((cu >> 3) % QREP) * 65536 + (((size_t)bb * H + (lane & 15)) * DC + 128 * wave) * 2);
    bf16x8 qa[4];
    {
        v4u v[4];
#pragma unroll
        for (int i = 0; i < 4; ++i) v[i] = (v4u){SENT, SENT, SENT, SENT};
        for (unsigned spins = 0;;) {
#pragma unroll
            for (int i = 0; i < 4; ++i)
                if (has_sent(v[i])) v[i] = __builtin_amdgcn_raw_buffer_load_b128(rs_ws, qbase + (32 * i + 8 * (lane >> 4)) * 2, 0, 16);
            bool bad = false;
#pragma unroll
            for (int i = 0; i < 4; ++i) bad |= has_sent(v[i]);
            if (!__any(bad)) break;
            if (spin_fail(spins, tmo)) break;
        }
#pragma unroll
        for (int i = 0; i < 4; ++i) qa[i] = __builtin_bit_cast(bf16x8, v[i]);
    }
    VM0();
    asm volatile("" : "+v"(xb));
    STAMP(3);
    STAMPW(14, 7);
    BAR();
    STAMP(10);
    {
        f32x4 acc[2] = {{0.f, 0.f, 0.f, 0.f}, {0.f, 0.f, 0.f, 0.f}};
#pragma unroll
        for (int nt = 0; nt < 2; ++nt)
#pragma unroll
            for (int i = 0; i < 4; ++i) {
                const v4u v = *(const LAS v4u*)(L + L_CKV + (16 * nt + (lane & 15)) * CKV_STRIDE + (128 * wave + 32 * i + 8 * (lane >> 4)) * 2);
                acc[nt] = __builtin_amdgcn_mfma_f32_16x16x32_bf16(qa[i], __builtin_bit_cast(bf16x8, v), acc[nt], 0, 0, 0);
            }
        LAS float* sc = (LAS float*)(S + L_SC);
#pragma unroll
        for (int nt = 0; nt < 2; ++nt)
#pragma unroll
            for (int r = 0; r < 4; ++r) sc[(wave * 16 + 4 * (lane >> 4) + r) * 32 + 16 * nt + (lane & 15)] = acc[nt][r];
    }
    LGKM0();
    BAR();
    float st_m, st_l;
    {
        const int h = tid >> 5, key = tid & 31;
        const LAS float* sc = (const LAS float*)(S + L_SC);
        float s = 0.f;
#pragma unroll
        for (int w = 0; w < 8; ++w) s += sc[(w * 16 + h) * 32 + key];
        float mx = s;
#pragma unroll
        for (int o = 1; o < 32; o <<= 1) mx = fmaxf(mx, __shfl_xor(mx, o));
        const unsigned pb = f2bf(__builtin_amdgcn_exp2f(s - mx));
        float l = __uint_as_float(pb << 16);
#pragma unroll
        for (int o = 1; o < 32; o <<= 1) l += __shfl_xor(l, o);
        ((LAS unsigned short*)(S + L_P))[h * 32 + key] = (unsigned short)pb;
        st_m = mx; st_l = l;
    }
    LGKM0();
    BAR();
    {
        const bf16x8 pf = __builtin_bit_cast(bf16x8, *(const LAS v4u*)(S + L_P + (lane & 15) * 64 + (lane >> 4) * 16));
        const int g = lane >> 4, q = (lane & 15) >> 2, p = lane & 3;
        v2u o2[8];
#pragma unroll
        for (int t = 0; t < 8; ++t) {
            const int c0 = 128 * wave + 16 * t;
            const LAS s16x4* a0 = (const LAS s16x4*)(L + L_CKV + (8 * g + q) * CKV_STRIDE + c0 * 2 + 8 * p);
            const LAS s16x4* a1 = (const LAS s16x4*)(L + L_CKV + (8 * g + 4 + q) * CKV_STRIDE + c0 * 2 + 8 * p);
            const s16x4 lo = __builtin_amdgcn_ds_read_tr16_b64_v4i16((LAS s16x4*)a0);
            const s16x4 hi = __builtin_amdgcn_ds_read_tr16_b64_v4i16((LAS s16x4*)a1);
            bf16x8 af;
            af[0] = lo[0]; af[1] = lo[1]; af[2] = lo[2]; af[3] = lo[3]; af[4] = hi[0]; af[5] = hi[1]; af[6] = hi[2]; af[7] = hi[3];
            f32x4 o = {0.f, 0.f, 0.f, 0.f};
            o = __builtin_amdgcn_mfma_f32_16x16x32_bf16(af, pf, o, 0, 0, 0);
            o2[t][0] = pack2(o[0], o[1]); o2[t][1] = pack2(o[2], o[3]);
        }
        LGKM0();
#pragma unroll
        for (int t = 0; t < 8; ++t) *(LAS v2u*)(L + L_ST + (lane & 15) * CKV_STRIDE + (128 * wave + 16 * t + 4 * g) * 2) = o2[t];
    }
    LGKM0();
    BAR();
    {
#pragma unroll
        for (int i = 0; i < 4; ++i) {
            const int h = 2 * wave + (i >> 1);
            v4u v = *(const LAS v4u*)(L + L_ST + h * CKV_STRIDE + (i & 1) * 1024 + lane * 16);
            __builtin_amdgcn_raw_buffer_store_b128(v, rs_ws, (int)(PCTX_OFF + ((((size_t)bb * NSPLIT + sp) * H + h) * DC) * 2 + (i & 1) * 1024 + lane * 16), 0, 16);
        }
        v4u fv;
        fv[0] = canon(__float_as_uint(st_m)); fv[1] = canon(__float_as_uint(st_l));
        fv[2] = canon(__float_as_uint(__shfl(st_m, 32))); fv[3] = canon(__float_as_uint(__shfl(st_l, 32)));
        if (lane != 0) fv = (v4u){0u, 0u, 0u, 0u};
        VM0();
        if (lane < FLGS / 16) __builtin_amdgcn_raw_buffer_store_b128(fv, rs_ws, (int)(FLG_OFF + ((size_t)cu * 8 + wave) * FLGS + lane * 16), 0, 16);
    }
    STAMP(4);
    if (tid < 448) { *(LAS v4u*)(S + L_WO + tid * 16) = wov[0]; *(LAS v4u*)(S + L_WO + (448 + tid) * 16) = wov[1]; }
    if (tid < 392) *(LAS v4u*)(S + L_W2 + tid * 16) = w2v;
    {
        const int j = lane >> 3, ch = lane & 7;
        v4u pc[4]; v2u ml[4];
        for (unsigned spins = 0;;) {
            bool bad = false;
#pragma unroll
            for (int i = 0; i < 4; ++i) {
                const int b = i >> 1, s = 16 * wave + (i & 1) * 8 + j;
                ml[i] = __builtin_amdgcn_raw_buffer_load_b64(rs_ws, (int)(FLG_OFF + (((size_t)b * 128 + s) * 8 + (hh >> 1)) * FLGS + (hh & 1) * 8), 0, 16);
            }
#pragma unroll
            for (int i = 0; i < 4; ++i) bad |= (ml[i][0] == SENT) || (ml[i][1] == SENT);
            if (!__any(bad)) break;
            if (spin_fail(spins, tmo)) break;
        }
        STAMP(5);
#pragma unroll
        for (int i = 0; i < 4; ++i) {
            const int b = i >> 1, s = 16 * wave + (i & 1) * 8 + j;
            pc[i] = __builtin_amdgcn_raw_buffer_load_b128(rs_ws, (int)(PCTX_OFF + ((((size_t)b * NSPLIT + s) * H + hh) * DC + 64 * cs + ch * 8) * 2), 0, 16);
        }
        LAS float* red = (LAS float*)(S + L_SC);
#pragma unroll
        for (int b = 0; b < 2; ++b) {
            const float m0 = __uint_as_float(ml[2 * b][0]), l0 = __uint_as_float(ml[2 * b][1]);
            const float m1 = __uint_as_float(ml[2 * b + 1][0]), l1 = __uint_as_float(ml[2 * b + 1][1]);
            float gm = fmaxf(m0, m1);
            gm = fmaxf(gm, __shfl_xor(gm, 8)); gm = fmaxf(gm, __shfl_xor(gm, 16)); gm = fmaxf(gm, __shfl_xor(gm, 32));
            const float e0 = __builtin_amdgcn_exp2f(m0 - gm), e1 = __builtin_amdgcn_exp2f(m1 - gm);
            float ls = e0 * l0 + e1 * l1;
            ls += __shfl_xor(ls, 8); ls += __shfl_xor(ls, 16); ls += __shfl_xor(ls, 32);
            float acc[8];
#pragma unroll
            for (int k = 0; k < 4; ++k) {
                acc[2 * k] = e0 * bflo(pc[2 * b][k]) + e1 * bflo(pc[2 * b + 1][k]);
                acc[2 * k + 1] = e0 * bfhi(pc[2 * b][k]) + e1 * bfhi(pc[2 * b + 1][k]);
            }
#pragma unroll
            for (int k = 0; k < 8; ++k) { float v = acc[k]; v += __shfl_xor(v, 8); v += __shfl_xor(v, 16); v += __shfl_xor(v, 32); acc[k] = v; }
            if (lane < 8) {
                *(LAS f32x4*)(red + (wave * 2 + b) * 64 + lane * 8) = (f32x4){acc[0], acc[1], acc[2], acc[3]};
                *(LAS f32x4*)(red + (wave * 2 + b) * 64 + lane * 8 + 4) = (f32x4){acc[4], acc[5], acc[6], acc[7]};
            }
            if (lane == 0) ((LAS f32x2*)(S + L_STAT))[wave * 2 + b] = (f32x2){gm, ls};
        }
    }
    STAMP(11);
    LGKM0();
    BAR();
    STAMP(12);
    if (tid < 128) {
        const int b = tid >> 6;
        const LAS float* red = (const LAS float*)(S + L_SC);
        const LAS f32x2* st = (const LAS f32x2*)(S + L_STAT);
        float gm = st[b][0];
#pragma unroll
        for (int w = 1; w < 8; ++w) gm = fmaxf(gm, st[w * 2 + b][0]);
        float s = 0.f, ls = 0.f;
#pragma unroll
        for (int w = 0; w < 8; ++w) {
            const f32x2 t = st[w * 2 + b];
            const float e = __builtin_amdgcn_exp2f(t[0] - gm);
            ls += e * t[1]; s += e * red[w * 128 + tid];
        }
        ((LAS float*)(S + L_CC))[tid] = s / ls;
    }
    LGKM0();
    BAR();
    {
        const LAS float* cc = (const LAS float*)(S + L_CC);
        f32x2 a[8];
#pragma unroll
        for (int j = 0; j < 8; ++j) a[j] = (f32x2){0.f, 0.f};
#pragma unroll
        for (int i = 0; i < 4; ++i) {
            const int c = 8 * wave + 2 * i + (lane >> 5);
            const v4u w = wuvv[i];
            const f32x2 cv = {cc[c], cc[64 + c]};
#pragma unroll
            for (int j = 0; j < 4; ++j) { a[2 * j] += cv * bflo(w[j]); a[2 * j + 1] += cv * bfhi(w[j]); }
        }
#pragma unroll
        for (int j = 0; j < 8; ++j) { a[j][0] += __shfl_xor(a[j][0], 32); a[j][1] += __shfl_xor(a[j][1], 32); }
        if (lane < 32) {
            LAS f32x4* red = (LAS f32x4*)(S + L_SC + 4096);
#pragma unroll
            for (int b = 0; b < 2; ++b) {
                red[((wave * 2 + b) * 256 + lane * 8) / 4] = (f32x4){a[0][b], a[1][b], a[2][b], a[3][b]};
                red[((wave * 2 + b) * 256 + lane * 8) / 4 + 1] = (f32x4){a[4][b], a[5][b], a[6][b], a[7][b]};
            }
        }
    }
    LGKM0();
    BAR();
    if (tid < 128) {
        const LAS f32x4* red = (const LAS f32x4*)(S + L_SC + 4096);
        const int b = (tid >> 2) & 1, d = (tid >> 3) * 16 + (tid & 3) * 4;
        const int ridx = b * 64 + (d >> 2);
        f32x4 s = red[ridx];
#pragma unroll
        for (int w = 1; w < 8; ++w) s += red[w * 128 + ridx];
        v4u v; v[0] = canon(__float_as_uint(s[0])); v[1] = canon(__float_as_uint(s[1])); v[2] = canon(__float_as_uint(s[2])); v[3] = canon(__float_as_uint(s[3]));
        __builtin_amdgcn_raw_buffer_store_b128(v, rs_ws, (int)(G4_OFF + ((((size_t)hh * 16 + (d >> 4)) * 16 + cs) * 32 + b * 16 + (d & 15)) * 4), 0, 16);
        STAMP(6);
        v4u g;
        for (unsigned spins = 0;;) {
            g = __builtin_amdgcn_raw_buffer_load_b128(rs_ws, (int)(G4_OFF + (size_t)(hh * 16 + cs) * 2048 + tid * 16), 0, 16);
            if (!__any(has_sent(g))) break;
            if (spin_fail(spins, tmo)) break;
        }
        f32x4 t; t[0] = __uint_as_float(g[0]); t[1] = __uint_as_float(g[1]); t[2] = __uint_as_float(g[2]); t[3] = __uint_as_float(g[3]);
#pragma unroll
        for (int o = 8; o < 64; o <<= 1) { t[0] += __shfl_xor(t[0], o); t[1] += __shfl_xor(t[1], o); t[2] += __shfl_xor(t[2], o); t[3] += __shfl_xor(t[3], o); }
        if (lane < 8) *(LAS f32x4*)(S + L_X6 + wave * 128 + lane * 16) = t;
        STAMP(7);
    }
    LGKM0();
    BAR();
    if (tid < 224) {
        const int j = tid >> 1, b = tid & 1;
        const LAS f32x4* x6 = (const LAS f32x4*)(S + L_X6);
        const LAS float* hm = (const LAS float*)(S + L_HM) + b * 8;
        f32x4 a = {0.f, 0.f, 0.f, 0.f};
#pragma unroll
        for (int q4 = 0; q4 < 4; ++q4) {
            const f32x4 cx = x6[b * 4 + q4] + x6[8 + b * 4 + q4];
#pragma unroll
            for (int k = 0; k < 4; ++k) {
                const v2u w = *(const LAS v2u*)(S + L_WO + (q4 * 4 + k) * (M * 2) + j * 8);
                const float c = cx[k];
                a[0] += c * bflo(w[0]); a[1] += c * bfhi(w[0]); a[2] += c * bflo(w[1]); a[3] += c * bfhi(w[1]);
            }
        }
#pragma unroll
        for (int r = 0; r < 7; ++r) {
            const v2u w = *(const LAS v2u*)(S + L_W2 + r * (M * 2) + j * 8);
            const float c = hm[r];
            a[0] += c * bflo(w[0]); a[1] += c * bfhi(w[0]); a[2] += c * bflo(w[1]); a[3] += c * bfhi(w[1]);
        }
        v4u v; v[0] = canon(__float_as_uint(a[0])); v[1] = canon(__float_as_uint(a[1])); v[2] = canon(__float_as_uint(a[2])); v[3] = canon(__float_as_uint(a[3]));
        __builtin_amdgcn_raw_buffer_store_b128(v, rs_ws, (int)(G5_OFF + (((size_t)(j >> 1) * 256 + cu) * 16 + b * 8 + (j & 1) * 4) * 4), 0, 16);
    }
    STAMP(8);
    if (cu < NC5) {
        v4u v0 = {SENT, SENT, SENT, SENT}, v1 = {SENT, SENT, SENT, SENT};
        for (unsigned spins = 0;;) {
            if (has_sent(v0)) v0 = __builtin_amdgcn_raw_buffer_load_b128(rs_ws, (int)(G5_OFF + (size_t)cu * 16384 + tid * 16), 0, 16);
            if (has_sent(v1)) v1 = __builtin_amdgcn_raw_buffer_load_b128(rs_ws, (int)(G5_OFF + (size_t)cu * 16384 + 8192 + tid * 16), 0, 16);
            if (!__any(has_sent(v0) || has_sent(v1))) break;
            if (spin_fail(spins, tmo)) break;
        }
        f32x4 s; s[0] = __uint_as_float(v0[0]) + __uint_as_float(v1[0]); s[1] = __uint_as_float(v0[1]) + __uint_as_float(v1[1]);
        s[2] = __uint_as_float(v0[2]) + __uint_as_float(v1[2]); s[3] = __uint_as_float(v0[3]) + __uint_as_float(v1[3]);
#pragma unroll
        for (int o = 4; o < 64; o <<= 1) { s[0] += __shfl_xor(s[0], o); s[1] += __shfl_xor(s[1], o); s[2] += __shfl_xor(s[2], o); s[3] += __shfl_xor(s[3], o); }
        LAS float* red = (LAS float*)(S + L_CC);
        if (lane < 4) *(LAS f32x4*)(red + wave * 16 + lane * 4) = s;
        LGKM0();
        BAR();
        if (tid < 16) {
            float t = 0.f;
#pragma unroll
            for (int w = 0; w < 8; ++w) t += red[w * 16 + tid];
            const int b = tid >> 3, m = cu * 8 + (tid & 7);
            t += xb;
            out[b * M + m] = (unsigned short)f2bf(t);
        }
        STAMP(9);
    }
}
}

extern "C" void kernel_launch(void* const* d_in, const int* in_sizes, int n_in,
                              void* d_out, int out_size, void* d_ws, size_t ws_size,
                              hipStream_t stream) {
    const unsigned short* x = (const unsigned short*)d_in[0];
    const unsigned short* ckv = (const unsigned short*)d_in[1];
    const unsigned short* wq = (const unsigned short*)d_in[2];
    const unsigned short* wuk = (const unsigned short*)d_in[3];
    const unsigned short* wuv = (const unsigned short*)d_in[4];
    const unsigned short* wo = (const unsigned short*)d_in[5];
    const unsigned short* w1 = (const unsigned short*)d_in[7];
    const unsigned short* b1 = (const unsigned short*)d_in[8];
    const unsigned short* w2 = (const unsigned short*)d_in[9];
    const unsigned short* b2 = (const unsigned short*)d_in[10];
    unsigned char* ws = (unsigned char*)d_ws;
    k1<<<368, 256, 0, stream>>>(x, wq, w1, ws, PFSRC, (const unsigned char*)ckv, (const unsigned char*)wuk, (const unsigned char*)wuv, (const unsigned char*)wo, (const unsigned char*)w2);
    k2<<<NCU, 512, 0, stream>>>(x, ckv, wuk, wuv, wo, b1, w2, b2, ws, (unsigned short*)d_out);
}
```

```cpp
#include <hip/hip_runtime.h>
#include <stdint.h>

#define GAS __attribute__((address_space(1)))
#define LAS __attribute__((address_space(3)))
typedef unsigned v4u __attribute__((ext_vector_type(4)));
typedef unsigned v2u __attribute__((ext_vector_type(2)));
typedef float f32x4 __attribute__((ext_vector_type(4)));
typedef float f32x2 __attribute__((ext_vector_type(2)));
typedef short bf16x8 __attribute__((ext_vector_type(8)));
typedef short s16x4 __attribute__((ext_vector_type(4)));
typedef GAS unsigned gu32;
typedef GAS unsigned long long gu64;
#define RLX_AGENT __ATOMIC_RELAXED, __HIP_MEMORY_SCOPE_AGENT

#define STAMPS 0
#define FILLMODE 0
#define S2LATE 1
#define PREF 0
#define PFSRC wuk
#define CODEPF 0
#define TOUCH 1
#define DLY 0
#define TLBW 0
#define WUKMAP 0
#define E4L 1
#define E2L 1
#define E3L 1
#define SLP 1
#define QREP 1
#define QBAR 0
#define K1FIX 1
#define CPF2 0
#define PIN() __builtin_amdgcn_sched_barrier(0)
#define ATTST 0
#define E2MODE 1
#define E3MODE 0

namespace {
constexpr int H = 16, SK = 4096, D = 256, M = 448, DC = 1024, F = 1792;
constexpr int NCU = 256;
constexpr int NSPLIT = 128, KPS = 32;
#define E5W 1
constexpr int NC5 = E5W == 2 ? 28 : E5W ? 56 : 112;

constexpr size_t FLG_OFF = 0;
constexpr size_t FLGS = E3L ? 128 : 64;
constexpr size_t TMO_OFF = 256 * 8 * FLGS;
constexpr size_t QABS_OFF = TMO_OFF + 4096;
constexpr size_t G4_OFF = QABS_OFF + 65536 * QREP;
constexpr size_t G4_BYTES = (size_t)256 * 512 * 4;
constexpr size_t G5_OFF = G4_OFF + G4_BYTES;
constexpr size_t G5_BYTES = (size_t)112 * 256 * 8 * 4;
constexpr size_t PCTX_OFF = G5_OFF + G5_BYTES;
constexpr size_t PCTX_BYTES = (size_t)2 * 128 * 16 * 1024 * 2;
constexpr size_t FILL_BYTES = E3MODE ? PCTX_OFF + PCTX_BYTES : PCTX_OFF;
constexpr size_t QP_OFF = PCTX_OFF + PCTX_BYTES;
constexpr size_t HP_OFF = QP_OFF + 4 * 2 * 4096 * 4;
constexpr size_t STAMP_OFF = HP_OFF + 65536;
constexpr size_t K1ST_OFF = STAMP_OFF + 256 * 16 * 8;
constexpr size_t WS_NEED = K1ST_OFF + 512 * 2 * 8;
static_assert(FILL_BYTES % 16 == 0, "fill");

constexpr int CKV_STRIDE = 2064;
constexpr int L_CKV = 0;
constexpr int LDS_DMA_BYTES = 32 * CKV_STRIDE;
constexpr int L_SC = 0;
constexpr int L_WO = L_SC + 20480;
constexpr int L_W2 = L_WO + 14336;
constexpr int L_QA = L_W2 + 7168;
constexpr int L_QS = L_QA + 2048;
constexpr int L_P = L_QS + 512;
constexpr int L_STAT = L_P + 1024;
constexpr int L_CC = L_STAT + 128;
constexpr int L_HM = L_CC + 512;
constexpr int L_X6 = L_HM + 128;
constexpr int L_ST = L_CKV;
constexpr int LDS_SC_BYTES = L_X6 + 256;
static_assert(LDS_DMA_BYTES + LDS_SC_BYTES <= 160 * 1024, "lds");

constexpr unsigned kSpinCap = 1u << 18;
constexpr unsigned SENT = 0xFFFFFFFFu;

__device__ __forceinline__ float bf2f(unsigned short u) { return __uint_as_float(((unsigned)u) << 16); }
__device__ __forceinline__ float bflo(unsigned u) { return __uint_as_float(u << 16); }
__device__ __forceinline__ float bfhi(unsigned u) { return __uint_as_float(u & 0xffff0000u); }
__device__ __forceinline__ unsigned f2bf(float f) { unsigned u = __float_as_uint(f); return (u + 0x7fffu + ((u >> 16) & 1u)) >> 16; }
__device__ __forceinline__ unsigned pack2(float a, float b) { return f2bf(a) | (f2bf(b) << 16); }
template <int C> __device__ __forceinline__ float dppf(float v) { return __int_as_float(__builtin_amdgcn_update_dpp(0, __float_as_int(v), C, 0xF, 0xF, true)); }
__device__ __forceinline__ float add16(float v) { auto r = __builtin_amdgcn_permlane16_swap(__float_as_uint(v), __float_as_uint(v), false, false); return __uint_as_float(r[0]) + __uint_as_float(r[1]); }
__device__ __forceinline__ float add32(float v) { auto r = __builtin_amdgcn_permlane32_swap(__float_as_uint(v), __float_as_uint(v), false, false); return __uint_as_float(r[0]) + __uint_as_float(r[1]); }
__device__ __forceinline__ float max16(float v) { auto r = __builtin_amdgcn_permlane16_swap(__float_as_uint(v), __float_as_uint(v), false, false); return fmaxf(__uint_as_float(r[0]), __uint_as_float(r[1])); }
__device__ __forceinline__ float max32(float v) { auto r = __builtin_amdgcn_permlane32_swap(__float_as_uint(v), __float_as_uint(v), false, false); return fmaxf(__uint_as_float(r[0]), __uint_as_float(r[1])); }
__device__ __forceinline__ float add_hi3(float v) { v += dppf<0x128>(v); v = add16(v); return add32(v); }
__device__ __forceinline__ float max_hi3(float v) { v = fmaxf(v, dppf<0x128>(v)); v = max16(v); return max32(v); }
__device__ __forceinline__ float add_lo5(float v) { v += dppf<0xB1>(v); v += dppf<0x4E>(v); v += dppf<0x141>(v); v += dppf<0x140>(v); return add16(v); }
__device__ __forceinline__ float max_lo5(float v) { v = fmaxf(v, dppf<0xB1>(v)); v = fmaxf(v, dppf<0x4E>(v)); v = fmaxf(v, dppf<0x141>(v)); v = fmaxf(v, dppf<0x140>(v)); return max16(v); }
__device__ __forceinline__ float wsum(float v) { return add32(add_lo5(v)); }
__device__ __forceinline__ unsigned long long rt() { return __builtin_amdgcn_s_memrealtime(); }

__global__ void __launch_bounds__(512, 1) k2(const unsigned short* __restrict__ x, const unsigned short* __restrict__ ckv,
                                             const unsigned short* __restrict__ wuk, const unsigned short* __restrict__ wuv,
                                             const unsigned short* __restrict__ wo, const unsigned short* __restrict__ b1,
                                             const unsigned short* __restrict__ w2, const unsigned short* __restrict__ b2,
                                             unsigned char* __restrict__ ws, unsigned short* __restrict__ out);
__global__ void __launch_bounds__(256) k1(const unsigned short* __restrict__ x, const unsigned short* __restrict__ wq,
                                          const unsigned short* __restrict__ w1, unsigned char* __restrict__ ws, const unsigned short* __restrict__ pf,
                                          const unsigned char* __restrict__ t_ckv, const unsigned char* __restrict__ t_wuk, const unsigned char* __restrict__ t_wuv,
                                          const unsigned char* __restrict__ t_wo, const unsigned char* __restrict__ t_w2) {
    __shared__ float xn[2][M];
    __shared__ float red[4][2][64];
    const int tid = threadIdx.x, wave = tid >> 6, lane = tid & 63;
    const int blk = blockIdx.x;
    const bool isq = blk < 256;
    const int idx = isq ? blk : blk - 256;
    const int mq = idx & 3, cg = idx >> 2;
    const int ncols = isq ? 4096 : F;
    const unsigned short* W = (isq ? wq : w1) + (size_t)(mq * 112) * ncols + cg * 64;
    const int r = tid >> 3, cch = tid & 7;
    unsigned short xr[7];
#pragma unroll
    for (int i = 0; i < 7; ++i) xr[i] = x[(wave & 1) * M + lane + 64 * i];
    PIN();
    v4u wv[4];
#pragma unroll
    for (int i = 0; i < 4; ++i) {
        const int row = (i < 3 || r < 16) ? r + 32 * i : 111;
        wv[i] = __builtin_nontemporal_load((const v4u*)(W + (size_t)row * ncols + cch * 8));
    }
    PIN();
    {
        const v4u sn = {SENT, SENT, SENT, SENT};
        v4u* g = (v4u*)ws;
        const unsigned tot16 = (unsigned)(FILL_BYTES / 16);
        for (unsigned i = blk * 256 + tid; i < tot16; i += gridDim.x * 256) g[i] = sn;
    }
    if (wave < 2) {
        float v[7]; float s = 0.f;
#pragma unroll
        for (int i = 0; i < 7; ++i) { v[i] = bf2f(xr[i]); s += v[i]; }
        s = wsum(s); const float mean = s * (1.0f / M);
        float ss = 0.f;
#pragma unroll
        for (int i = 0; i < 7; ++i) { const float d = v[i] - mean; ss += d * d; }
        ss = wsum(ss);
        const float rs = 1.0f / sqrtf(ss * (1.0f / M) + 1e-5f);
#pragma unroll
        for (int i = 0; i < 7; ++i) xn[wave][lane + 64 * i] = (v[i] - mean) * rs;
    }
    __syncthreads();
    float a[2][8];
#pragma unroll
    for (int j = 0; j < 8; ++j) { a[0][j] = 0.f; a[1][j] = 0.f; }
#pragma unroll
    for (int i = 0; i < 4; ++i) {
        int row = mq * 112 + r + 32 * i; if (row >= M) row = M - 1;
        const bool ok = (i < 3 || r < 16);
        const float x0 = ok ? xn[0][row] : 0.f, x1 = ok ? xn[1][row] : 0.f;
#pragma unroll
        for (int j = 0; j < 4; ++j) {
            const float lo = bflo(wv[i][j]), hi = bfhi(wv[i][j]);
            a[0][2 * j] += x0 * lo; a[0][2 * j + 1] += x0 * hi;
            a[1][2 * j] += x1 * lo; a[1][2 * j + 1] += x1 * hi;
        }
    }
#pragma unroll
    for (int b = 0; b < 2; ++b)
#pragma unroll
        for (int j = 0; j < 8; ++j) a[b][j] = add_hi3(a[b][j]);
    if (lane < 8) {
#pragma unroll
        for (int b = 0; b < 2; ++b)
#pragma unroll
            for (int j = 0; j < 8; ++j) red[wave][b][lane * 8 + j] = a[b][j];
    }
    __syncthreads();
    if (tid < 128) {
        const int b = tid >> 6, c = tid & 63;
        const float s = red[0][b][c] + red[1][b][c] + red[2][b][c] + red[3][b][c];
        float* dst = (float*)(ws + (isq ? QP_OFF : HP_OFF));
        dst[((size_t)mq * 2 + b) * ncols + cg * 64 + c] = s;
    }
}

__device__ __forceinline__ bool spin_fail(unsigned& spins, gu32* tmo) {
    __builtin_amdgcn_s_sleep(SLP);
    if (++spins > kSpinCap) { __hip_atomic_store(tmo, 1u, RLX_AGENT); return true; }
    return (spins & 1023u) == 0u && __builtin_amdgcn_readfirstlane(__hip_atomic_load(tmo, RLX_AGENT)) == 1u;
}
__device__ __forceinline__ bool has_sent(v4u v) { return v[0] == SENT || v[1] == SENT || v[2] == SENT || v[3] == SENT; }
__device__ __forceinline__ unsigned canon(unsigned u) { return u == SENT ? 0x7FC00000u : u; }
__device__ __forceinline__ unsigned canonb(unsigned u) { return u == SENT ? 0xFFC0FFC0u : u; }

#define GLDS_NT(src, dst) __builtin_amdgcn_global_load_lds((const unsigned*)(src), (LAS unsigned*)(dst), 16, 0, 2)
#define BAR() __builtin_amdgcn_s_barrier()
#define LGKM0() asm volatile("s_waitcnt lgkmcnt(0)" ::: "memory")
#define VM0() asm volatile("s_waitcnt vmcnt(0)" ::: "memory")
#define STAMP(i) do {} while (0)
#define STAMPW(i, w) do {} while (0)

__global__ void __launch_bounds__(512, 1) k2(const unsigned short* __restrict__ x, const unsigned short* __restrict__ ckv,
                                             const unsigned short* __restrict__ wuk, const unsigned short* __restrict__ wuv,
                                             const unsigned short* __restrict__ wo, const unsigned short* __restrict__ b1,
                                             const unsigned short* __restrict__ w2, const unsigned short* __restrict__ b2,
                                             unsigned char* __restrict__ ws, unsigned short* __restrict__ out) {
    __shared__ __attribute__((aligned(16))) unsigned char lds_dma[LDS_DMA_BYTES];
    __shared__ __attribute__((aligned(16))) unsigned char lds_sc[LDS_SC_BYTES];
    LAS unsigned char* L = (LAS unsigned char*)lds_dma;
    LAS unsigned char* S = (LAS unsigned char*)lds_sc;
    const int tid = threadIdx.x, wave = __builtin_amdgcn_readfirstlane(tid >> 6), lane = tid & 63;
    const int cu = blockIdx.x;
#define CSROT 0
    const int hh = cu >> 4, cs = (cu + CSROT) & 15;
    const int bb = cu >> 7, sp = cu & 127;
    gu32* tmo = (gu32*)(ws + TMO_OFF);
    const __amdgpu_buffer_rsrc_t rs_ws = __builtin_amdgcn_make_buffer_rsrc(ws, 0, (int)WS_NEED, 0x00020000);
    STAMP(0);

    float qp[4];
    {
        const float* p = (const float*)(ws + QP_OFF) + (size_t)(tid >> 8) * 4096 + hh * D + (tid & 255);
#pragma unroll
        for (int i = 0; i < 4; ++i) asm volatile("global_load_dword %0, %1, off" : "=v"(qp[i]) : "v"(p + (size_t)i * 8192));
    }
    v4u wk[8];
    {
        const unsigned short* src = ckv + ((size_t)bb * SK + (size_t)sp * KPS + 4 * wave) * DC;
        if (wave < 4) {
            const unsigned short* p = wuk + ((size_t)(64 * cs + 16 * wave + (lane & 15)) * H + hh) * D + 8 * (lane >> 4);
#pragma unroll
            for (int ks = 0; ks < 8; ++ks) asm volatile("global_load_dwordx4 %0, %1, off nt" : "=v"(wk[ks]) : "v"(p + 32 * ks));
        }
#pragma unroll
        for (int i = 0; i < 8; ++i) GLDS_NT(src + (size_t)i * 512 + lane * 8, L + L_CKV + (4 * wave + (i >> 1)) * CKV_STRIDE + (i & 1) * 1024);
    }
    PIN();
    if (wave < 4) asm volatile("s_waitcnt vmcnt(16)" ::: "memory");
    else asm volatile("s_waitcnt vmcnt(8)" ::: "memory");
    asm volatile("" : "+v"(qp[0]), "+v"(qp[1]), "+v"(qp[2]), "+v"(qp[3]));
    {
        float s = (qp[0] + qp[1]) + (qp[2] + qp[3]);
        s *= 0.0625f * 1.4426950408889634f;
        const unsigned hi = f2bf(s);
        const unsigned lo = f2bf(s - __uint_as_float(hi << 16));
        const int b = tid >> 8, d = tid & 255;
        ((LAS unsigned short*)(S + L_QA))[(2 * b) * 256 + d] = (unsigned short)hi;
        ((LAS unsigned short*)(S + L_QA))[(2 * b + 1) * 256 + d] = (unsigned short)lo;
    }
    LGKM0();
    BAR();
    STAMP(1);
    if (wave < 4) {
        v4u av[8];
#pragma unroll
        for (int ks = 0; ks < 8; ++ks) {
            av[ks] = *(const LAS v4u*)(S + L_QA + (lane & 3) * 512 + (32 * ks + 8 * (lane >> 4)) * 2);
        }
        const bool keep = (lane & 15) < 4;
        asm volatile("s_waitcnt vmcnt(8)" : "+v"(wk[0]), "+v"(wk[1]), "+v"(wk[2]), "+v"(wk[3]), "+v"(wk[4]), "+v"(wk[5]), "+v"(wk[6]), "+v"(wk[7]));
        f32x4 acc = {0.f, 0.f, 0.f, 0.f};
#pragma unroll
        for (int ks = 0; ks < 8; ++ks) {
            v4u a = av[ks];
            a[0] = keep ? a[0] : 0u; a[1] = keep ? a[1] : 0u; a[2] = keep ? a[2] : 0u; a[3] = keep ? a[3] : 0u;
            acc = __builtin_amdgcn_mfma_f32_16x16x32_bf16(__builtin_bit_cast(bf16x8, a), __builtin_bit_cast(bf16x8, wk[ks]), acc, 0, 0, 0);
        }
        LAS unsigned short* qs = (LAS unsigned short*)(S + L_QS) + wave * 16;
        if (lane < 16) {
            qs[lane] = (unsigned short)f2bf(acc[0] + acc[1]);
            qs[64 + lane] = (unsigned short)f2bf(acc[2] + acc[3]);
        }
        LGKM0();
    }
    BAR();
    if (tid < 16 * QREP) {
        v4u v = *(const LAS v4u*)(S + L_QS + (lane & 15) * 16);
        v[0] = canonb(v[0]); v[1] = canonb(v[1]); v[2] = canonb(v[2]); v[3] = canonb(v[3]);
        __builtin_amdgcn_raw_buffer_store_b128(v, rs_ws, (int)(QABS_OFF + (size_t)(tid >> 4) * 65536 + (((size_t)((lane >> 3) & 1) * H + hh) * DC + 64 * cs + 8 * (lane & 7)) * 2), 0, 16);
    } else if (wave == 4 && lane < 14) {
        const int b = lane / 7, r = lane % 7, f = cu * 7 + r;
        const float* hp = (const float*)(ws + HP_OFF);
        float hv = bf2f(b1[f]);
#pragma unroll
        for (int p = 0; p < 4; ++p) hv += hp[((size_t)p * 2 + b) * F + f];
        const float t = tanhf(0.7978845608028654f * (hv + 0.044715f * hv * hv * hv));
        ((LAS float*)(S + L_HM))[b * 8 + r] = 0.5f * hv * (1.0f + t);
    }
    STAMP(2);
    v4u wuvv[4], wov[2], w2v;
    float xb = 0.f;
    {
        const int b = (tid >> 3) & 1, m = (cu < NC5 ? cu : 0) * 8 + (tid & 7);
        xb = bf2f(x[b * M + m]) + bf2f(b2[m]);
    }
    const int qbase = (int)(QABS_OFF + (size_t)((cu >> 3) % QREP) * 65536 + (((size_t)bb * H + (lane & 15)) * DC + 128 * wave) * 2);
    bf16x8 qa[4];
    {
        v4u v[4];
#pragma unroll
        for (int i = 0; i < 4; ++i) v[i] = (v4u){SENT, SENT, SENT, SENT};
        for (unsigned spins = 0;;) {
#pragma unroll
            for (int i = 0; i < 4; ++i)
                if (has_sent(v[i])) v[i] = __builtin_amdgcn_raw_buffer_load_b128(rs_ws, qbase + (32 * i + 8 * (lane >> 4)) * 2, 0, 16);
            bool bad = false;
#pragma unroll
            for (int i = 0; i < 4; ++i) bad |= has_sent(v[i]);
            if (!__any(bad)) break;
            if (spin_fail(spins, tmo)) break;
        }
#pragma unroll
        for (int i = 0; i < 4; ++i) qa[i] = __builtin_bit_cast(bf16x8, v[i]);
    }
    VM0();
    asm volatile("" : "+v"(xb));
    STAMP(3);
    STAMPW(14, 7);
    BAR();
    STAMP(10);
    PIN();
    {
#pragma unroll
        for (int i = 0; i < 4; ++i) {
            const int c = 64 * cs + 8 * wave + 2 * i + (lane >> 5);
            wuvv[i] = __builtin_nontemporal_load((const v4u*)(wuv + ((size_t)c * H + hh) * D + (lane & 31) * 8));
        }
        const unsigned short* so = wo + ((size_t)hh * D + cs * 16) * M;
        const int t2 = tid < 448 ? tid : 447;
        wov[0] = __builtin_nontemporal_load((const v4u*)(so + (size_t)t2 * 8));
        wov[1] = __builtin_nontemporal_load((const v4u*)(so + (size_t)(448 + t2) * 8));
        w2v = __builtin_nontemporal_load((const v4u*)(w2 + (size_t)cu * 7 * M + (size_t)(tid < 392 ? tid : 391) * 8));
    }
    PIN();
    {
        f32x4 acc[2] = {{0.f, 0.f, 0.f, 0.f}, {0.f, 0.f, 0.f, 0.f}};
#pragma unroll
        for (int nt = 0; nt < 2; ++nt)
#pragma unroll
            for (int i = 0; i < 4; ++i) {
                const v4u v = *(const LAS v4u*)(L + L_CKV + (16 * nt + (lane & 15)) * CKV_STRIDE + (128 * wave + 32 * i + 8 * (lane >> 4)) * 2);
                acc[nt] = __builtin_amdgcn_mfma_f32_16x16x32_bf16(qa[i], __builtin_bit_cast(bf16x8, v), acc[nt], 0, 0, 0);
            }
        LAS float* sc = (LAS float*)(S + L_SC);
#pragma unroll
        for (int nt = 0; nt < 2; ++nt)
#pragma unroll
            for (int r = 0; r < 4; ++r) sc[(wave * 16 + 4 * (lane >> 4) + r) * 32 + 16 * nt + (lane & 15)] = acc[nt][r];
    }
    LGKM0();
    BAR();
    float st_m, st_l;
    {
        const int h = tid >> 5, key = tid & 31;
        const LAS float* sc = (const LAS float*)(S + L_SC);
        float s = 0.f;
#pragma unroll
        for (int w = 0; w < 8; ++w) s += sc[(w * 16 + h) * 32 + key];
        float mx = s;
        mx = max_lo5(mx);
        const unsigned pb = f2bf(__builtin_amdgcn_exp2f(s - mx));
        float l = __uint_as_float(pb << 16);
        l = add_lo5(l);
        ((LAS unsigned short*)(S + L_P))[h * 32 + key] = (unsigned short)pb;
        st_m = mx; st_l = l;
    }
    LGKM0();
    BAR();
    {
        const bf16x8 pf = __builtin_bit_cast(bf16x8, *(const LAS v4u*)(S + L_P + (lane & 15) * 64 + (lane >> 4) * 16));
        const int g = lane >> 4, q = (lane & 15) >> 2, p = lane & 3;
        v2u o2[8];
#pragma unroll
        for (int t = 0; t < 8; ++t) {
            const int c0 = 128 * wave + 16 * t;
            const LAS s16x4* a0 = (const LAS s16x4*)(L + L_CKV + (8 * g + q) * CKV_STRIDE + c0 * 2 + 8 * p);
            const LAS s16x4* a1 = (const LAS s16x4*)(L + L_CKV + (8 * g + 4 + q) * CKV_STRIDE + c0 * 2 + 8 * p);
            const s16x4 lo = __builtin_amdgcn_ds_read_tr16_b64_v4i16((LAS s16x4*)a0);
            const s16x4 hi = __builtin_amdgcn_ds_read_tr16_b64_v4i16((LAS s16x4*)a1);
            bf16x8 af;
            af[0] = lo[0]; af[1] = lo[1]; af[2] = lo[2]; af[3] = lo[3]; af[4] = hi[0]; af[5] = hi[1]; af[6] = hi[2]; af[7] = hi[3];
            f32x4 o = {0.f, 0.f, 0.f, 0.f};
            o = __builtin_amdgcn_mfma_f32_16x16x32_bf16(af, pf, o, 0, 0, 0);
            o2[t][0] = pack2(o[0], o[1]); o2[t][1] = pack2(o[2], o[3]);
        }
        LGKM0();
#pragma unroll
        for (int t = 0; t < 8; ++t) *(LAS v2u*)(L + L_ST + (lane & 15) * CKV_STRIDE + (128 * wave + 16 * t + 4 * g) * 2) = o2[t];
    }
    LGKM0();
    BAR();
    STAMP(15);
    {
#pragma unroll
        for (int i = 0; i < 4; ++i) {
            const int h = 2 * wave + (i >> 1);
            v4u v = *(const LAS v4u*)(L + L_ST + h * CKV_STRIDE + (i & 1) * 1024 + lane * 16);
            __builtin_amdgcn_raw_buffer_store_b128(v, rs_ws, (int)(PCTX_OFF + ((((size_t)bb * NSPLIT + sp) * H + h) * DC) * 2 + (i & 1) * 1024 + lane * 16), 0, 16);
        }
        v4u fv;
        fv[0] = canon(__float_as_uint(st_m)); fv[1] = canon(__float_as_uint(st_l));
        fv[2] = canon(__builtin_amdgcn_readlane(__float_as_uint(st_m), 32)); fv[3] = canon(__builtin_amdgcn_readlane(__float_as_uint(st_l), 32));
        if (lane != 0) fv = (v4u){0u, 0u, 0u, 0u};
        VM0();
        if (lane < FLGS / 16) __builtin_amdgcn_raw_buffer_store_b128(fv, rs_ws, (int)(FLG_OFF + ((size_t)cu * 8 + wave) * FLGS + lane * 16), 0, 16);
    }
    STAMP(4);
    if (tid < 448) { *(LAS v4u*)(S + L_WO + tid * 16) = wov[0]; *(LAS v4u*)(S + L_WO + (448 + tid) * 16) = wov[1]; }
    if (tid < 392) *(LAS v4u*)(S + L_W2 + tid * 16) = w2v;
    {
        const int j = lane >> 3, ch = lane & 7;
        v4u pc[4]; v2u ml[4];
        for (unsigned spins = 0;;) {
            bool bad = false;
#pragma unroll
            for (int i = 0; i < 4; ++i) {
                const int b = i >> 1, s = 16 * wave + (i & 1) * 8 + j;
                ml[i] = __builtin_amdgcn_raw_buffer_load_b64(rs_ws, (int)(FLG_OFF + (((size_t)b * 128 + s) * 8 + (hh >> 1)) * FLGS + (hh & 1) * 8), 0, 16);
            }
#pragma unroll
            for (int i = 0; i < 4; ++i) bad |= (ml[i][0] == SENT) || (ml[i][1] == SENT);
            if (!__any(bad)) break;
            if (spin_fail(spins, tmo)) break;
        }
        STAMP(5);
#pragma unroll
        for (int i = 0; i < 4; ++i) {
            const int b = i >> 1, s = 16 * wave + (i & 1) * 8 + j;
            pc[i] = __builtin_amdgcn_raw_buffer_load_b128(rs_ws, (int)(PCTX_OFF + ((((size_t)b * NSPLIT + s) * H + hh) * DC + 64 * cs + ch * 8) * 2), 0, 16);
        }
        LAS float* red = (LAS float*)(S + L_SC);
#pragma unroll
        for (int b = 0; b < 2; ++b) {
            const float m0 = __uint_as_float(ml[2 * b][0]), l0 = __uint_as_float(ml[2 * b][1]);
            const float m1 = __uint_as_float(ml[2 * b + 1][0]), l1 = __uint_as_float(ml[2 * b + 1][1]);
            float gm = fmaxf(m0, m1);
            gm = max_hi3(gm);
            const float e0 = __builtin_amdgcn_exp2f(m0 - gm), e1 = __builtin_amdgcn_exp2f(m1 - gm);
            float ls = e0 * l0 + e1 * l1;
            ls = add_hi3(ls);
            float acc[8];
#pragma unroll
            for (int k = 0; k < 4; ++k) {
                acc[2 * k] = e0 * bflo(pc[2 * b][k]) + e1 * bflo(pc[2 * b + 1][k]);
                acc[2 * k + 1] = e0 * bfhi(pc[2 * b][k]) + e1 * bfhi(pc[2 * b + 1][k]);
            }
#pragma unroll
            for (int k = 0; k < 8; ++k) acc[k] = add_hi3(acc[k]);
            if (lane < 8) {
                *(LAS f32x4*)(red + (wave * 2 + b) * 64 + lane * 8) = (f32x4){acc[0], acc[1], acc[2], acc[3]};
                *(LAS f32x4*)(red + (wave * 2 + b) * 64 + lane * 8 + 4) = (f32x4){acc[4], acc[5], acc[6], acc[7]};
            }
            if (lane == 0) ((LAS f32x2*)(S + L_STAT))[wave * 2 + b] = (f32x2){gm, ls};
        }
    }
    STAMP(11);
    LGKM0();
    BAR();
    STAMP(12);
    if (tid < 128) {
        const int b = tid >> 6;
        const LAS float* red = (const LAS float*)(S + L_SC);
        const LAS f32x2* st = (const LAS f32x2*)(S + L_STAT);
        float gm = st[b][0];
#pragma unroll
        for (int w = 1; w < 8; ++w) gm = fmaxf(gm, st[w * 2 + b][0]);
        float s = 0.f, ls = 0.f;
#pragma unroll
        for (int w = 0; w < 8; ++w) {
            const f32x2 t = st[w * 2 + b];
            const float e = __builtin_amdgcn_exp2f(t[0] - gm);
            ls += e * t[1]; s += e * red[w * 128 + tid];
        }
        ((LAS float*)(S + L_CC))[tid] = s / ls;
    }
    LGKM0();
    BAR();
    {
        const LAS float* cc = (const LAS float*)(S + L_CC);
        f32x2 a[8];
#pragma unroll
        for (int j = 0; j < 8; ++j) a[j] = (f32x2){0.f, 0.f};
#pragma unroll
        for (int i = 0; i < 4; ++i) {
            const int c = 8 * wave + 2 * i + (lane >> 5);
            const v4u w = wuvv[i];
            const f32x2 cv = {cc[c], cc[64 + c]};
#pragma unroll
            for (int j = 0; j < 4; ++j) { a[2 * j] += cv * bflo(w[j]); a[2 * j + 1] += cv * bfhi(w[j]); }
        }
#pragma unroll
        for (int j = 0; j < 8; ++j) { a[j][0] = add32(a[j][0]); a[j][1] = add32(a[j][1]); }
        if (lane < 32) {
            LAS f32x4* red = (LAS f32x4*)(S + L_SC + 4096);
#pragma unroll
            for (int b = 0; b < 2; ++b) {
                red[((wave * 2 + b) * 256 + lane * 8) / 4] = (f32x4){a[0][b], a[1][b], a[2][b], a[3][b]};
                red[((wave * 2 + b) * 256 + lane * 8) / 4 + 1] = (f32x4){a[4][b], a[5][b], a[6][b], a[7][b]};
            }
        }
    }
    LGKM0();
    BAR();
    if (tid < 128) {
        const LAS f32x4* red = (const LAS f32x4*)(S + L_SC + 4096);
        const int b = (tid >> 2) & 1, d = (tid >> 3) * 16 + (tid & 3) * 4;
        const int ridx = b * 64 + (d >> 2);
        f32x4 s = red[ridx];
#pragma unroll
        for (int w = 1; w < 8; ++w) s += red[w * 128 + ridx];
        v4u v; v[0] = canon(__float_as_uint(s[0])); v[1] = canon(__float_as_uint(s[1])); v[2] = canon(__float_as_uint(s[2])); v[3] = canon(__float_as_uint(s[3]));
        __builtin_amdgcn_raw_buffer_store_b128(v, rs_ws, (int)(G4_OFF + ((((size_t)hh * 16 + (d >> 4)) * 16 + cs) * 32 + b * 16 + (d & 15)) * 4), 0, 16);
        STAMP(6);
        v4u g;
        for (unsigned spins = 0;;) {
            g = __builtin_amdgcn_raw_buffer_load_b128(rs_ws, (int)(G4_OFF + (size_t)(hh * 16 + cs) * 2048 + tid * 16), 0, 16);
            if (!__any(has_sent(g))) break;
            if (spin_fail(spins, tmo)) break;
        }
        f32x4 t; t[0] = __uint_as_float(g[0]); t[1] = __uint_as_float(g[1]); t[2] = __uint_as_float(g[2]); t[3] = __uint_as_float(g[3]);
        t[0] = add_hi3(t[0]); t[1] = add_hi3(t[1]); t[2] = add_hi3(t[2]); t[3] = add_hi3(t[3]);
        if (lane < 8) *(LAS f32x4*)(S + L_X6 + wave * 128 + lane * 16) = t;
        STAMP(7);
    }
    LGKM0();
    BAR();
    if (tid < 224) {
        const int j = tid >> 1, b = tid & 1;
        const LAS f32x4* x6 = (const LAS f32x4*)(S + L_X6);
        const LAS float* hm = (const LAS float*)(S + L_HM) + b * 8;
        f32x4 a = {0.f, 0.f, 0.f, 0.f};
#pragma unroll
        for (int q4 = 0; q4 < 4; ++q4) {
            const f32x4 cx = x6[b * 4 + q4] + x6[8 + b * 4 + q4];
#pragma unroll
            for (int k = 0; k < 4; ++k) {
                const v2u w = *(const LAS v2u*)(S + L_WO + (q4 * 4 + k) * (M * 2) + j * 8);
                const float c = cx[k];
                a[0] += c * bflo(w[0]); a[1] += c * bfhi(w[0]); a[2] += c * bflo(w[1]); a[3] += c * bfhi(w[1]);
            }
        }
#pragma unroll
        for (int r = 0; r < 7; ++r) {
            const v2u w = *(const LAS v2u*)(S + L_W2 + r * (M * 2) + j * 8);
            const float c = hm[r];
            a[0] += c * bflo(w[0]); a[1] += c * bfhi(w[0]); a[2] += c * bflo(w[1]); a[3] += c * bfhi(w[1]);
        }
        v4u v; v[0] = canon(__float_as_uint(a[0])); v[1] = canon(__float_as_uint(a[1])); v[2] = canon(__float_as_uint(a[2])); v[3] = canon(__float_as_uint(a[3]));
        __builtin_amdgcn_raw_buffer_store_b128(v, rs_ws, (int)(G5_OFF + (((size_t)(j >> 1) * 256 + cu) * 16 + b * 8 + (j & 1) * 4) * 4), 0, 16);
    }
    STAMP(8);
    if (cu < NC5) {
        v4u v0 = {SENT, SENT, SENT, SENT}, v1 = {SENT, SENT, SENT, SENT};
        for (unsigned spins = 0;;) {
            if (has_sent(v0)) v0 = __builtin_amdgcn_raw_buffer_load_b128(rs_ws, (int)(G5_OFF + (size_t)cu * 16384 + tid * 16), 0, 16);
            if (has_sent(v1)) v1 = __builtin_amdgcn_raw_buffer_load_b128(rs_ws, (int)(G5_OFF + (size_t)cu * 16384 + 8192 + tid * 16), 0, 16);
            if (!__any(has_sent(v0) || has_sent(v1))) break;
            if (spin_fail(spins, tmo)) break;
        }
        f32x4 s; s[0] = __uint_as_float(v0[0]) + __uint_as_float(v1[0]); s[1] = __uint_as_float(v0[1]) + __uint_as_float(v1[1]);
        s[2] = __uint_as_float(v0[2]) + __uint_as_float(v1[2]); s[3] = __uint_as_float(v0[3]) + __uint_as_float(v1[3]);
#pragma unroll
        for (int k = 0; k < 4; ++k) { float v = s[k]; v += dppf<0x124>(v); s[k] = add_hi3(v); }
        LAS float* red = (LAS float*)(S + L_CC);
        if (lane < 4) *(LAS f32x4*)(red + wave * 16 + lane * 4) = s;
        LGKM0();
        BAR();
        if (tid < 16) {
            float t = 0.f;
#pragma unroll
            for (int w = 0; w < 8; ++w) t += red[w * 16 + tid];
            const int b = tid >> 3, m = cu * 8 + (tid & 7);
            t += xb;
            out[b * M + m] = (unsigned short)f2bf(t);
        }
        STAMP(9);
    }
}
}

extern "C" void kernel_launch(void* const* d_in, const int* in_sizes, int n_in,
                              void* d_out, int out_size, void* d_ws, size_t ws_size,
                              hipStream_t stream) {
    const unsigned short* x = (const unsigned short*)d_in[0];
    const unsigned short* ckv = (const unsigned short*)d_in[1];
    const unsigned short* wq = (const unsigned short*)d_in[2];
    const unsigned short* wuk = (const unsigned short*)d_in[3];
    const unsigned short* wuv = (const unsigned short*)d_in[4];
    const unsigned short* wo = (const unsigned short*)d_in[5];
    const unsigned short* w1 = (const unsigned short*)d_in[7];
    const unsigned short* b1 = (const unsigned short*)d_in[8];
    const unsigned short* w2 = (const unsigned short*)d_in[9];
    const unsigned short* b2 = (const unsigned short*)d_in[10];
    unsigned char* ws = (unsigned char*)d_ws;
    k1<<<368, 256, 0, stream>>>(x, wq, w1, ws, PFSRC, (const unsigned char*)ckv, (const unsigned char*)wuk, (const unsigned char*)wuv, (const unsigned char*)wo, (const unsigned char*)w2);
    k2<<<NCU, 512, 0, stream>>>(x, ckv, wuk, wuv, wo, b1, w2, b2, ws, (unsigned short*)d_out);
}
```

```cpp
#include <hip/hip_runtime.h>
#include <stdint.h>

#define GAS __attribute__((address_space(1)))
#define LAS __attribute__((address_space(3)))
typedef unsigned v4u __attribute__((ext_vector_type(4)));
typedef unsigned v2u __attribute__((ext_vector_type(2)));
typedef float f32x4 __attribute__((ext_vector_type(4)));
typedef float f32x2 __attribute__((ext_vector_type(2)));
typedef short bf16x8 __attribute__((ext_vector_type(8)));
typedef short s16x4 __attribute__((ext_vector_type(4)));
typedef __bf16 bf16x2_t __attribute__((ext_vector_type(2)));
typedef GAS unsigned gu32;
typedef GAS unsigned long long gu64;
#define RLX_AGENT __ATOMIC_RELAXED, __HIP_MEMORY_SCOPE_AGENT

#define STAMPS 0
#define ATTST 0
#define PVW 1
#define PRE 1
#define CVT 1
#define P5M 0
#define S2POS 1
#define PAUX 16
#define PLAUX 16
#define E3DLY 0
#define E3SLP 1
#define E3PP 0
#define E2D 0
#define E3D 8
#define E4D 0
#define E5D 0
#define SDLY(n) do { _Pragma("unroll") for (int i_ = 0; i_ < (n) / 4; ++i_) __builtin_amdgcn_s_sleep(16); if ((n) % 4) __builtin_amdgcn_s_sleep(4 * ((n) % 4)); } while (0)
#define PIN() __builtin_amdgcn_sched_barrier(0)

namespace {
constexpr int H = 16, SK = 4096, D = 256, M = 448, DC = 1024, F = 1792;
constexpr int NCU = 256;
constexpr int NSPLIT = 128, KPS = 32;
constexpr int NC5 = 56;

constexpr size_t FLG_OFF = 0;
constexpr size_t FLGS = 128;
constexpr size_t TMO_OFF = 256 * 8 * FLGS;
constexpr size_t QABS_OFF = TMO_OFF + 4096;
constexpr size_t G4_OFF = QABS_OFF + 65536;
constexpr size_t G4_BYTES = (size_t)256 * 512 * 4;
constexpr size_t G5_OFF = G4_OFF + G4_BYTES;
constexpr size_t G5_BYTES = (size_t)56 * 256 * 16 * 4;
constexpr size_t PCTX_OFF = G5_OFF + G5_BYTES;
constexpr size_t PCTX_BYTES = (size_t)2 * 128 * 16 * 1024 * 2;
constexpr size_t FILL_BYTES = PCTX_OFF;
constexpr size_t QP_OFF = PCTX_OFF + PCTX_BYTES;
constexpr size_t HP_OFF = QP_OFF + 4 * 2 * 4096 * 4;
constexpr size_t STAMP_OFF = HP_OFF + 65536;
constexpr size_t K1ST_OFF = STAMP_OFF + 256 * 16 * 8;
constexpr size_t WS_NEED = K1ST_OFF + 512 * 2 * 8;
static_assert(FILL_BYTES % 16 == 0, "fill");

constexpr int CKV_STRIDE = 2064;
constexpr int L_CKV = 0;
constexpr int LDS_DMA_BYTES = 32 * CKV_STRIDE;
constexpr int L_SC = 0;
constexpr int L_WO = L_SC + 20480;
constexpr int L_W2 = L_WO + 14336;
constexpr int L_QA = L_W2 + 7168;
constexpr int L_QS = L_QA + 2048;
constexpr int L_P = L_QS + 512;
constexpr int L_STAT = L_P + 1024;
constexpr int L_CC = L_STAT + 128;
constexpr int L_HM = L_CC + 512;
constexpr int L_X6 = L_HM + 128;
constexpr int L_ML = L_X6 + 256;
constexpr int L_ST = L_CKV;
constexpr int LDS_SC_BYTES = L_ML + 128;
static_assert(LDS_DMA_BYTES + LDS_SC_BYTES <= 160 * 1024, "lds");

constexpr unsigned kSpinCap = 1u << 18;
constexpr unsigned SENT = 0xFFFFFFFFu;

__device__ __forceinline__ float bf2f(unsigned short u) { return __uint_as_float(((unsigned)u) << 16); }
__device__ __forceinline__ float bflo(unsigned u) { return __uint_as_float(u << 16); }
__device__ __forceinline__ float bfhi(unsigned u) { return __uint_as_float(u & 0xffff0000u); }
__device__ __forceinline__ unsigned f2bf(float f) { unsigned u = __float_as_uint(f); return (u + 0x7fffu + ((u >> 16) & 1u)) >> 16; }
__device__ __forceinline__ unsigned pack2(float a, float b) { const f32x2 v = {a, b}; return __builtin_bit_cast(unsigned, __builtin_convertvector(v, bf16x2_t)); }
template <int C> __device__ __forceinline__ float dppf(float v) { return __int_as_float(__builtin_amdgcn_update_dpp(0, __float_as_int(v), C, 0xF, 0xF, true)); }
__device__ __forceinline__ float add16(float v) { auto r = __builtin_amdgcn_permlane16_swap(__float_as_uint(v), __float_as_uint(v), false, false); return __uint_as_float(r[0]) + __uint_as_float(r[1]); }
__device__ __forceinline__ float add32(float v) { auto r = __builtin_amdgcn_permlane32_swap(__float_as_uint(v), __float_as_uint(v), false, false); return __uint_as_float(r[0]) + __uint_as_float(r[1]); }
__device__ __forceinline__ float max16(float v) { auto r = __builtin_amdgcn_permlane16_swap(__float_as_uint(v), __float_as_uint(v), false, false); return fmaxf(__uint_as_float(r[0]), __uint_as_float(r[1])); }
__device__ __forceinline__ float max32(float v) { auto r = __builtin_amdgcn_permlane32_swap(__float_as_uint(v), __float_as_uint(v), false, false); return fmaxf(__uint_as_float(r[0]), __uint_as_float(r[1])); }
__device__ __forceinline__ float add_hi3(float v) { v += dppf<0x128>(v); v = add16(v); return add32(v); }
__device__ __forceinline__ float max_hi3(float v) { v = fmaxf(v, dppf<0x128>(v)); v = max16(v); return max32(v); }
__device__ __forceinline__ float add_lo5(float v) { v += dppf<0xB1>(v); v += dppf<0x4E>(v); v += dppf<0x141>(v); v += dppf<0x140>(v); return add16(v); }
__device__ __forceinline__ float max_lo5(float v) { v = fmaxf(v, dppf<0xB1>(v)); v = fmaxf(v, dppf<0x4E>(v)); v = fmaxf(v, dppf<0x141>(v)); v = fmaxf(v, dppf<0x140>(v)); return max16(v); }
__device__ __forceinline__ float wsum(float v) { return add32(add_lo5(v)); }
__device__ __forceinline__ unsigned long long rt() { return __builtin_amdgcn_s_memrealtime(); }

__global__ void __launch_bounds__(256) k1(const unsigned short* __restrict__ x, const unsigned short* __restrict__ wq,
                                          const unsigned short* __restrict__ w1, unsigned char* __restrict__ ws) {
    __shared__ float xn[2][M];
    __shared__ float red[4][2][64];
    const int tid = threadIdx.x, wave = tid >> 6, lane = tid & 63;
    const int blk = blockIdx.x;
    const bool isq = blk < 256;
    const int idx = isq ? blk : blk - 256;
    const int mq = idx & 3, cg = idx >> 2;
    const int ncols = isq ? 4096 : F;
    const unsigned short* W = (isq ? wq : w1) + (size_t)(mq * 112) * ncols + cg * 64;
    const int r = tid >> 3, cch = tid & 7;
    unsigned short xr[7];
#pragma unroll
    for (int i = 0; i < 7; ++i) xr[i] = x[(wave & 1) * M + lane + 64 * i];
    PIN();
    v4u wv[4];
#pragma unroll
    for (int i = 0; i < 4; ++i) {
        const int row = (i < 3 || r < 16) ? r + 32 * i : 111;
        wv[i] = __builtin_nontemporal_load((const v4u*)(W + (size_t)row * ncols + cch * 8));
    }
    PIN();
    {
        const v4u sn = {SENT, SENT, SENT, SENT};
        v4u* g = (v4u*)ws;
        const unsigned tot16 = (unsigned)(FILL_BYTES / 16);
        for (unsigned i = blk * 256 + tid; i < tot16; i += gridDim.x * 256) g[i] = sn;
    }
    if (wave < 2) {
        float v[7]; float s = 0.f;
#pragma unroll
        for (int i = 0; i < 7; ++i) { v[i] = bf2f(xr[i]); s += v[i]; }
        s = wsum(s); const float mean = s * (1.0f / M);
        float ss = 0.f;
#pragma unroll
        for (int i = 0; i < 7; ++i) { const float d = v[i] - mean; ss += d * d; }
        ss = wsum(ss);
        const float rs = 1.0f / sqrtf(ss * (1.0f / M) + 1e-5f);
#pragma unroll
        for (int i = 0; i < 7; ++i) xn[wave][lane + 64 * i] = (v[i] - mean) * rs;
    }
    __syncthreads();
    float a[2][8];
#pragma unroll
    for (int j = 0; j < 8; ++j) { a[0][j] = 0.f; a[1][j] = 0.f; }
#pragma unroll
    for (int i = 0; i < 4; ++i) {
        int row = mq * 112 + r + 32 * i; if (row >= M) row = M - 1;
        const bool ok = (i < 3 || r < 16);
        const float x0 = ok ? xn[0][row] : 0.f, x1 = ok ? xn[1][row] : 0.f;
#pragma unroll
        for (int j = 0; j < 4; ++j) {
            const float lo = bflo(wv[i][j]), hi = bfhi(wv[i][j]);
            a[0][2 * j] += x0 * lo; a[0][2 * j + 1] += x0 * hi;
            a[1][2 * j] += x1 * lo; a[1][2 * j + 1] += x1 * hi;
        }
    }
#pragma unroll
    for (int b = 0; b < 2; ++b)
#pragma unroll
        for (int j = 0; j < 8; ++j) a[b][j] = add_hi3(a[b][j]);
    if (lane < 8) {
#pragma unroll
        for (int b = 0; b < 2; ++b)
#pragma unroll
            for (int j = 0; j < 8; ++j) red[wave][b][lane * 8 + j] = a[b][j];
    }
    __syncthreads();
    if (tid < 128) {
        const int b = tid >> 6, c = tid & 63;
        const float s = red[0][b][c] + red[1][b][c] + red[2][b][c] + red[3][b][c];
        float* dst = (float*)(ws + (isq ? QP_OFF : HP_OFF));
        dst[((size_t)mq * 2 + b) * ncols + cg * 64 + c] = s;
    }
}

__device__ __forceinline__ bool spin_fail(unsigned& spins, gu32* tmo) {
    __builtin_amdgcn_s_sleep(1);
    if (++spins > kSpinCap) { __hip_atomic_store(tmo, 1u, RLX_AGENT); return true; }
    return (spins & 1023u) == 0u && __builtin_amdgcn_readfirstlane(__hip_atomic_load(tmo, RLX_AGENT)) == 1u;
}
__device__ __forceinline__ bool has_sent(v4u v) { return v[0] == SENT || v[1] == SENT || v[2] == SENT || v[3] == SENT; }
__device__ __forceinline__ unsigned canon(unsigned u) { return u == SENT ? 0x7FC00000u : u; }
__device__ __forceinline__ unsigned canonb(unsigned u) { return u == SENT ? 0xFFC0FFC0u : u; }

#define GLDS_NT(src, dst) __builtin_amdgcn_global_load_lds((const unsigned*)(src), (LAS unsigned*)(dst), 16, 0, 2)
#define BAR() __builtin_amdgcn_s_barrier()
#define LGKM0() asm volatile("s_waitcnt lgkmcnt(0)" ::: "memory")
#define VM0() asm volatile("s_waitcnt vmcnt(0)" ::: "memory")
#define STAMP(i) do {} while (0)
#define STAMPW(i, w) do {} while (0)

#define WUV_ROW(i) (64 * cs + 8 * wave + 2 * (i) + (lane >> 5))
#define STAGE2_LOADS() do { \
        _Pragma("unroll") for (int i = 0; i < 4; ++i) \
            wuvv[i] = __builtin_nontemporal_load((const v4u*)(wuv + ((size_t)WUV_ROW(i) * H + hh) * D + (lane & 31) * 8)); \
        const unsigned short* so = wo + ((size_t)hh * D + cs * 16) * M; \
        const int t2 = tid < 448 ? tid : 447; \
        wov[0] = __builtin_nontemporal_load((const v4u*)(so + (size_t)t2 * 8)); \
        wov[1] = __builtin_nontemporal_load((const v4u*)(so + (size_t)(448 + t2) * 8)); \
        w2v = __builtin_nontemporal_load((const v4u*)(w2 + (size_t)cu * 7 * M + (size_t)(tid < 392 ? tid : 391) * 8)); \
    } while (0)
#define STAGE2_LOADS_ASM() do { \
        _Pragma("unroll") for (int i = 0; i < 4; ++i) \
            asm volatile("global_load_dwordx4 %0, %1, off nt" : "=v"(wuvv[i]) : "v"(wuv + ((size_t)WUV_ROW(i) * H + hh) * D + (lane & 31) * 8) : "memory"); \
        const unsigned short* so = wo + ((size_t)hh * D + cs * 16) * M; \
        const int t2 = tid < 448 ? tid : 447; \
        asm volatile("global_load_dwordx4 %0, %1, off nt" : "=v"(wov[0]) : "v"(so + (size_t)t2 * 8) : "memory"); \
        asm volatile("global_load_dwordx4 %0, %1, off nt" : "=v"(wov[1]) : "v"(so + (size_t)(448 + t2) * 8) : "memory"); \
        asm volatile("global_load_dwordx4 %0, %1, off nt" : "=v"(w2v) : "v"(w2 + (size_t)cu * 7 * M + (size_t)(tid < 392 ? tid : 391) * 8) : "memory"); \
    } while (0)
__global__ void __launch_bounds__(512, 1) k2(const unsigned short* __restrict__ x, const unsigned short* __restrict__ ckv,
                                             const unsigned short* __restrict__ wuk, const unsigned short* __restrict__ wuv,
                                             const unsigned short* __restrict__ wo, const unsigned short* __restrict__ b1,
                                             const unsigned short* __restrict__ w2, const unsigned short* __restrict__ b2,
                                             unsigned char* __restrict__ ws, unsigned short* __restrict__ out) {
    __shared__ __attribute__((aligned(16))) unsigned char lds_dma[LDS_DMA_BYTES];
    __shared__ __attribute__((aligned(16))) unsigned char lds_sc[LDS_SC_BYTES];
    LAS unsigned char* L = (LAS unsigned char*)lds_dma;
    LAS unsigned char* S = (LAS unsigned char*)lds_sc;
    const int tid = threadIdx.x, wave = __builtin_amdgcn_readfirstlane(tid >> 6), lane = tid & 63;
    const int cu = blockIdx.x;
    const int hh = cu >> 4, cs = cu & 15;
    const int bb = cu >> 7, sp = cu & 127;
    gu32* tmo = (gu32*)(ws + TMO_OFF);
    const __amdgpu_buffer_rsrc_t rs_ws = __builtin_amdgcn_make_buffer_rsrc(ws, 0, (int)WS_NEED, 0x00020000);
    STAMP(0);

    float qp[4];
    {
        const float* p = (const float*)(ws + QP_OFF) + (size_t)(tid >> 8) * 4096 + hh * D + (tid & 255);
#pragma unroll
        for (int i = 0; i < 4; ++i) asm volatile("global_load_dword %0, %1, off" : "=v"(qp[i]) : "v"(p + (size_t)i * 8192));
    }
    v4u wk[8];
    {
        const unsigned short* src = ckv + ((size_t)bb * SK + (size_t)sp * KPS + 4 * wave) * DC;
        if (wave < 4) {
            const unsigned short* p = wuk + ((size_t)(64 * cs + 16 * wave + (lane & 15)) * H + hh) * D + 8 * (lane >> 4);
#pragma unroll
            for (int ks = 0; ks < 8; ++ks) asm volatile("global_load_dwordx4 %0, %1, off nt" : "=v"(wk[ks]) : "v"(p + 32 * ks));
        }
#pragma unroll
        for (int i = 0; i < 8; ++i) GLDS_NT(src + (size_t)i * 512 + lane * 8, L + L_CKV + (4 * wave + (i >> 1)) * CKV_STRIDE + (i & 1) * 1024);
    }
    PIN();
    if (wave < 4) asm volatile("s_waitcnt vmcnt(16)" ::: "memory");
    else asm volatile("s_waitcnt vmcnt(8)" ::: "memory");
    asm volatile("" : "+v"(qp[0]), "+v"(qp[1]), "+v"(qp[2]), "+v"(qp[3]));
    {
        float s = (qp[0] + qp[1]) + (qp[2] + qp[3]);
        s *= 0.0625f * 1.4426950408889634f;
        const unsigned hi = f2bf(s);
        const unsigned lo = f2bf(s - __uint_as_float(hi << 16));
        const int b = tid >> 8, d = tid & 255;
        ((LAS unsigned short*)(S + L_QA))[(2 * b) * 256 + d] = (unsigned short)hi;
        ((LAS unsigned short*)(S + L_QA))[(2 * b + 1) * 256 + d] = (unsigned short)lo;
    }
    LGKM0();
    BAR();
    STAMP(1);
    if (wave < 4) {
        v4u av[8];
#pragma unroll
        for (int ks = 0; ks < 8; ++ks) {
            av[ks] = *(const LAS v4u*)(S + L_QA + (lane & 3) * 512 + (32 * ks + 8 * (lane >> 4)) * 2);
        }
        const bool keep = (lane & 15) < 4;
        asm volatile("s_waitcnt vmcnt(8)" : "+v"(wk[0]), "+v"(wk[1]), "+v"(wk[2]), "+v"(wk[3]), "+v"(wk[4]), "+v"(wk[5]), "+v"(wk[6]), "+v"(wk[7]));
        f32x4 acc = {0.f, 0.f, 0.f, 0.f};
#pragma unroll
        for (int ks = 0; ks < 8; ++ks) {
            v4u a = av[ks];
            a[0] = keep ? a[0] : 0u; a[1] = keep ? a[1] : 0u; a[2] = keep ? a[2] : 0u; a[3] = keep ? a[3] : 0u;
            acc = __builtin_amdgcn_mfma_f32_16x16x32_bf16(__builtin_bit_cast(bf16x8, a), __builtin_bit_cast(bf16x8, wk[ks]), acc, 0, 0, 0);
        }
        LAS unsigned short* qs = (LAS unsigned short*)(S + L_QS) + wave * 16;
        if (lane < 16) {
            qs[lane] = (unsigned short)f2bf(acc[0] + acc[1]);
            qs[64 + lane] = (unsigned short)f2bf(acc[2] + acc[3]);
        }
        LGKM0();
    }
    BAR();
    float xb = 0.f;
    {
        const int b = (tid >> 3) & 1, m = (cu < NC5 ? cu : 0) * 8 + (tid & 7);
        xb = bf2f(x[b * M + m]) + bf2f(b2[m]);
    }
    STAMP(2);
    PIN();
    if (tid < 16) {
        v4u v = *(const LAS v4u*)(S + L_QS + lane * 16);
        v[0] = canonb(v[0]); v[1] = canonb(v[1]); v[2] = canonb(v[2]); v[3] = canonb(v[3]);
        __builtin_amdgcn_raw_buffer_store_b128(v, rs_ws, (int)(QABS_OFF + (((size_t)(lane >> 3) * H + hh) * DC + 64 * cs + 8 * (lane & 7)) * 2), 0, 16);
    } else if (wave == 4 && lane < 14) {
        const int b = lane / 7, r = lane % 7, f = cu * 7 + r;
        const float* hp = (const float*)(ws + HP_OFF);
        float hv = bf2f(b1[f]);
#pragma unroll
        for (int p = 0; p < 4; ++p) hv += hp[((size_t)p * 2 + b) * F + f];
        const float t = tanhf(0.7978845608028654f * (hv + 0.044715f * hv * hv * hv));
        ((LAS float*)(S + L_HM))[b * 8 + r] = 0.5f * hv * (1.0f + t);
    }
    PIN();
    v4u wuvv[4], wov[2], w2v;
    const int g4 = lane >> 4;
    if (wave == 0) asm volatile("s_waitcnt vmcnt(1)" ::: "memory");
    else VM0();
    STAMPW(13, 7);
    BAR();
    v4u kf[8]; v2u vf[16];
    {
#pragma unroll
        for (int nt = 0; nt < 2; ++nt)
#pragma unroll
            for (int i = 0; i < 4; ++i)
                kf[nt * 4 + i] = *(const LAS v4u*)(L + L_CKV + (16 * nt + (lane & 15)) * CKV_STRIDE + (128 * wave + 32 * i + 8 * g4) * 2);
        const int q = (lane & 15) >> 2, p = lane & 3;
#pragma unroll
        for (int t = 0; t < 8; ++t) {
            const int c0 = 128 * wave + 16 * t;
            vf[2 * t] = __builtin_bit_cast(v2u, __builtin_amdgcn_ds_read_tr16_b64_v4i16((LAS s16x4*)(L + L_CKV + (8 * g4 + q) * CKV_STRIDE + c0 * 2 + 8 * p)));
            vf[2 * t + 1] = __builtin_bit_cast(v2u, __builtin_amdgcn_ds_read_tr16_b64_v4i16((LAS s16x4*)(L + L_CKV + (8 * g4 + 4 + q) * CKV_STRIDE + c0 * 2 + 8 * p)));
        }
        asm volatile("" : "+v"(kf[0]), "+v"(kf[1]), "+v"(kf[2]), "+v"(kf[3]), "+v"(kf[4]), "+v"(kf[5]), "+v"(kf[6]), "+v"(kf[7]));
        asm volatile("" : "+v"(vf[0]), "+v"(vf[1]), "+v"(vf[2]), "+v"(vf[3]), "+v"(vf[4]), "+v"(vf[5]), "+v"(vf[6]), "+v"(vf[7]));
        asm volatile("" : "+v"(vf[8]), "+v"(vf[9]), "+v"(vf[10]), "+v"(vf[11]), "+v"(vf[12]), "+v"(vf[13]), "+v"(vf[14]), "+v"(vf[15]));
    }
    PIN();
    const int qbase = (int)(QABS_OFF + (((size_t)bb * H + (lane & 15)) * DC + 128 * wave) * 2);
    bf16x8 qa[4];
    {
        v4u v[4];
#pragma unroll
        for (int i = 0; i < 4; ++i) v[i] = (v4u){SENT, SENT, SENT, SENT};
        SDLY(E2D);
        for (unsigned spins = 0;;) {
#pragma unroll
            for (int i = 0; i < 4; ++i)
                if (has_sent(v[i])) v[i] = __builtin_amdgcn_raw_buffer_load_b128(rs_ws, qbase + (32 * i + 8 * g4) * 2, 0, 16);
            bool bad = false;
#pragma unroll
            for (int i = 0; i < 4; ++i) bad |= has_sent(v[i]);
            if (!__any(bad)) break;
            if (spin_fail(spins, tmo)) break;
        }
#pragma unroll
        for (int i = 0; i < 4; ++i) qa[i] = __builtin_bit_cast(bf16x8, v[i]);
    }
    STAMP(3);
    STAMPW(14, 7);
    STAMP(10);
    {
        f32x4 acc[2] = {{0.f, 0.f, 0.f, 0.f}, {0.f, 0.f, 0.f, 0.f}};
#pragma unroll
        for (int i = 0; i < 4; ++i)
#pragma unroll
            for (int nt = 0; nt < 2; ++nt) {
                const v4u v = kf[nt * 4 + i];
                acc[nt] = __builtin_amdgcn_mfma_f32_16x16x32_bf16(qa[i], __builtin_bit_cast(bf16x8, v), acc[nt], 0, 0, 0);
            }
        LAS float* sc = (LAS float*)(S + L_SC);
#pragma unroll
        for (int nt = 0; nt < 2; ++nt)
#pragma unroll
            for (int r = 0; r < 4; ++r) sc[(wave * 16 + 4 * g4 + r) * 32 + 16 * nt + (lane & 15)] = acc[nt][r];
    }
    PIN();
    STAGE2_LOADS();
    PIN();
    LGKM0();
    BAR();
    float st_m, st_l;
    {
        const int h = tid >> 5, key = tid & 31;
        const LAS float* sc = (const LAS float*)(S + L_SC);
        float s = 0.f;
#pragma unroll
        for (int w = 0; w < 8; ++w) s += sc[(w * 16 + h) * 32 + key];
        float mx = s;
        mx = max_lo5(mx);
        const unsigned pb = f2bf(__builtin_amdgcn_exp2f(s - mx));
        float l = __uint_as_float(pb << 16);
        l = add_lo5(l);
        ((LAS unsigned short*)(S + L_P))[h * 32 + key] = (unsigned short)pb;
        st_m = mx; st_l = l;
        if (key == 0) ((LAS v2u*)(S + L_ML))[h] = (v2u){canon(__float_as_uint(mx)), canon(__float_as_uint(l))};
    }
    LGKM0();
    BAR();
    v4u fv;
    {
        const bf16x8 pf = __builtin_bit_cast(bf16x8, *(const LAS v4u*)(S + L_P + (lane & 15) * 64 + g4 * 16));
        fv = *(const LAS v4u*)(S + L_ML + (lane & 7) * 16);
        v2u o2[8];
#pragma unroll
        for (int t = 0; t < 8; ++t) {
            const v4u av = {vf[2 * t][0], vf[2 * t][1], vf[2 * t + 1][0], vf[2 * t + 1][1]};
            const bf16x8 af = __builtin_bit_cast(bf16x8, av);
            f32x4 o = {0.f, 0.f, 0.f, 0.f};
            o = __builtin_amdgcn_mfma_f32_16x16x32_bf16(af, pf, o, 0, 0, 0);
            o2[t][0] = pack2(o[0], o[1]); o2[t][1] = pack2(o[2], o[3]);
        }
#pragma unroll
        for (int t = 0; t < 8; ++t) *(LAS v2u*)(L + L_ST + (lane & 15) * CKV_STRIDE + (128 * wave + 16 * t + 4 * g4) * 2) = o2[t];
    }
    LGKM0();
    STAMP(15);
    {
#pragma unroll
        for (int i = 0; i < 4; ++i) {
            const int h = 4 * i + g4;
            const v4u v = *(const LAS v4u*)(L + L_ST + h * CKV_STRIDE + 256 * wave + (lane & 15) * 16);
            __builtin_amdgcn_raw_buffer_store_b128(v, rs_ws, (int)(PCTX_OFF + ((((size_t)bb * NSPLIT + sp) * H + h) * DC) * 2 + 256 * wave + (lane & 15) * 16), 0, PAUX);
        }
        VM0();
        if (lane < 8) __builtin_amdgcn_raw_buffer_store_b128(fv, rs_ws, (int)(FLG_OFF + ((size_t)cu * 8 + wave) * FLGS + lane * 16), 0, 16);
    }
    STAMP(4);
    if (tid < 448) { *(LAS v4u*)(S + L_WO + tid * 16) = wov[0]; *(LAS v4u*)(S + L_WO + (448 + tid) * 16) = wov[1]; }
    if (tid < 392) *(LAS v4u*)(S + L_W2 + tid * 16) = w2v;
    {
        const int j = lane >> 3, ch = lane & 7;
        v4u pc[4]; v2u ml[4];
        const int fo = (cs >> 1) * (int)FLGS + hh * 8;
        SDLY(E3D);
        for (unsigned spins = 0;;) {
            bool bad = false;
#pragma unroll
            for (int i = 0; i < 4; ++i) {
                const int b = i >> 1, s = 16 * wave + (i & 1) * 8 + j;
                ml[i] = __builtin_amdgcn_raw_buffer_load_b64(rs_ws, (int)(FLG_OFF + ((size_t)b * 128 + s) * 8 * FLGS) + fo, 0, 16);
            }
#pragma unroll
            for (int i = 0; i < 4; ++i) bad |= (ml[i][0] == SENT) || (ml[i][1] == SENT);
            if (!__any(bad)) break;
            if (spin_fail(spins, tmo)) break;
        }
        STAMP(5);
#pragma unroll
        for (int i = 0; i < 4; ++i) {
            const int b = i >> 1, s = 16 * wave + (i & 1) * 8 + j;
            pc[i] = __builtin_amdgcn_raw_buffer_load_b128(rs_ws, (int)(PCTX_OFF + ((((size_t)b * NSPLIT + s) * H + hh) * DC + 64 * cs + ch * 8) * 2), 0, PLAUX);
        }
        LAS float* red = (LAS float*)(S + L_SC);
#pragma unroll
        for (int b = 0; b < 2; ++b) {
            const float m0 = __uint_as_float(ml[2 * b][0]), l0 = __uint_as_float(ml[2 * b][1]);
            const float m1 = __uint_as_float(ml[2 * b + 1][0]), l1 = __uint_as_float(ml[2 * b + 1][1]);
            float gm = fmaxf(m0, m1);
            gm = max_hi3(gm);
            const float e0 = __builtin_amdgcn_exp2f(m0 - gm), e1 = __builtin_amdgcn_exp2f(m1 - gm);
            float ls = e0 * l0 + e1 * l1;
            ls = add_hi3(ls);
            float acc[8];
#pragma unroll
            for (int k = 0; k < 4; ++k) {
                acc[2 * k] = e0 * bflo(pc[2 * b][k]) + e1 * bflo(pc[2 * b + 1][k]);
                acc[2 * k + 1] = e0 * bfhi(pc[2 * b][k]) + e1 * bfhi(pc[2 * b + 1][k]);
            }
#pragma unroll
            for (int k = 0; k < 8; ++k) acc[k] = add_hi3(acc[k]);
            if (lane < 8) {
                *(LAS f32x4*)(red + (wave * 2 + b) * 64 + lane * 8) = (f32x4){acc[0], acc[1], acc[2], acc[3]};
                *(LAS f32x4*)(red + (wave * 2 + b) * 64 + lane * 8 + 4) = (f32x4){acc[4], acc[5], acc[6], acc[7]};
            }
            if (lane == 0) ((LAS f32x2*)(S + L_STAT))[wave * 2 + b] = (f32x2){gm, ls};
        }
    }
    STAMP(11);
    LGKM0();
    BAR();
    STAMP(12);
    if (tid < 128) {
        const int b = tid >> 6;
        const LAS float* red = (const LAS float*)(S + L_SC);
        const LAS f32x2* st = (const LAS f32x2*)(S + L_STAT);
        float gm = st[b][0];
#pragma unroll
        for (int w = 1; w < 8; ++w) gm = fmaxf(gm, st[w * 2 + b][0]);
        float s = 0.f, ls = 0.f;
#pragma unroll
        for (int w = 0; w < 8; ++w) {
            const f32x2 t = st[w * 2 + b];
            const float e = __builtin_amdgcn_exp2f(t[0] - gm);
            ls += e * t[1]; s += e * red[w * 128 + tid];
        }
        ((LAS float*)(S + L_CC))[tid] = s / ls;
    }
    LGKM0();
    BAR();
    {
        f32x2 a[8];
#pragma unroll
        for (int j = 0; j < 8; ++j) a[j] = (f32x2){0.f, 0.f};
#pragma unroll
        for (int i = 0; i < 4; ++i) {
            const v4u w = wuvv[i];
            const LAS float* cc = (const LAS float*)(S + L_CC);
            const int c = 8 * wave + 2 * i + (lane >> 5);
            const f32x2 cv = {cc[c], cc[64 + c]};
#pragma unroll
            for (int j = 0; j < 4; ++j) { a[2 * j] += cv * bflo(w[j]); a[2 * j + 1] += cv * bfhi(w[j]); }
        }
#pragma unroll
        for (int j = 0; j < 8; ++j) { a[j][0] = add32(a[j][0]); a[j][1] = add32(a[j][1]); }
        if (lane < 32) {
            LAS f32x4* red = (LAS f32x4*)(S + L_SC + 4096);
#pragma unroll
            for (int b = 0; b < 2; ++b) {
                red[((wave * 2 + b) * 256 + lane * 8) / 4] = (f32x4){a[0][b], a[1][b], a[2][b], a[3][b]};
                red[((wave * 2 + b) * 256 + lane * 8) / 4 + 1] = (f32x4){a[4][b], a[5][b], a[6][b], a[7][b]};
            }
        }
    }
    LGKM0();
    BAR();
    if (tid < 128) {
        const LAS f32x4* red = (const LAS f32x4*)(S + L_SC + 4096);
        const int b = (tid >> 2) & 1, d = (tid >> 3) * 16 + (tid & 3) * 4;
        const int ridx = b * 64 + (d >> 2);
        f32x4 s = red[ridx];
#pragma unroll
        for (int w = 1; w < 8; ++w) s += red[w * 128 + ridx];
        v4u v; v[0] = canon(__float_as_uint(s[0])); v[1] = canon(__float_as_uint(s[1])); v[2] = canon(__float_as_uint(s[2])); v[3] = canon(__float_as_uint(s[3]));
        __builtin_amdgcn_raw_buffer_store_b128(v, rs_ws, (int)(G4_OFF + ((((size_t)hh * 16 + (d >> 4)) * 16 + cs) * 32 + b * 16 + (d & 15)) * 4), 0, 16);
        STAMP(6);
        v4u g;
        SDLY(E4D);
        for (unsigned spins = 0;;) {
            g = __builtin_amdgcn_raw_buffer_load_b128(rs_ws, (int)(G4_OFF + (size_t)(hh * 16 + cs) * 2048 + tid * 16), 0, 16);
            if (!__any(has_sent(g))) break;
            if (spin_fail(spins, tmo)) break;
        }
        f32x4 t; t[0] = __uint_as_float(g[0]); t[1] = __uint_as_float(g[1]); t[2] = __uint_as_float(g[2]); t[3] = __uint_as_float(g[3]);
        t[0] = add_hi3(t[0]); t[1] = add_hi3(t[1]); t[2] = add_hi3(t[2]); t[3] = add_hi3(t[3]);
        if (lane < 8) *(LAS f32x4*)(S + L_X6 + wave * 128 + lane * 16) = t;
        STAMP(7);
    }
    LGKM0();
    BAR();
    if (tid < 224) {
        const int j = tid >> 1, b = tid & 1;
        const LAS f32x4* x6 = (const LAS f32x4*)(S + L_X6);
        const LAS float* hm = (const LAS float*)(S + L_HM) + b * 8;
        f32x4 a = {0.f, 0.f, 0.f, 0.f};
#pragma unroll
        for (int q4 = 0; q4 < 4; ++q4) {
            const f32x4 cx = x6[b * 4 + q4] + x6[8 + b * 4 + q4];
#pragma unroll
            for (int k = 0; k < 4; ++k) {
                const v2u w = *(const LAS v2u*)(S + L_WO + (q4 * 4 + k) * (M * 2) + j * 8);
                const float c = cx[k];
                a[0] += c * bflo(w[0]); a[1] += c * bfhi(w[0]); a[2] += c * bflo(w[1]); a[3] += c * bfhi(w[1]);
            }
        }
#pragma unroll
        for (int r = 0; r < 7; ++r) {
            const v2u w = *(const LAS v2u*)(S + L_W2 + r * (M * 2) + j * 8);
            const float c = hm[r];
            a[0] += c * bflo(w[0]); a[1] += c * bfhi(w[0]); a[2] += c * bflo(w[1]); a[3] += c * bfhi(w[1]);
        }
        v4u v; v[0] = canon(__float_as_uint(a[0])); v[1] = canon(__float_as_uint(a[1])); v[2] = canon(__float_as_uint(a[2])); v[3] = canon(__float_as_uint(a[3]));
        __builtin_amdgcn_raw_buffer_store_b128(v, rs_ws, (int)(G5_OFF + (((size_t)(j >> 1) * 256 + cu) * 16 + b * 8 + (j & 1) * 4) * 4), 0, 16);
    }
    STAMP(8);
    if (cu < NC5) {
        v4u v0 = {SENT, SENT, SENT, SENT}, v1 = {SENT, SENT, SENT, SENT};
        SDLY(E5D);
        for (unsigned spins = 0;;) {
            if (has_sent(v0)) v0 = __builtin_amdgcn_raw_buffer_load_b128(rs_ws, (int)(G5_OFF + (size_t)cu * 16384 + tid * 16), 0, 16);
            if (has_sent(v1)) v1 = __builtin_amdgcn_raw_buffer_load_b128(rs_ws, (int)(G5_OFF + (size_t)cu * 16384 + 8192 + tid * 16), 0, 16);
            if (!__any(has_sent(v0) || has_sent(v1))) break;
            if (spin_fail(spins, tmo)) break;
        }
        f32x4 s; s[0] = __uint_as_float(v0[0]) + __uint_as_float(v1[0]); s[1] = __uint_as_float(v0[1]) + __uint_as_float(v1[1]);
        s[2] = __uint_as_float(v0[2]) + __uint_as_float(v1[2]); s[3] = __uint_as_float(v0[3]) + __uint_as_float(v1[3]);
#pragma unroll
        for (int k = 0; k < 4; ++k) { float v = s[k]; v += dppf<0x124>(v); s[k] = add_hi3(v); }
        LAS float* red = (LAS float*)(S + L_CC);
        if (lane < 4) *(LAS f32x4*)(red + wave * 16 + lane * 4) = s;
        LGKM0();
        BAR();
        if (tid < 16) {
            float t = 0.f;
#pragma unroll
            for (int w = 0; w < 8; ++w) t += red[w * 16 + tid];
            const int b = tid >> 3, m = cu * 8 + (tid & 7);
            t += xb;
            out[b * M + m] = (unsigned short)f2bf(t);
        }
        STAMP(9);
    }
}
}

extern "C" void kernel_launch(void* const* d_in, const int* in_sizes, int n_in,
                              void* d_out, int out_size, void* d_ws, size_t ws_size,
                              hipStream_t stream) {
    const unsigned short* x = (const unsigned short*)d_in[0];
    const unsigned short* ckv = (const unsigned short*)d_in[1];
    const unsigned short* wq = (const unsigned short*)d_in[2];
    const unsigned short* wuk = (const unsigned short*)d_in[3];
    const unsigned short* wuv = (const unsigned short*)d_in[4];
    const unsigned short* wo = (const unsigned short*)d_in[5];
    const unsigned short* w1 = (const unsigned short*)d_in[7];
    const unsigned short* b1 = (const unsigned short*)d_in[8];
    const unsigned short* w2 = (const unsigned short*)d_in[9];
    const unsigned short* b2 = (const unsigned short*)d_in[10];
    unsigned char* ws = (unsigned char*)d_ws;
    k1<<<368, 256, 0, stream>>>(x, wq, w1, ws);
    k2<<<NCU, 512, 0, stream>>>(x, ckv, wuk, wuv, wo, b1, w2, b2, ws, (unsigned short*)d_out);
}
```

```cpp
#include <hip/hip_runtime.h>
#include <stdint.h>

#define GAS __attribute__((address_space(1)))
#define LAS __attribute__((address_space(3)))
typedef unsigned v4u __attribute__((ext_vector_type(4)));
typedef unsigned v2u __attribute__((ext_vector_type(2)));
typedef float f32x4 __attribute__((ext_vector_type(4)));
typedef float f32x2 __attribute__((ext_vector_type(2)));
typedef short bf16x8 __attribute__((ext_vector_type(8)));
typedef short s16x4 __attribute__((ext_vector_type(4)));
typedef __bf16 bf16x2_t __attribute__((ext_vector_type(2)));
typedef GAS unsigned gu32;
typedef GAS unsigned long long gu64;
#define RLX_AGENT __ATOMIC_RELAXED, __HIP_MEMORY_SCOPE_AGENT

#define STAMPS 0
#define ATTST 0
#define PVW 1
#define PRE 1
#define CVT 1
#define P5M 0
#define S2POS 1
#define PAUX 16
#define PLAUX 16
#define E3DLY 0
#define E3SLP 1
#define E3PP 0
#define E2D 0
#define E3D 8
#define E4D 0
#define E5D 0
#define SDLY(n) do { _Pragma("unroll") for (int i_ = 0; i_ < (n) / 4; ++i_) __builtin_amdgcn_s_sleep(16); if ((n) % 4) __builtin_amdgcn_s_sleep(4 * ((n) % 4)); } while (0)
#define QHL 1
#define E2C 0
#define CKVPOS 0
#define E2ST 0
#define PIN() __builtin_amdgcn_sched_barrier(0)

namespace {
constexpr int H = 16, SK = 4096, D = 256, M = 448, DC = 1024, F = 1792;
constexpr int NCU = 256;
constexpr int NSPLIT = 128, KPS = 32;
constexpr int NC5 = 56;

constexpr size_t FLG_OFF = 0;
constexpr size_t FLGS = 128;
constexpr size_t TMO_OFF = 256 * 8 * FLGS;
constexpr size_t QABS_OFF = TMO_OFF + 4096;
constexpr size_t G4_OFF = QABS_OFF + 65536 * (1 + QHL);
constexpr size_t G4_BYTES = (size_t)256 * 512 * 4;
constexpr size_t G5_OFF = G4_OFF + G4_BYTES;
constexpr size_t G5_BYTES = (size_t)56 * 256 * 16 * 4;
constexpr size_t PCTX_OFF = G5_OFF + G5_BYTES;
constexpr size_t PCTX_BYTES = (size_t)2 * 128 * 16 * 1024 * 2;
constexpr size_t FILL_BYTES = PCTX_OFF;
constexpr size_t QP_OFF = PCTX_OFF + PCTX_BYTES;
constexpr size_t HP_OFF = QP_OFF + 4 * 2 * 4096 * 4;
constexpr size_t STAMP_OFF = HP_OFF + 65536;
constexpr size_t K1ST_OFF = STAMP_OFF + 256 * 16 * 8;
constexpr size_t WS_NEED = K1ST_OFF + 512 * 2 * 8;
static_assert(FILL_BYTES % 16 == 0, "fill");

constexpr int CKV_STRIDE = 2064;
constexpr int L_CKV = 0;
constexpr int LDS_DMA_BYTES = 32 * CKV_STRIDE;
constexpr int L_SC = 0;
constexpr int L_WO = L_SC + 20480;
constexpr int L_W2 = L_WO + 14336;
constexpr int L_QA = L_W2 + 7168;
constexpr int L_QS = L_QA + 2048;
constexpr int L_P = L_QS + 512;
constexpr int L_STAT = L_P + 1024;
constexpr int L_CC = L_STAT + 128;
constexpr int L_HM = L_CC + 512;
constexpr int L_X6 = L_HM + 128;
constexpr int L_ML = L_X6 + 256;
constexpr int L_ST = L_CKV;
constexpr int LDS_SC_BYTES = L_ML + 128;
static_assert(LDS_DMA_BYTES + LDS_SC_BYTES <= 160 * 1024, "lds");

constexpr unsigned kSpinCap = 1u << 18;
constexpr unsigned SENT = 0xFFFFFFFFu;

__device__ __forceinline__ float bf2f(unsigned short u) { return __uint_as_float(((unsigned)u) << 16); }
__device__ __forceinline__ float bflo(unsigned u) { return __uint_as_float(u << 16); }
__device__ __forceinline__ float bfhi(unsigned u) { return __uint_as_float(u & 0xffff0000u); }
__device__ __forceinline__ unsigned f2bf(float f) { unsigned u = __float_as_uint(f); return (u + 0x7fffu + ((u >> 16) & 1u)) >> 16; }
__device__ __forceinline__ unsigned pack2(float a, float b) { const f32x2 v = {a, b}; return __builtin_bit_cast(unsigned, __builtin_convertvector(v, bf16x2_t)); }
template <int C> __device__ __forceinline__ float dppf(float v) { return __int_as_float(__builtin_amdgcn_update_dpp(0, __float_as_int(v), C, 0xF, 0xF, true)); }
__device__ __forceinline__ float add16(float v) { auto r = __builtin_amdgcn_permlane16_swap(__float_as_uint(v), __float_as_uint(v), false, false); return __uint_as_float(r[0]) + __uint_as_float(r[1]); }
__device__ __forceinline__ float add32(float v) { auto r = __builtin_amdgcn_permlane32_swap(__float_as_uint(v), __float_as_uint(v), false, false); return __uint_as_float(r[0]) + __uint_as_float(r[1]); }
__device__ __forceinline__ float max16(float v) { auto r = __builtin_amdgcn_permlane16_swap(__float_as_uint(v), __float_as_uint(v), false, false); return fmaxf(__uint_as_float(r[0]), __uint_as_float(r[1])); }
__device__ __forceinline__ float max32(float v) { auto r = __builtin_amdgcn_permlane32_swap(__float_as_uint(v), __float_as_uint(v), false, false); return fmaxf(__uint_as_float(r[0]), __uint_as_float(r[1])); }
__device__ __forceinline__ float add_hi3(float v) { v += dppf<0x128>(v); v = add16(v); return add32(v); }
__device__ __forceinline__ float max_hi3(float v) { v = fmaxf(v, dppf<0x128>(v)); v = max16(v); return max32(v); }
__device__ __forceinline__ float add_lo5(float v) { v += dppf<0xB1>(v); v += dppf<0x4E>(v); v += dppf<0x141>(v); v += dppf<0x140>(v); return add16(v); }
__device__ __forceinline__ float max_lo5(float v) { v = fmaxf(v, dppf<0xB1>(v)); v = fmaxf(v, dppf<0x4E>(v)); v = fmaxf(v, dppf<0x141>(v)); v = fmaxf(v, dppf<0x140>(v)); return max16(v); }
__device__ __forceinline__ float wsum(float v) { return add32(add_lo5(v)); }
__device__ __forceinline__ unsigned long long rt() { return __builtin_amdgcn_s_memrealtime(); }

__global__ void __launch_bounds__(256) k1(const unsigned short* __restrict__ x, const unsigned short* __restrict__ wq,
                                          const unsigned short* __restrict__ w1, unsigned char* __restrict__ ws) {
    __shared__ float xn[2][M];
    __shared__ float red[4][2][64];
    const int tid = threadIdx.x, wave = tid >> 6, lane = tid & 63;
    const int blk = blockIdx.x;
    const bool isq = blk < 256;
    const int idx = isq ? blk : blk - 256;
    const int mq = idx & 3, cg = idx >> 2;
    const int ncols = isq ? 4096 : F;
    const unsigned short* W = (isq ? wq : w1) + (size_t)(mq * 112) * ncols + cg * 64;
    const int r = tid >> 3, cch = tid & 7;
    unsigned short xr[7];
#pragma unroll
    for (int i = 0; i < 7; ++i) xr[i] = x[(wave & 1) * M + lane + 64 * i];
    PIN();
    v4u wv[4];
#pragma unroll
    for (int i = 0; i < 4; ++i) {
        const int row = (i < 3 || r < 16) ? r + 32 * i : 111;
        wv[i] = __builtin_nontemporal_load((const v4u*)(W + (size_t)row * ncols + cch * 8));
    }
    PIN();
    {
        const v4u sn = {SENT, SENT, SENT, SENT};
        v4u* g = (v4u*)ws;
        const unsigned tot16 = (unsigned)(FILL_BYTES / 16);
        for (unsigned i = blk * 256 + tid; i < tot16; i += gridDim.x * 256) g[i] = sn;
    }
    if (wave < 2) {
        float v[7]; float s = 0.f;
#pragma unroll
        for (int i = 0; i < 7; ++i) { v[i] = bf2f(xr[i]); s += v[i]; }
        s = wsum(s); const float mean = s * (1.0f / M);
        float ss = 0.f;
#pragma unroll
        for (int i = 0; i < 7; ++i) { const float d = v[i] - mean; ss += d * d; }
        ss = wsum(ss);
        const float rs = 1.0f / sqrtf(ss * (1.0f / M) + 1e-5f);
#pragma unroll
        for (int i = 0; i < 7; ++i) xn[wave][lane + 64 * i] = (v[i] - mean) * rs;
    }
    __syncthreads();
    float a[2][8];
#pragma unroll
    for (int j = 0; j < 8; ++j) { a[0][j] = 0.f; a[1][j] = 0.f; }
#pragma unroll
    for (int i = 0; i < 4; ++i) {
        int row = mq * 112 + r + 32 * i; if (row >= M) row = M - 1;
        const bool ok = (i < 3 || r < 16);
        const float x0 = ok ? xn[0][row] : 0.f, x1 = ok ? xn[1][row] : 0.f;
#pragma unroll
        for (int j = 0; j < 4; ++j) {
            const float lo = bflo(wv[i][j]), hi = bfhi(wv[i][j]);
            a[0][2 * j] += x0 * lo; a[0][2 * j + 1] += x0 * hi;
            a[1][2 * j] += x1 * lo; a[1][2 * j + 1] += x1 * hi;
        }
    }
#pragma unroll
    for (int b = 0; b < 2; ++b)
#pragma unroll
        for (int j = 0; j < 8; ++j) a[b][j] = add_hi3(a[b][j]);
    if (lane < 8) {
#pragma unroll
        for (int b = 0; b < 2; ++b)
#pragma unroll
            for (int j = 0; j < 8; ++j) red[wave][b][lane * 8 + j] = a[b][j];
    }
    __syncthreads();
    if (tid < 128) {
        const int b = tid >> 6, c = tid & 63;
        const float s = red[0][b][c] + red[1][b][c] + red[2][b][c] + red[3][b][c];
        float* dst = (float*)(ws + (isq ? QP_OFF : HP_OFF));
        dst[((size_t)mq * 2 + b) * ncols + cg * 64 + c] = s;
    }
}

__device__ __forceinline__ bool spin_fail(unsigned& spins, gu32* tmo) {
    __builtin_amdgcn_s_sleep(1);
    if (++spins > kSpinCap) { __hip_atomic_store(tmo, 1u, RLX_AGENT); return true; }
    return (spins & 1023u) == 0u && __builtin_amdgcn_readfirstlane(__hip_atomic_load(tmo, RLX_AGENT)) == 1u;
}
__device__ __forceinline__ bool has_sent(v4u v) { return v[0] == SENT || v[1] == SENT || v[2] == SENT || v[3] == SENT; }
__device__ __forceinline__ unsigned canon(unsigned u) { return u == SENT ? 0x7FC00000u : u; }
__device__ __forceinline__ unsigned canonb(unsigned u) { return u == SENT ? 0xFFC0FFC0u : u; }

#define GLDS_NT(src, dst) __builtin_amdgcn_global_load_lds((const unsigned*)(src), (LAS unsigned*)(dst), 16, 0, 2)
#define BAR() __builtin_amdgcn_s_barrier()
#define LGKM0() asm volatile("s_waitcnt lgkmcnt(0)" ::: "memory")
#define VM0() asm volatile("s_waitcnt vmcnt(0)" ::: "memory")
#define STAMP(i) do {} while (0)
#define STAMPW(i, w) do {} while (0)

#define WUV_ROW(i) (64 * cs + 8 * wave + 2 * (i) + (lane >> 5))
#define STAGE2_LOADS() do { \
        _Pragma("unroll") for (int i = 0; i < 4; ++i) \
            wuvv[i] = __builtin_nontemporal_load((const v4u*)(wuv + ((size_t)WUV_ROW(i) * H + hh) * D + (lane & 31) * 8)); \
        const unsigned short* so = wo + ((size_t)hh * D + cs * 16) * M; \
        const int t2 = tid < 448 ? tid : 447; \
        wov[0] = __builtin_nontemporal_load((const v4u*)(so + (size_t)t2 * 8)); \
        wov[1] = __builtin_nontemporal_load((const v4u*)(so + (size_t)(448 + t2) * 8)); \
        w2v = __builtin_nontemporal_load((const v4u*)(w2 + (size_t)cu * 7 * M + (size_t)(tid < 392 ? tid : 391) * 8)); \
    } while (0)
#define STAGE2_LOADS_ASM() do { \
        _Pragma("unroll") for (int i = 0; i < 4; ++i) \
            asm volatile("global_load_dwordx4 %0, %1, off nt" : "=v"(wuvv[i]) : "v"(wuv + ((size_t)WUV_ROW(i) * H + hh) * D + (lane & 31) * 8) : "memory"); \
        const unsigned short* so = wo + ((size_t)hh * D + cs * 16) * M; \
        const int t2 = tid < 448 ? tid : 447; \
        asm volatile("global_load_dwordx4 %0, %1, off nt" : "=v"(wov[0]) : "v"(so + (size_t)t2 * 8) : "memory"); \
        asm volatile("global_load_dwordx4 %0, %1, off nt" : "=v"(wov[1]) : "v"(so + (size_t)(448 + t2) * 8) : "memory"); \
        asm volatile("global_load_dwordx4 %0, %1, off nt" : "=v"(w2v) : "v"(w2 + (size_t)cu * 7 * M + (size_t)(tid < 392 ? tid : 391) * 8) : "memory"); \
    } while (0)
__global__ void __launch_bounds__(512, 1) k2(const unsigned short* __restrict__ x, const unsigned short* __restrict__ ckv,
                                             const unsigned short* __restrict__ wuk, const unsigned short* __restrict__ wuv,
                                             const unsigned short* __restrict__ wo, const unsigned short* __restrict__ b1,
                                             const unsigned short* __restrict__ w2, const unsigned short* __restrict__ b2,
                                             unsigned char* __restrict__ ws, unsigned short* __restrict__ out) {
    __shared__ __attribute__((aligned(16))) unsigned char lds_dma[LDS_DMA_BYTES];
    __shared__ __attribute__((aligned(16))) unsigned char lds_sc[LDS_SC_BYTES];
    LAS unsigned char* L = (LAS unsigned char*)lds_dma;
    LAS unsigned char* S = (LAS unsigned char*)lds_sc;
    const int tid = threadIdx.x, wave = __builtin_amdgcn_readfirstlane(tid >> 6), lane = tid & 63;
    const int cu = blockIdx.x;
    const int hh = cu >> 4, cs = cu & 15;
    const int bb = cu >> 7, sp = cu & 127;
    gu32* tmo = (gu32*)(ws + TMO_OFF);
    const __amdgpu_buffer_rsrc_t rs_ws = __builtin_amdgcn_make_buffer_rsrc(ws, 0, (int)WS_NEED, 0x00020000);
    STAMP(0);

    float qp[4];
    {
        const float* p = (const float*)(ws + QP_OFF) + (size_t)(tid >> 8) * 4096 + hh * D + (tid & 255);
#pragma unroll
        for (int i = 0; i < 4; ++i) asm volatile("global_load_dword %0, %1, off" : "=v"(qp[i]) : "v"(p + (size_t)i * 8192));
    }
    v4u wk[8];
    {
        const unsigned short* src = ckv + ((size_t)bb * SK + (size_t)sp * KPS + 4 * wave) * DC;
        if (wave < 4) {
            const unsigned short* p = wuk + ((size_t)(64 * cs + 16 * wave + (lane & 15)) * H + hh) * D + 8 * (lane >> 4);
#pragma unroll
            for (int ks = 0; ks < 8; ++ks) asm volatile("global_load_dwordx4 %0, %1, off nt" : "=v"(wk[ks]) : "v"(p + 32 * ks));
        }
        if (CKVPOS == 0 || (CKVPOS == 2 && wave >= 4)) {
#pragma unroll
            for (int i = 0; i < 8; ++i) GLDS_NT(src + (size_t)i * 512 + lane * 8, L + L_CKV + (4 * wave + (i >> 1)) * CKV_STRIDE + (i & 1) * 1024);
        }
    }
    PIN();
    if (wave < 4) asm volatile("s_waitcnt vmcnt(16)" ::: "memory");
    else asm volatile("s_waitcnt vmcnt(8)" ::: "memory");
    asm volatile("" : "+v"(qp[0]), "+v"(qp[1]), "+v"(qp[2]), "+v"(qp[3]));
    {
        float s = (qp[0] + qp[1]) + (qp[2] + qp[3]);
        s *= 0.0625f * 1.4426950408889634f;
        const unsigned hi = f2bf(s);
        const unsigned lo = f2bf(s - __uint_as_float(hi << 16));
        const int b = tid >> 8, d = tid & 255;
        ((LAS unsigned short*)(S + L_QA))[(2 * b) * 256 + d] = (unsigned short)hi;
        ((LAS unsigned short*)(S + L_QA))[(2 * b + 1) * 256 + d] = (unsigned short)lo;
    }
    LGKM0();
    BAR();
    STAMP(1);
    if (wave < 4) {
        v4u av[8];
#pragma unroll
        for (int ks = 0; ks < 8; ++ks) {
            av[ks] = *(const LAS v4u*)(S + L_QA + (lane & 3) * 512 + (32 * ks + 8 * (lane >> 4)) * 2);
        }
        const bool keep = (lane & 15) < 4;
        asm volatile("s_waitcnt vmcnt(8)" : "+v"(wk[0]), "+v"(wk[1]), "+v"(wk[2]), "+v"(wk[3]), "+v"(wk[4]), "+v"(wk[5]), "+v"(wk[6]), "+v"(wk[7]));
        f32x4 acc = {0.f, 0.f, 0.f, 0.f};
#pragma unroll
        for (int ks = 0; ks < 8; ++ks) {
            v4u a = av[ks];
            a[0] = keep ? a[0] : 0u; a[1] = keep ? a[1] : 0u; a[2] = keep ? a[2] : 0u; a[3] = keep ? a[3] : 0u;
            acc = __builtin_amdgcn_mfma_f32_16x16x32_bf16(__builtin_bit_cast(bf16x8, a), __builtin_bit_cast(bf16x8, wk[ks]), acc, 0, 0, 0);
        }
        LAS unsigned short* qs = (LAS unsigned short*)(S + L_QS) + wave * 16;
        if (lane < 16) {
            const float v0 = acc[0] + acc[1], v1 = acc[2] + acc[3];
            const unsigned h0 = f2bf(v0), h1 = f2bf(v1);
            qs[lane] = (unsigned short)h0;
            qs[64 + lane] = (unsigned short)f2bf(v0 - __uint_as_float(h0 << 16));
            qs[128 + lane] = (unsigned short)h1;
            qs[192 + lane] = (unsigned short)f2bf(v1 - __uint_as_float(h1 << 16));
        }
        LGKM0();
    }
    BAR();
    float xb = 0.f;
    {
        const int b = (tid >> 3) & 1, m = (cu < NC5 ? cu : 0) * 8 + (tid & 7);
        xb = bf2f(x[b * M + m]) + bf2f(b2[m]);
    }
    STAMP(2);
    PIN();
    if (tid < 16 * (1 + QHL)) {
        v4u v = *(const LAS v4u*)(S + L_QS + lane * 16);
        v[0] = canonb(v[0]); v[1] = canonb(v[1]); v[2] = canonb(v[2]); v[3] = canonb(v[3]);
        __builtin_amdgcn_raw_buffer_store_b128(v, rs_ws, (int)(QABS_OFF + ((((size_t)(lane >> 4) * H + hh) * 2 + ((lane >> 3) & 1)) * DC + 64 * cs + 8 * (lane & 7)) * 2), 0, 16);
    } else if (wave == 4 && lane < 14) {
        const int b = lane / 7, r = lane % 7, f = cu * 7 + r;
        const float* hp = (const float*)(ws + HP_OFF);
        float hv = bf2f(b1[f]);
#pragma unroll
        for (int p = 0; p < 4; ++p) hv += hp[((size_t)p * 2 + b) * F + f];
        const float t = tanhf(0.7978845608028654f * (hv + 0.044715f * hv * hv * hv));
        ((LAS float*)(S + L_HM))[b * 8 + r] = 0.5f * hv * (1.0f + t);
    }
    PIN();
    v4u wuvv[4], wov[2], w2v;
    const int g4 = lane >> 4;
    if (wave == 0) asm volatile("s_waitcnt vmcnt(1)" ::: "memory");
    else VM0();
    STAMPW(13, 7);
    BAR();
    v4u kf[8]; v2u vf[16];
    {
#pragma unroll
        for (int nt = 0; nt < 2; ++nt)
#pragma unroll
            for (int i = 0; i < 4; ++i)
                kf[nt * 4 + i] = *(const LAS v4u*)(L + L_CKV + (16 * nt + (lane & 15)) * CKV_STRIDE + (128 * wave + 32 * i + 8 * g4) * 2);
        const int q = (lane & 15) >> 2, p = lane & 3;
#pragma unroll
        for (int t = 0; t < 8; ++t) {
            const int c0 = 128 * wave + 16 * t;
            vf[2 * t] = __builtin_bit_cast(v2u, __builtin_amdgcn_ds_read_tr16_b64_v4i16((LAS s16x4*)(L + L_CKV + (8 * g4 + q) * CKV_STRIDE + c0 * 2 + 8 * p)));
            vf[2 * t + 1] = __builtin_bit_cast(v2u, __builtin_amdgcn_ds_read_tr16_b64_v4i16((LAS s16x4*)(L + L_CKV + (8 * g4 + 4 + q) * CKV_STRIDE + c0 * 2 + 8 * p)));
        }
        asm volatile("" : "+v"(kf[0]), "+v"(kf[1]), "+v"(kf[2]), "+v"(kf[3]), "+v"(kf[4]), "+v"(kf[5]), "+v"(kf[6]), "+v"(kf[7]));
        asm volatile("" : "+v"(vf[0]), "+v"(vf[1]), "+v"(vf[2]), "+v"(vf[3]), "+v"(vf[4]), "+v"(vf[5]), "+v"(vf[6]), "+v"(vf[7]));
        asm volatile("" : "+v"(vf[8]), "+v"(vf[9]), "+v"(vf[10]), "+v"(vf[11]), "+v"(vf[12]), "+v"(vf[13]), "+v"(vf[14]), "+v"(vf[15]));
    }
    PIN();
    constexpr int NQ = 4 * (1 + QHL);
    const int qbase = (int)(QABS_OFF + (((size_t)bb * H + (lane & 15)) * (1 + QHL) * DC + 128 * wave) * 2);
    bf16x8 qa[NQ];
    {
        v4u v[NQ];
#pragma unroll
        for (int i = 0; i < NQ; ++i) v[i] = (v4u){SENT, SENT, SENT, SENT};
        SDLY(E2D);
        for (unsigned spins = 0;;) {
#pragma unroll
            for (int i = 0; i < NQ; ++i)
                if (has_sent(v[i])) v[i] = __builtin_amdgcn_raw_buffer_load_b128(rs_ws, qbase + (i >> 2) * (DC * 2) + (32 * (i & 3) + 8 * g4) * 2, 0, 16);
            bool bad = false;
#pragma unroll
            for (int i = 0; i < NQ; ++i) bad |= has_sent(v[i]);
            if (!__any(bad)) break;
            if (spin_fail(spins, tmo)) break;
        }
#pragma unroll
        for (int i = 0; i < NQ; ++i) qa[i] = __builtin_bit_cast(bf16x8, v[i]);
    }
    STAMP(3);
    STAMPW(14, 7);
    STAMP(10);
    {
        f32x4 acc[2] = {{0.f, 0.f, 0.f, 0.f}, {0.f, 0.f, 0.f, 0.f}};
#pragma unroll
        for (int i = 0; i < NQ; ++i)
#pragma unroll
            for (int nt = 0; nt < 2; ++nt) {
                const v4u v = kf[nt * 4 + (i & 3)];
                acc[nt] = __builtin_amdgcn_mfma_f32_16x16x32_bf16(qa[i], __builtin_bit_cast(bf16x8, v), acc[nt], 0, 0, 0);
            }
        LAS float* sc = (LAS float*)(S + L_SC);
#pragma unroll
        for (int nt = 0; nt < 2; ++nt)
#pragma unroll
            for (int r = 0; r < 4; ++r) sc[(wave * 16 + 4 * g4 + r) * 32 + 16 * nt + (lane & 15)] = acc[nt][r];
    }
    PIN();
    STAGE2_LOADS();
    PIN();
    LGKM0();
    BAR();
    float st_m, st_l;
    {
        const int h = tid >> 5, key = tid & 31;
        const LAS float* sc = (const LAS float*)(S + L_SC);
        float s = 0.f;
#pragma unroll
        for (int w = 0; w < 8; ++w) s += sc[(w * 16 + h) * 32 + key];
        float mx = s;
        mx = max_lo5(mx);
        const unsigned pb = f2bf(__builtin_amdgcn_exp2f(s - mx));
        float l = __uint_as_float(pb << 16);
        l = add_lo5(l);
        ((LAS unsigned short*)(S + L_P))[h * 32 + key] = (unsigned short)pb;
        st_m = mx; st_l = l;
        if (key == 0) ((LAS v2u*)(S + L_ML))[h] = (v2u){canon(__float_as_uint(mx)), canon(__float_as_uint(l))};
    }
    LGKM0();
    BAR();
    v4u fv;
    {
        const bf16x8 pf = __builtin_bit_cast(bf16x8, *(const LAS v4u*)(S + L_P + (lane & 15) * 64 + g4 * 16));
        fv = *(const LAS v4u*)(S + L_ML + (lane & 7) * 16);
        v2u o2[8];
#pragma unroll
        for (int t = 0; t < 8; ++t) {
            const v4u av = {vf[2 * t][0], vf[2 * t][1], vf[2 * t + 1][0], vf[2 * t + 1][1]};
            const bf16x8 af = __builtin_bit_cast(bf16x8, av);
            f32x4 o = {0.f, 0.f, 0.f, 0.f};
            o = __builtin_amdgcn_mfma_f32_16x16x32_bf16(af, pf, o, 0, 0, 0);
            o2[t][0] = pack2(o[0], o[1]); o2[t][1] = pack2(o[2], o[3]);
        }
#pragma unroll
        for (int t = 0; t < 8; ++t) *(LAS v2u*)(L + L_ST + (lane & 15) * CKV_STRIDE + (128 * wave + 16 * t + 4 * g4) * 2) = o2[t];
    }
    LGKM0();
    STAMP(15);
    {
#pragma unroll
        for (int i = 0; i < 4; ++i) {
            const int h = 4 * i + g4;
            const v4u v = *(const LAS v4u*)(L + L_ST + h * CKV_STRIDE + 256 * wave + (lane & 15) * 16);
            __builtin_amdgcn_raw_buffer_store_b128(v, rs_ws, (int)(PCTX_OFF + ((((size_t)bb * NSPLIT + sp) * H + h) * DC) * 2 + 256 * wave + (lane & 15) * 16), 0, PAUX);
        }
        VM0();
        if (lane < 8) __builtin_amdgcn_raw_buffer_store_b128(fv, rs_ws, (int)(FLG_OFF + ((size_t)cu * 8 + wave) * FLGS + lane * 16), 0, 16);
    }
    STAMP(4);
    if (tid < 448) { *(LAS v4u*)(S + L_WO + tid * 16) = wov[0]; *(LAS v4u*)(S + L_WO + (448 + tid) * 16) = wov[1]; }
    if (tid < 392) *(LAS v4u*)(S + L_W2 + tid * 16) = w2v;
    {
        const int j = lane >> 3, ch = lane & 7;
        v4u pc[4]; v2u ml[4];
        const int fo = (cs >> 1) * (int)FLGS + hh * 8;
        SDLY(E3D);
        for (unsigned spins = 0;;) {
            bool bad = false;
#pragma unroll
            for (int i = 0; i < 4; ++i) {
                const int b = i >> 1, s = 16 * wave + (i & 1) * 8 + j;
                ml[i] = __builtin_amdgcn_raw_buffer_load_b64(rs_ws, (int)(FLG_OFF + ((size_t)b * 128 + s) * 8 * FLGS) + fo, 0, 16);
            }
#pragma unroll
            for (int i = 0; i < 4; ++i) bad |= (ml[i][0] == SENT) || (ml[i][1] == SENT);
            if (!__any(bad)) break;
            if (spin_fail(spins, tmo)) break;
        }
        if (!E2ST) STAMP(5);
#pragma unroll
        for (int i = 0; i < 4; ++i) {
            const int b = i >> 1, s = 16 * wave + (i & 1) * 8 + j;
            pc[i] = __builtin_amdgcn_raw_buffer_load_b128(rs_ws, (int)(PCTX_OFF + ((((size_t)b * NSPLIT + s) * H + hh) * DC + 64 * cs + ch * 8) * 2), 0, PLAUX);
        }
        LAS float* red = (LAS float*)(S + L_SC);
#pragma unroll
        for (int b = 0; b < 2; ++b) {
            const float m0 = __uint_as_float(ml[2 * b][0]), l0 = __uint_as_float(ml[2 * b][1]);
            const float m1 = __uint_as_float(ml[2 * b + 1][0]), l1 = __uint_as_float(ml[2 * b + 1][1]);
            float gm = fmaxf(m0, m1);
            gm = max_hi3(gm);
            const float e0 = __builtin_amdgcn_exp2f(m0 - gm), e1 = __builtin_amdgcn_exp2f(m1 - gm);
            float ls = e0 * l0 + e1 * l1;
            ls = add_hi3(ls);
            float acc[8];
#pragma unroll
            for (int k = 0; k < 4; ++k) {
                acc[2 * k] = e0 * bflo(pc[2 * b][k]) + e1 * bflo(pc[2 * b + 1][k]);
                acc[2 * k + 1] = e0 * bfhi(pc[2 * b][k]) + e1 * bfhi(pc[2 * b + 1][k]);
            }
#pragma unroll
            for (int k = 0; k < 8; ++k) acc[k] = add_hi3(acc[k]);
            if (lane < 8) {
                *(LAS f32x4*)(red + (wave * 2 + b) * 64 + lane * 8) = (f32x4){acc[0], acc[1], acc[2], acc[3]};
                *(LAS f32x4*)(red + (wave * 2 + b) * 64 + lane * 8 + 4) = (f32x4){acc[4], acc[5], acc[6], acc[7]};
            }
            if (lane == 0) ((LAS f32x2*)(S + L_STAT))[wave * 2 + b] = (f32x2){gm, ls};
        }
    }
    STAMP(11);
    LGKM0();
    BAR();
    STAMP(12);
    if (tid < 128) {
        const int b = tid >> 6;
        const LAS float* red = (const LAS float*)(S + L_SC);
        const LAS f32x2* st = (const LAS f32x2*)(S + L_STAT);
        float gm = st[b][0];
#pragma unroll
        for (int w = 1; w < 8; ++w) gm = fmaxf(gm, st[w * 2 + b][0]);
        float s = 0.f, ls = 0.f;
#pragma unroll
        for (int w = 0; w < 8; ++w) {
            const f32x2 t = st[w * 2 + b];
            const float e = __builtin_amdgcn_exp2f(t[0] - gm);
            ls += e * t[1]; s += e * red[w * 128 + tid];
        }
        ((LAS float*)(S + L_CC))[tid] = s / ls;
    }
    LGKM0();
    BAR();
    {
        f32x2 a[8];
#pragma unroll
        for (int j = 0; j < 8; ++j) a[j] = (f32x2){0.f, 0.f};
#pragma unroll
        for (int i = 0; i < 4; ++i) {
            const v4u w = wuvv[i];
            const LAS float* cc = (const LAS float*)(S + L_CC);
            const int c = 8 * wave + 2 * i + (lane >> 5);
            const f32x2 cv = {cc[c], cc[64 + c]};
#pragma unroll
            for (int j = 0; j < 4; ++j) { a[2 * j] += cv * bflo(w[j]); a[2 * j + 1] += cv * bfhi(w[j]); }
        }
#pragma unroll
        for (int j = 0; j < 8; ++j) { a[j][0] = add32(a[j][0]); a[j][1] = add32(a[j][1]); }
        if (lane < 32) {
            LAS f32x4* red = (LAS f32x4*)(S + L_SC + 4096);
#pragma unroll
            for (int b = 0; b < 2; ++b) {
                red[((wave * 2 + b) * 256 + lane * 8) / 4] = (f32x4){a[0][b], a[1][b], a[2][b], a[3][b]};
                red[((wave * 2 + b) * 256 + lane * 8) / 4 + 1] = (f32x4){a[4][b], a[5][b], a[6][b], a[7][b]};
            }
        }
    }
    LGKM0();
    BAR();
    if (tid < 128) {
        const LAS f32x4* red = (const LAS f32x4*)(S + L_SC + 4096);
        const int b = (tid >> 2) & 1, d = (tid >> 3) * 16 + (tid & 3) * 4;
        const int ridx = b * 64 + (d >> 2);
        f32x4 s = red[ridx];
#pragma unroll
        for (int w = 1; w < 8; ++w) s += red[w * 128 + ridx];
        v4u v; v[0] = canon(__float_as_uint(s[0])); v[1] = canon(__float_as_uint(s[1])); v[2] = canon(__float_as_uint(s[2])); v[3] = canon(__float_as_uint(s[3]));
        __builtin_amdgcn_raw_buffer_store_b128(v, rs_ws, (int)(G4_OFF + ((((size_t)hh * 16 + (d >> 4)) * 16 + cs) * 32 + b * 16 + (d & 15)) * 4), 0, 16);
        if (!E2ST) STAMP(6);
        v4u g;
        SDLY(E4D);
        for (unsigned spins = 0;;) {
            g = __builtin_amdgcn_raw_buffer_load_b128(rs_ws, (int)(G4_OFF + (size_t)(hh * 16 + cs) * 2048 + tid * 16), 0, 16);
            if (!__any(has_sent(g))) break;
            if (spin_fail(spins, tmo)) break;
        }
        f32x4 t; t[0] = __uint_as_float(g[0]); t[1] = __uint_as_float(g[1]); t[2] = __uint_as_float(g[2]); t[3] = __uint_as_float(g[3]);
        t[0] = add_hi3(t[0]); t[1] = add_hi3(t[1]); t[2] = add_hi3(t[2]); t[3] = add_hi3(t[3]);
        if (lane < 8) *(LAS f32x4*)(S + L_X6 + wave * 128 + lane * 16) = t;
        if (!E2ST) STAMP(7);
    }
    LGKM0();
    BAR();
    if (tid < 224) {
        const int j = tid >> 1, b = tid & 1;
        const LAS f32x4* x6 = (const LAS f32x4*)(S + L_X6);
        const LAS float* hm = (const LAS float*)(S + L_HM) + b * 8;
        f32x4 a = {0.f, 0.f, 0.f, 0.f};
#pragma unroll
        for (int q4 = 0; q4 < 4; ++q4) {
            const f32x4 cx = x6[b * 4 + q4] + x6[8 + b * 4 + q4];
#pragma unroll
            for (int k = 0; k < 4; ++k) {
                const v2u w = *(const LAS v2u*)(S + L_WO + (q4 * 4 + k) * (M * 2) + j * 8);
                const float c = cx[k];
                a[0] += c * bflo(w[0]); a[1] += c * bfhi(w[0]); a[2] += c * bflo(w[1]); a[3] += c * bfhi(w[1]);
            }
        }
#pragma unroll
        for (int r = 0; r < 7; ++r) {
            const v2u w = *(const LAS v2u*)(S + L_W2 + r * (M * 2) + j * 8);
            const float c = hm[r];
            a[0] += c * bflo(w[0]); a[1] += c * bfhi(w[0]); a[2] += c * bflo(w[1]); a[3] += c * bfhi(w[1]);
        }
        v4u v; v[0] = canon(__float_as_uint(a[0])); v[1] = canon(__float_as_uint(a[1])); v[2] = canon(__float_as_uint(a[2])); v[3] = canon(__float_as_uint(a[3]));
        __builtin_amdgcn_raw_buffer_store_b128(v, rs_ws, (int)(G5_OFF + (((size_t)(j >> 1) * 256 + cu) * 16 + b * 8 + (j & 1) * 4) * 4), 0, 16);
    }
    if (!E2ST) STAMP(8);
    if (cu < NC5) {
        v4u v0 = {SENT, SENT, SENT, SENT}, v1 = {SENT, SENT, SENT, SENT};
        SDLY(E5D);
        for (unsigned spins = 0;;) {
            if (has_sent(v0)) v0 = __builtin_amdgcn_raw_buffer_load_b128(rs_ws, (int)(G5_OFF + (size_t)cu * 16384 + tid * 16), 0, 16);
            if (has_sent(v1)) v1 = __builtin_amdgcn_raw_buffer_load_b128(rs_ws, (int)(G5_OFF + (size_t)cu * 16384 + 8192 + tid * 16), 0, 16);
            if (!__any(has_sent(v0) || has_sent(v1))) break;
            if (spin_fail(spins, tmo)) break;
        }
        f32x4 s; s[0] = __uint_as_float(v0[0]) + __uint_as_float(v1[0]); s[1] = __uint_as_float(v0[1]) + __uint_as_float(v1[1]);
        s[2] = __uint_as_float(v0[2]) + __uint_as_float(v1[2]); s[3] = __uint_as_float(v0[3]) + __uint_as_float(v1[3]);
#pragma unroll
        for (int k = 0; k < 4; ++k) { float v = s[k]; v += dppf<0x124>(v); s[k] = add_hi3(v); }
        LAS float* red = (LAS float*)(S + L_CC);
        if (lane < 4) *(LAS f32x4*)(red + wave * 16 + lane * 4) = s;
        LGKM0();
        BAR();
        if (tid < 16) {
            float t = 0.f;
#pragma unroll
            for (int w = 0; w < 8; ++w) t += red[w * 16 + tid];
            const int b = tid >> 3, m = cu * 8 + (tid & 7);
            t += xb;
            out[b * M + m] = (unsigned short)f2bf(t);
        }
        STAMP(9);
    }
}
}

extern "C" void kernel_launch(void* const* d_in, const int* in_sizes, int n_in,
                              void* d_out, int out_size, void* d_ws, size_t ws_size,
                              hipStream_t stream) {
    const unsigned short* x = (const unsigned short*)d_in[0];
    const unsigned short* ckv = (const unsigned short*)d_in[1];
    const unsigned short* wq = (const unsigned short*)d_in[2];
    const unsigned short* wuk = (const unsigned short*)d_in[3];
    const unsigned short* wuv = (const unsigned short*)d_in[4];
    const unsigned short* wo = (const unsigned short*)d_in[5];
    const unsigned short* w1 = (const unsigned short*)d_in[7];
    const unsigned short* b1 = (const unsigned short*)d_in[8];
    const unsigned short* w2 = (const unsigned short*)d_in[9];
    const unsigned short* b2 = (const unsigned short*)d_in[10];
    unsigned char* ws = (unsigned char*)d_ws;
    k1<<<368, 256, 0, stream>>>(x, wq, w1, ws);
    k2<<<NCU, 512, 0, stream>>>(x, ckv, wuk, wuv, wo, b1, w2, b2, ws, (unsigned short*)d_out);
}
```

```cpp
#include <hip/hip_runtime.h>
#include <stdint.h>

#define GAS __attribute__((address_space(1)))
#define LAS __attribute__((address_space(3)))
typedef unsigned v4u __attribute__((ext_vector_type(4)));
typedef unsigned v2u __attribute__((ext_vector_type(2)));
typedef float f32x4 __attribute__((ext_vector_type(4)));
typedef float f32x2 __attribute__((ext_vector_type(2)));
typedef short bf16x8 __attribute__((ext_vector_type(8)));
typedef short s16x4 __attribute__((ext_vector_type(4)));
typedef __bf16 bf16x2_t __attribute__((ext_vector_type(2)));
typedef GAS unsigned gu32;
typedef GAS unsigned long long gu64;
#define RLX_AGENT __ATOMIC_RELAXED, __HIP_MEMORY_SCOPE_AGENT

#define STAMPS 0
#define FBAR 1
#define CKV 0
#define FST 0
#define S2M 0
#define PFD 2
#define K1PF 0
#define WIN 16
#define E2D2 0
#define E2D 8
#define E3D 8
#define SDLY(n) do { _Pragma("unroll") for (int i_ = 0; i_ < (n) / 4; ++i_) __builtin_amdgcn_s_sleep(16); if ((n) % 4) __builtin_amdgcn_s_sleep(4 * ((n) % 4)); } while (0)
#define PIN() __builtin_amdgcn_sched_barrier(0)

namespace {
constexpr int H = 16, SK = 4096, D = 256, M = 448, DC = 1024, F = 1792;
constexpr int NCU = 256;
constexpr int NSPLIT = 128, KPS = 32;
constexpr int NC5 = 56;

constexpr size_t FLG_OFF = 0;
constexpr size_t FLGS = 128;
constexpr size_t TMO_OFF = 256 * 8 * FLGS;
constexpr size_t QABS_OFF = TMO_OFF + 4096;
constexpr size_t G4_OFF = QABS_OFF + 131072;
constexpr size_t G4_BYTES = (size_t)256 * 512 * 4;
constexpr size_t G5_OFF = G4_OFF + G4_BYTES;
constexpr size_t G5_BYTES = (size_t)56 * 256 * 16 * 4;
constexpr size_t PCTX_OFF = G5_OFF + G5_BYTES;
constexpr size_t PCTX_BYTES = (size_t)2 * 128 * 16 * 1024 * 2;
constexpr size_t FILL_BYTES = PCTX_OFF;
constexpr size_t QP_OFF = PCTX_OFF + PCTX_BYTES;
constexpr size_t HP_OFF = QP_OFF + 4 * 2 * 4096 * 4;
constexpr size_t STAMP_OFF = HP_OFF + 65536;
constexpr size_t K1ST_OFF = STAMP_OFF + 256 * 16 * 8;
constexpr size_t WS_NEED = K1ST_OFF + 512 * 2 * 8;
static_assert(FILL_BYTES % 16 == 0, "fill");

constexpr int CKV_STRIDE = 2064;
constexpr int L_CKV = 0;
constexpr int L_WK = 32 * CKV_STRIDE;
constexpr int WK_STRIDE = 1040;
constexpr int LDS_DMA_BYTES = L_WK + 32 * WK_STRIDE;
constexpr int L_SC = 0;
constexpr int L_WO = L_SC + 20480;
constexpr int L_W2 = L_WO + 14336;
constexpr int L_QA = L_W2 + 7168;
constexpr int L_QS = L_QA + 2048;
constexpr int L_P = L_QS + 512;
constexpr int L_STAT = L_P + 1024;
constexpr int L_CC = L_STAT + 128;
constexpr int L_HM = L_CC + 512;
constexpr int L_X6 = L_HM + 128;
constexpr int L_ML = L_X6 + 256;
constexpr int L_ST = L_CKV;
constexpr int LDS_SC_BYTES = L_ML + 128;
static_assert(LDS_DMA_BYTES + LDS_SC_BYTES <= 160 * 1024, "lds");

constexpr unsigned kSpinCap = 1u << 18;
constexpr unsigned SENT = 0xFFFFFFFFu;

__device__ __forceinline__ float bf2f(unsigned short u) { return __uint_as_float(((unsigned)u) << 16); }
__device__ __forceinline__ float bflo(unsigned u) { return __uint_as_float(u << 16); }
__device__ __forceinline__ float bfhi(unsigned u) { return __uint_as_float(u & 0xffff0000u); }
__device__ __forceinline__ unsigned f2bf(float f) { unsigned u = __float_as_uint(f); return (u + 0x7fffu + ((u >> 16) & 1u)) >> 16; }
__device__ __forceinline__ unsigned pack2(float a, float b) { const f32x2 v = {a, b}; return __builtin_bit_cast(unsigned, __builtin_convertvector(v, bf16x2_t)); }
template <int C> __device__ __forceinline__ float dppf(float v) { return __int_as_float(__builtin_amdgcn_update_dpp(0, __float_as_int(v), C, 0xF, 0xF, true)); }
__device__ __forceinline__ float add16(float v) { auto r = __builtin_amdgcn_permlane16_swap(__float_as_uint(v), __float_as_uint(v), false, false); return __uint_as_float(r[0]) + __uint_as_float(r[1]); }
__device__ __forceinline__ float add32(float v) { auto r = __builtin_amdgcn_permlane32_swap(__float_as_uint(v), __float_as_uint(v), false, false); return __uint_as_float(r[0]) + __uint_as_float(r[1]); }
__device__ __forceinline__ float max16(float v) { auto r = __builtin_amdgcn_permlane16_swap(__float_as_uint(v), __float_as_uint(v), false, false); return fmaxf(__uint_as_float(r[0]), __uint_as_float(r[1])); }
__device__ __forceinline__ float max32(float v) { auto r = __builtin_amdgcn_permlane32_swap(__float_as_uint(v), __float_as_uint(v), false, false); return fmaxf(__uint_as_float(r[0]), __uint_as_float(r[1])); }
__device__ __forceinline__ float add_hi3(float v) { v += dppf<0x128>(v); v = add16(v); return add32(v); }
__device__ __forceinline__ float max_hi3(float v) { v = fmaxf(v, dppf<0x128>(v)); v = max16(v); return max32(v); }
__device__ __forceinline__ float add_lo5(float v) { v += dppf<0xB1>(v); v += dppf<0x4E>(v); v += dppf<0x141>(v); v += dppf<0x140>(v); return add16(v); }
__device__ __forceinline__ float max_lo5(float v) { v = fmaxf(v, dppf<0xB1>(v)); v = fmaxf(v, dppf<0x4E>(v)); v = fmaxf(v, dppf<0x141>(v)); v = fmaxf(v, dppf<0x140>(v)); return max16(v); }
__device__ __forceinline__ float wsum(float v) { return add32(add_lo5(v)); }
__device__ __forceinline__ unsigned long long rt() { return __builtin_amdgcn_s_memrealtime(); }

#define BAR() __builtin_amdgcn_s_barrier()
#define LGKM0() asm volatile("s_waitcnt lgkmcnt(0)" ::: "memory")
#define VM0() asm volatile("s_waitcnt vmcnt(0)" ::: "memory")
#define MFMA(a, b, c) __builtin_amdgcn_mfma_f32_16x16x32_bf16(__builtin_bit_cast(bf16x8, a), __builtin_bit_cast(bf16x8, b), c, 0, 0, 0)

__global__ void __launch_bounds__(K1PF ? 320 : 256) k1(const unsigned short* __restrict__ x, const unsigned short* __restrict__ wq,
                                          const unsigned short* __restrict__ w1, unsigned char* __restrict__ ws,
                                          const unsigned short* __restrict__ wuk) {
    __shared__ float xn[2][M];
    __shared__ float red[4][2][64];
    const int tid = threadIdx.x, wave = __builtin_amdgcn_readfirstlane(tid >> 6), lane = tid & 63;
    const int blk = blockIdx.x;
    const bool isq = blk < 256;
    const int idx = isq ? blk : blk - 256;
    const int mq = idx & 3, cg = idx >> 2;
    const int ncols = isq ? 4096 : F;
    const unsigned short* W = (isq ? wq : w1) + (size_t)(mq * 112) * ncols + cg * 64;
    const int r = tid >> 3, cch = tid & 7;
    unsigned short xr[7];
#pragma unroll
    for (int i = 0; i < 7; ++i) xr[i] = x[(wave & 1) * M + lane + 64 * i];
    PIN();
    v4u wv[4];
#pragma unroll
    for (int i = 0; i < 4; ++i) {
        const int row = (i < 3 || r < 16) ? r + 32 * i : 111;
        wv[i] = __builtin_nontemporal_load((const v4u*)(W + (size_t)row * ncols + cch * 8));
    }
    PIN();
    {
        const v4u sn = {SENT, SENT, SENT, SENT};
        v4u* g = (v4u*)ws;
        const unsigned tot16 = (unsigned)(FILL_BYTES / 16);
        for (unsigned i = blk * 256 + tid; i < tot16; i += gridDim.x * 256) g[i] = sn;
    }
    if (wave < 2) {
        float v[7]; float s = 0.f;
#pragma unroll
        for (int i = 0; i < 7; ++i) { v[i] = bf2f(xr[i]); s += v[i]; }
        s = wsum(s); const float mean = s * (1.0f / M);
        float ss = 0.f;
#pragma unroll
        for (int i = 0; i < 7; ++i) { const float d = v[i] - mean; ss += d * d; }
        ss = wsum(ss);
        const float rs = 1.0f / sqrtf(ss * (1.0f / M) + 1e-5f);
#pragma unroll
        for (int i = 0; i < 7; ++i) xn[wave][lane + 64 * i] = (v[i] - mean) * rs;
    }
    __syncthreads();
    float a[2][8];
#pragma unroll
    for (int j = 0; j < 8; ++j) { a[0][j] = 0.f; a[1][j] = 0.f; }
#pragma unroll
    for (int i = 0; i < 4; ++i) {
        int row = mq * 112 + r + 32 * i; if (row >= M) row = M - 1;
        const bool ok = (i < 3 || r < 16);
        const float x0 = ok ? xn[0][row] : 0.f, x1 = ok ? xn[1][row] : 0.f;
#pragma unroll
        for (int j = 0; j < 4; ++j) {
            const float lo = bflo(wv[i][j]), hi = bfhi(wv[i][j]);
            a[0][2 * j] += x0 * lo; a[0][2 * j + 1] += x0 * hi;
            a[1][2 * j] += x1 * lo; a[1][2 * j + 1] += x1 * hi;
        }
    }
#pragma unroll
    for (int b = 0; b < 2; ++b)
#pragma unroll
        for (int j = 0; j < 8; ++j) a[b][j] = add_hi3(a[b][j]);
    if (lane < 8) {
#pragma unroll
        for (int b = 0; b < 2; ++b)
#pragma unroll
            for (int j = 0; j < 8; ++j) red[wave][b][lane * 8 + j] = a[b][j];
    }
    __syncthreads();
    if (tid < 128) {
        const int b = tid >> 6, c = tid & 63;
        const float s = red[0][b][c] + red[1][b][c] + red[2][b][c] + red[3][b][c];
        float* dst = (float*)(ws + (isq ? QP_OFF : HP_OFF));
        dst[((size_t)mq * 2 + b) * ncols + cg * 64 + c] = s;
    }
}

__device__ __forceinline__ bool spin_fail(unsigned& spins, gu32* tmo) {
    __builtin_amdgcn_s_sleep(1);
    if (++spins > kSpinCap) { __hip_atomic_store(tmo, 1u, RLX_AGENT); return true; }
    return (spins & 1023u) == 0u && __builtin_amdgcn_readfirstlane(__hip_atomic_load(tmo, RLX_AGENT)) == 1u;
}
__device__ __forceinline__ bool has_sent(v4u v) { return v[0] == SENT || v[1] == SENT || v[2] == SENT || v[3] == SENT; }
__device__ __forceinline__ unsigned canon(unsigned u) { return u == SENT ? 0x7FC00000u : u; }
__device__ __forceinline__ unsigned canonb(unsigned u) { return u == SENT ? 0xFFC0FFC0u : u; }

#define GLDS_NT(src, dst) __builtin_amdgcn_global_load_lds((const unsigned*)(src), (LAS unsigned*)(dst), 16, 0, 2)
#define STAMP(i) do {} while (0)
#define STAMPW(i, w) do {} while (0)

#define WUV_ROW(i) (64 * cs + 8 * wave + 2 * (i) + (lane >> 5))
#define STAGE2_B() do { \
        _Pragma("unroll") for (int i = 0; i < 4; ++i) \
            asm volatile("global_load_dwordx4 %0, %1, off nt" : "=v"(wuvv[i]) : "v"(wuv + ((size_t)WUV_ROW(i) * H + hh) * D + (lane & 31) * 8) : "memory"); \
    } while (0)
#define STAGE2_A() do { \
        const unsigned short* so = wo + ((size_t)hh * D + cs * 16) * M; \
        const int t2 = tid < 448 ? tid : 447; \
        asm volatile("global_load_dwordx4 %0, %1, off nt" : "=v"(wov[0]) : "v"(so + (size_t)t2 * 8) : "memory"); \
        asm volatile("global_load_dwordx4 %0, %1, off nt" : "=v"(wov[1]) : "v"(so + (size_t)(448 + t2) * 8) : "memory"); \
        asm volatile("global_load_dwordx4 %0, %1, off nt" : "=v"(w2v) : "v"(w2 + (size_t)cu * 7 * M + (size_t)(tid < 392 ? tid : 391) * 8) : "memory"); \
    } while (0)
__global__ void __launch_bounds__(512, 1) k2(const unsigned short* __restrict__ x, const unsigned short* __restrict__ ckv,
                                             const unsigned short* __restrict__ wuk, const unsigned short* __restrict__ wuv,
                                             const unsigned short* __restrict__ wo, const unsigned short* __restrict__ b1,
                                             const unsigned short* __restrict__ w2, const unsigned short* __restrict__ b2,
                                             unsigned char* __restrict__ ws, unsigned short* __restrict__ out) {
    __shared__ __attribute__((aligned(16))) unsigned char lds_dma[LDS_DMA_BYTES];
    __shared__ __attribute__((aligned(16))) unsigned char lds_sc[LDS_SC_BYTES];
    LAS unsigned char* L = (LAS unsigned char*)lds_dma;
    LAS unsigned char* S = (LAS unsigned char*)lds_sc;
    const int tid = threadIdx.x, wave = __builtin_amdgcn_readfirstlane(tid >> 6), lane = tid & 63;
    const int cu = blockIdx.x;
    const int hh = cu >> 4, cs = cu & 15;
    const int bb = cu >> 7, sp = cu & 127;
    const int g4 = lane >> 4;
    gu32* tmo = (gu32*)(ws + TMO_OFF);
    const __amdgpu_buffer_rsrc_t rs_ws = __builtin_amdgcn_make_buffer_rsrc(ws, 0, (int)WS_NEED, 0x00020000);
    STAMP(0);

    float qp[4];
    {
        const float* p = (const float*)(ws + QP_OFF) + (size_t)(tid >> 8) * 4096 + hh * D + (tid & 255);
#pragma unroll
        for (int i = 0; i < 4; ++i) asm volatile("global_load_dword %0, %1, off" : "=v"(qp[i]) : "v"(p + (size_t)i * 8192) : "memory");
    }
    if (wave < 4) {
#pragma unroll
        for (int i = 0; i < 8; ++i) {
            const int r = 2 * i + (lane >> 5);
            const int ck = (lane & 31) ^ ((r & 7) << 2);
            GLDS_NT(wuk + ((size_t)(64 * cs + 16 * wave + r) * H + hh) * D + 8 * ck, L + L_WK + (8 * wave + i) * WK_STRIDE);
        }
    }
    PIN();
#define CKV_DMA() do { \
        const unsigned short* src = ckv + ((size_t)bb * SK + (size_t)sp * KPS + 4 * wave) * DC; \
        _Pragma("unroll") for (int i = 0; i < 8; ++i) GLDS_NT(src + (size_t)i * 512 + lane * 8, L + L_CKV + (4 * wave + (i >> 1)) * CKV_STRIDE + (i & 1) * 1024); \
    } while (0)
    BAR();
    PIN();
    CKV_DMA();
    PIN();
    if (wave < 4) asm volatile("s_waitcnt vmcnt(16)" ::: "memory");
    else asm volatile("s_waitcnt vmcnt(8)" ::: "memory");
    asm volatile("" : "+v"(qp[0]), "+v"(qp[1]), "+v"(qp[2]), "+v"(qp[3]));
    {
        float s = (qp[0] + qp[1]) + (qp[2] + qp[3]);
        s *= 0.0625f * 1.4426950408889634f;
        const unsigned hi = f2bf(s);
        const unsigned lo = f2bf(s - __uint_as_float(hi << 16));
        const int b = tid >> 8, d = tid & 255;
        ((LAS unsigned short*)(S + L_QA))[(2 * b) * 256 + d] = (unsigned short)hi;
        ((LAS unsigned short*)(S + L_QA))[(2 * b + 1) * 256 + d] = (unsigned short)lo;
    }
    LGKM0();
    BAR();
    STAMPW(1, 5);
    if (wave < 4) {
        v4u av[8], wk[8];
#pragma unroll
        for (int ks = 0; ks < 8; ++ks) av[ks] = *(const LAS v4u*)(S + L_QA + (lane & 3) * 512 + (32 * ks + 8 * g4) * 2);
        const bool keep = (lane & 15) < 4;
        asm volatile("s_waitcnt vmcnt(8)" ::: "memory");
        {
            const unsigned wa = (unsigned)(size_t)(L + L_WK + (8 * wave + ((lane & 15) >> 1)) * WK_STRIDE + (lane & 1) * 512 + g4 * 16);
#pragma unroll
            for (int ks = 0; ks < 8; ++ks) asm volatile("ds_read_b128 %0, %1" : "=v"(wk[ks]) : "v"(wa + ((ks ^ (lane & 7)) << 6)) : "memory");
            asm volatile("s_waitcnt lgkmcnt(0)" : "+v"(wk[0]), "+v"(wk[1]), "+v"(wk[2]), "+v"(wk[3]), "+v"(wk[4]), "+v"(wk[5]), "+v"(wk[6]), "+v"(wk[7]) :: "memory");
        }
        f32x4 acc = {0.f, 0.f, 0.f, 0.f};
#pragma unroll
        for (int ks = 0; ks < 8; ++ks) {
            v4u a = av[ks];
            a[0] = keep ? a[0] : 0u; a[1] = keep ? a[1] : 0u; a[2] = keep ? a[2] : 0u; a[3] = keep ? a[3] : 0u;
            acc = MFMA(a, wk[ks], acc);
        }
        LAS unsigned short* qs = (LAS unsigned short*)(S + L_QS) + wave * 64;
        if (lane < 16) {
            const float v0 = acc[0] + acc[1], v1 = acc[2] + acc[3];
            const unsigned h0 = f2bf(v0), h1 = f2bf(v1);
            qs[lane] = (unsigned short)h0;
            qs[16 + lane] = (unsigned short)f2bf(v0 - __uint_as_float(h0 << 16));
            qs[32 + lane] = (unsigned short)h1;
            qs[48 + lane] = (unsigned short)f2bf(v1 - __uint_as_float(h1 << 16));
        }
        LGKM0();
        if (lane < 8) {
            v4u v = *(const LAS v4u*)(S + L_QS + wave * 128 + lane * 16);
            v[0] = canonb(v[0]); v[1] = canonb(v[1]); v[2] = canonb(v[2]); v[3] = canonb(v[3]);
            __builtin_amdgcn_raw_buffer_store_b128(v, rs_ws, (int)QABS_OFF + (((((lane >> 2) * 8 + (cs >> 1)) * 8 + ((lane >> 1) & 1) * 4 + (cs & 1) * 2 + (wave >> 1)) * 64) + hh + 16 * ((wave & 1) * 2 + (lane & 1))) * 16, 0, 16);
        }
        STAMP(2);
    }
    PIN();
    BAR();
    SDLY(E2D);
    PIN();
    const int qbase = (int)QABS_OFF + (bb * 8 + wave) * 8192 + lane * 16;
    v4u qa[8];
#pragma unroll
    for (int i = 0; i < 8; ++i) qa[i] = __builtin_amdgcn_raw_buffer_load_b128(rs_ws, qbase + i * 1024, 0, 16);
    STAMPW(14, 7);
    asm volatile("s_waitcnt vmcnt(8)" ::: "memory");
    STAMPW(13, 7);
    STAMPW(11, 1);
    BAR();
    v4u kf[8];
#pragma unroll
    for (int nt = 0; nt < 2; ++nt)
#pragma unroll
        for (int i = 0; i < 4; ++i)
            kf[nt * 4 + i] = *(const LAS v4u*)(L + L_CKV + (16 * nt + (lane & 15)) * CKV_STRIDE + (128 * wave + 32 * i + 8 * g4) * 2);
    PIN();
    for (unsigned spins = 0;;) {
        bool bad = false;
#pragma unroll
        for (int i = 0; i < 8; ++i) bad |= has_sent(qa[i]);
        if (!__any(bad)) break;
        if (spin_fail(spins, tmo)) break;
#pragma unroll
        for (int i = 0; i < 8; ++i)
            if (has_sent(qa[i])) qa[i] = __builtin_amdgcn_raw_buffer_load_b128(rs_ws, qbase + i * 1024, 0, 16);
    }
    STAMPW(12, 7);
    v4u wuvv[4], wov[2], w2v;
    {
        f32x4 acc[2] = {{0.f, 0.f, 0.f, 0.f}, {0.f, 0.f, 0.f, 0.f}};
#pragma unroll
        for (int i = 0; i < 8; ++i)
#pragma unroll
            for (int nt = 0; nt < 2; ++nt) acc[nt] = MFMA(qa[i], kf[nt * 4 + (i & 3)], acc[nt]);
        LAS float* sc = (LAS float*)(S + L_SC);
#pragma unroll
        for (int nt = 0; nt < 2; ++nt)
#pragma unroll
            for (int r = 0; r < 4; ++r) sc[(wave * 16 + 4 * g4 + r) * 32 + 16 * nt + (lane & 15)] = acc[nt][r];
    }
    PIN();
    STAGE2_B();
    STAGE2_A();
    PIN();
    v2u vf[16];
    {
        const int q = (lane & 15) >> 2, p = lane & 3;
#pragma unroll
        for (int t = 0; t < 8; ++t) {
            const int c0 = 128 * wave + 16 * t;
            vf[2 * t] = __builtin_bit_cast(v2u, __builtin_amdgcn_ds_read_tr16_b64_v4i16((LAS s16x4*)(L + L_CKV + (8 * g4 + q) * CKV_STRIDE + c0 * 2 + 8 * p)));
            vf[2 * t + 1] = __builtin_bit_cast(v2u, __builtin_amdgcn_ds_read_tr16_b64_v4i16((LAS s16x4*)(L + L_CKV + (8 * g4 + 4 + q) * CKV_STRIDE + c0 * 2 + 8 * p)));
        }
        asm volatile("" : "+v"(vf[0]), "+v"(vf[1]), "+v"(vf[2]), "+v"(vf[3]), "+v"(vf[4]), "+v"(vf[5]), "+v"(vf[6]), "+v"(vf[7]));
        asm volatile("" : "+v"(vf[8]), "+v"(vf[9]), "+v"(vf[10]), "+v"(vf[11]), "+v"(vf[12]), "+v"(vf[13]), "+v"(vf[14]), "+v"(vf[15]));
    }
    LGKM0();
    BAR();
    STAMP(3);
    {
        const int h = tid >> 5, key = tid & 31;
        const LAS float* sc = (const LAS float*)(S + L_SC);
        float s = 0.f;
#pragma unroll
        for (int w = 0; w < 8; ++w) s += sc[(w * 16 + h) * 32 + key];
        float mx = s;
        mx = max_lo5(mx);
        const unsigned pb = f2bf(__builtin_amdgcn_exp2f(s - mx));
        float l = __uint_as_float(pb << 16);
        l = add_lo5(l);
        ((LAS unsigned short*)(S + L_P))[h * 32 + key] = (unsigned short)pb;
        if (key == 0) ((LAS v2u*)(S + L_ML))[h] = (v2u){canon(__float_as_uint(mx)), canon(__float_as_uint(l))};
    }
    LGKM0();
    BAR();
    STAMP(10);
    v4u fv;
    {
        const v4u pf = *(const LAS v4u*)(S + L_P + (lane & 15) * 64 + g4 * 16);
        fv = *(const LAS v4u*)(S + L_ML + (lane & 7) * 16);
        v2u o2[8];
#pragma unroll
        for (int t = 0; t < 8; ++t) {
            const v4u av = {vf[2 * t][0], vf[2 * t][1], vf[2 * t + 1][0], vf[2 * t + 1][1]};
            f32x4 o = {0.f, 0.f, 0.f, 0.f};
            o = MFMA(av, pf, o);
            o2[t][0] = pack2(o[0], o[1]); o2[t][1] = pack2(o[2], o[3]);
        }
#pragma unroll
        for (int t = 0; t < 8; ++t) *(LAS v2u*)(L + L_ST + (lane & 15) * CKV_STRIDE + (128 * wave + 16 * t + 4 * g4) * 2) = o2[t];
    }
    LGKM0();
    STAMP(15);
    {
#pragma unroll
        for (int i = 0; i < 4; ++i) {
            const int h = 4 * i + g4;
            const v4u v = *(const LAS v4u*)(L + L_ST + h * CKV_STRIDE + 256 * wave + (lane & 15) * 16);
            __builtin_amdgcn_raw_buffer_store_b128(v, rs_ws, (int)(PCTX_OFF + ((((size_t)bb * NSPLIT + sp) * H + h) * DC) * 2 + 256 * wave + (lane & 15) * 16), 0, 16);
        }
        VM0();
        asm volatile("" : "+v"(wuvv[0]), "+v"(wuvv[1]), "+v"(wuvv[2]), "+v"(wuvv[3]));
        asm volatile("" : "+v"(wov[0]), "+v"(wov[1]), "+v"(w2v));
        if (lane < 8) __builtin_amdgcn_raw_buffer_store_b128(fv, rs_ws, (int)(FLG_OFF + ((size_t)cu * 8 + wave) * FLGS + lane * 16), 0, 16);
    }
    STAMP(4);
    PIN();
    float xb = 0.f;
    {
        const int b = (tid >> 3) & 1, m = (cu < NC5 ? cu : 0) * 8 + (tid & 7);
        xb = bf2f(x[b * M + m]) + bf2f(b2[m]);
    }
    if (wave == 4 && lane < 14) {
        const int b = lane / 7, r = lane % 7, f = cu * 7 + r;
        const float* hp = (const float*)(ws + HP_OFF);
        float hv = bf2f(b1[f]);
#pragma unroll
        for (int p = 0; p < 4; ++p) hv += hp[((size_t)p * 2 + b) * F + f];
        const float t = tanhf(0.7978845608028654f * (hv + 0.044715f * hv * hv * hv));
        ((LAS float*)(S + L_HM))[b * 8 + r] = 0.5f * hv * (1.0f + t);
    }
    if (tid < 448) { *(LAS v4u*)(S + L_WO + tid * 16) = wov[0]; *(LAS v4u*)(S + L_WO + (448 + tid) * 16) = wov[1]; }
    if (tid < 392) *(LAS v4u*)(S + L_W2 + tid * 16) = w2v;
    asm volatile("" : "+v"(xb));
    PIN();
    {
        const int j = lane >> 3, ch = lane & 7;
        v4u pc[4]; v2u ml[4];
        const int fo = (cs >> 1) * (int)FLGS + hh * 8;
        SDLY(E3D);
        for (unsigned spins = 0;;) {
            bool bad = false;
#pragma unroll
            for (int i = 0; i < 4; ++i) {
                const int b = i >> 1, s = 16 * wave + (i & 1) * 8 + j;
                ml[i] = __builtin_amdgcn_raw_buffer_load_b64(rs_ws, (int)(FLG_OFF + ((size_t)b * 128 + s) * 8 * FLGS) + fo, 0, 16);
            }
#pragma unroll
            for (int i = 0; i < 4; ++i) bad |= (ml[i][0] == SENT) || (ml[i][1] == SENT);
            if (!__any(bad)) break;
            if (spin_fail(spins, tmo)) break;
        }
        if (!FST) STAMP(5);
#pragma unroll
        for (int i = 0; i < 4; ++i) {
            const int b = i >> 1, s = 16 * wave + (i & 1) * 8 + j;
            pc[i] = __builtin_amdgcn_raw_buffer_load_b128(rs_ws, (int)(PCTX_OFF + ((((size_t)b * NSPLIT + s) * H + hh) * DC + 64 * cs + ch * 8) * 2), 0, 16);
        }
        LAS float* red = (LAS float*)(S + L_SC);
#pragma unroll
        for (int b = 0; b < 2; ++b) {
            const float m0 = __uint_as_float(ml[2 * b][0]), l0 = __uint_as_float(ml[2 * b][1]);
            const float m1 = __uint_as_float(ml[2 * b + 1][0]), l1 = __uint_as_float(ml[2 * b + 1][1]);
            float gm = fmaxf(m0, m1);
            gm = max_hi3(gm);
            const float e0 = __builtin_amdgcn_exp2f(m0 - gm), e1 = __builtin_amdgcn_exp2f(m1 - gm);
            float ls = e0 * l0 + e1 * l1;
            ls = add_hi3(ls);
            float acc[8];
#pragma unroll
            for (int k = 0; k < 4; ++k) {
                acc[2 * k] = e0 * bflo(pc[2 * b][k]) + e1 * bflo(pc[2 * b + 1][k]);
                acc[2 * k + 1] = e0 * bfhi(pc[2 * b][k]) + e1 * bfhi(pc[2 * b + 1][k]);
            }
#pragma unroll
            for (int k = 0; k < 8; ++k) acc[k] = add_hi3(acc[k]);
            if (lane < 8) {
                *(LAS f32x4*)(red + (wave * 2 + b) * 64 + lane * 8) = (f32x4){acc[0], acc[1], acc[2], acc[3]};
                *(LAS f32x4*)(red + (wave * 2 + b) * 64 + lane * 8 + 4) = (f32x4){acc[4], acc[5], acc[6], acc[7]};
            }
            if (lane == 0) ((LAS f32x2*)(S + L_STAT))[wave * 2 + b] = (f32x2){gm, ls};
        }
    }
    LGKM0();
    BAR();
    if (tid < 128) {
        const int b = tid >> 6;
        const LAS float* red = (const LAS float*)(S + L_SC);
        const LAS f32x2* st = (const LAS f32x2*)(S + L_STAT);
        float gm = st[b][0];
#pragma unroll
        for (int w = 1; w < 8; ++w) gm = fmaxf(gm, st[w * 2 + b][0]);
        float s = 0.f, ls = 0.f;
#pragma unroll
        for (int w = 0; w < 8; ++w) {
            const f32x2 t = st[w * 2 + b];
            const float e = __builtin_amdgcn_exp2f(t[0] - gm);
            ls += e * t[1]; s += e * red[w * 128 + tid];
        }
        ((LAS float*)(S + L_CC))[tid] = s / ls;
    }
    LGKM0();
    BAR();
    {
        f32x2 a[8];
#pragma unroll
        for (int j = 0; j < 8; ++j) a[j] = (f32x2){0.f, 0.f};
#pragma unroll
        for (int i = 0; i < 4; ++i) {
            const v4u w = wuvv[i];
            const LAS float* cc = (const LAS float*)(S + L_CC);
            const int c = 8 * wave + 2 * i + (lane >> 5);
            const f32x2 cv = {cc[c], cc[64 + c]};
#pragma unroll
            for (int j = 0; j < 4; ++j) { a[2 * j] += cv * bflo(w[j]); a[2 * j + 1] += cv * bfhi(w[j]); }
        }
#pragma unroll
        for (int j = 0; j < 8; ++j) { a[j][0] = add32(a[j][0]); a[j][1] = add32(a[j][1]); }
        if (lane < 32) {
            LAS f32x4* red = (LAS f32x4*)(S + L_SC + 4096);
#pragma unroll
            for (int b = 0; b < 2; ++b) {
                red[((wave * 2 + b) * 256 + lane * 8) / 4] = (f32x4){a[0][b], a[1][b], a[2][b], a[3][b]};
                red[((wave * 2 + b) * 256 + lane * 8) / 4 + 1] = (f32x4){a[4][b], a[5][b], a[6][b], a[7][b]};
            }
        }
    }
    LGKM0();
    BAR();
    if (tid < 128) {
        const LAS f32x4* red = (const LAS f32x4*)(S + L_SC + 4096);
        const int b = (tid >> 2) & 1, d = (tid >> 3) * 16 + (tid & 3) * 4;
        const int ridx = b * 64 + (d >> 2);
        f32x4 s = red[ridx];
#pragma unroll
        for (int w = 1; w < 8; ++w) s += red[w * 128 + ridx];
        v4u v; v[0] = canon(__float_as_uint(s[0])); v[1] = canon(__float_as_uint(s[1])); v[2] = canon(__float_as_uint(s[2])); v[3] = canon(__float_as_uint(s[3]));
        __builtin_amdgcn_raw_buffer_store_b128(v, rs_ws, (int)(G4_OFF + ((((size_t)hh * 16 + (d >> 4)) * 16 + cs) * 32 + b * 16 + (d & 15)) * 4), 0, 16);
        if (!FST) STAMP(6);
        v4u g;
        for (unsigned spins = 0;;) {
            g = __builtin_amdgcn_raw_buffer_load_b128(rs_ws, (int)(G4_OFF + (size_t)(hh * 16 + cs) * 2048 + tid * 16), 0, 16);
            if (!__any(has_sent(g))) break;
            if (spin_fail(spins, tmo)) break;
        }
        f32x4 t; t[0] = __uint_as_float(g[0]); t[1] = __uint_as_float(g[1]); t[2] = __uint_as_float(g[2]); t[3] = __uint_as_float(g[3]);
        t[0] = add_hi3(t[0]); t[1] = add_hi3(t[1]); t[2] = add_hi3(t[2]); t[3] = add_hi3(t[3]);
        if (lane < 8) *(LAS f32x4*)(S + L_X6 + wave * 128 + lane * 16) = t;
        if (!FST) STAMP(7);
    }
    LGKM0();
    BAR();
    if (tid < 224) {
        const int j = tid >> 1, b = tid & 1;
        const LAS f32x4* x6 = (const LAS f32x4*)(S + L_X6);
        const LAS float* hm = (const LAS float*)(S + L_HM) + b * 8;
        f32x4 a = {0.f, 0.f, 0.f, 0.f};
#pragma unroll
        for (int q4 = 0; q4 < 4; ++q4) {
            const f32x4 cx = x6[b * 4 + q4] + x6[8 + b * 4 + q4];
#pragma unroll
            for (int k = 0; k < 4; ++k) {
                const v2u w = *(const LAS v2u*)(S + L_WO + (q4 * 4 + k) * (M * 2) + j * 8);
                const float c = cx[k];
                a[0] += c * bflo(w[0]); a[1] += c * bfhi(w[0]); a[2] += c * bflo(w[1]); a[3] += c * bfhi(w[1]);
            }
        }
#pragma unroll
        for (int r = 0; r < 7; ++r) {
            const v2u w = *(const LAS v2u*)(S + L_W2 + r * (M * 2) + j * 8);
            const float c = hm[r];
            a[0] += c * bflo(w[0]); a[1] += c * bfhi(w[0]); a[2] += c * bflo(w[1]); a[3] += c * bfhi(w[1]);
        }
        v4u v; v[0] = canon(__float_as_uint(a[0])); v[1] = canon(__float_as_uint(a[1])); v[2] = canon(__float_as_uint(a[2])); v[3] = canon(__float_as_uint(a[3]));
        __builtin_amdgcn_raw_buffer_store_b128(v, rs_ws, (int)(G5_OFF + (((size_t)(j >> 1) * 256 + cu) * 16 + b * 8 + (j & 1) * 4) * 4), 0, 16);
    }
    STAMP(8);
    if (cu < NC5) {
        v4u v0 = {SENT, SENT, SENT, SENT}, v1 = {SENT, SENT, SENT, SENT};
        for (unsigned spins = 0;;) {
            if (has_sent(v0)) v0 = __builtin_amdgcn_raw_buffer_load_b128(rs_ws, (int)(G5_OFF + (size_t)cu * 16384 + tid * 16), 0, 16);
            if (has_sent(v1)) v1 = __builtin_amdgcn_raw_buffer_load_b128(rs_ws, (int)(G5_OFF + (size_t)cu * 16384 + 8192 + tid * 16), 0, 16);
            if (!__any(has_sent(v0) || has_sent(v1))) break;
            if (spin_fail(spins, tmo)) break;
        }
        f32x4 s; s[0] = __uint_as_float(v0[0]) + __uint_as_float(v1[0]); s[1] = __uint_as_float(v0[1]) + __uint_as_float(v1[1]);
        s[2] = __uint_as_float(v0[2]) + __uint_as_float(v1[2]); s[3] = __uint_as_float(v0[3]) + __uint_as_float(v1[3]);
#pragma unroll
        for (int k = 0; k < 4; ++k) { float v = s[k]; v += dppf<0x124>(v); s[k] = add_hi3(v); }
        LAS float* red = (LAS float*)(S + L_CC);
        if (lane < 4) *(LAS f32x4*)(red + wave * 16 + lane * 4) = s;
        LGKM0();
        BAR();
        if (tid < 16) {
            float t = 0.f;
#pragma unroll
            for (int w = 0; w < 8; ++w) t += red[w * 16 + tid];
            const int b = tid >> 3, m = cu * 8 + (tid & 7);
            t += xb;
            out[b * M + m] = (unsigned short)f2bf(t);
        }
        STAMP(9);
    }
}
}

extern "C" void kernel_launch(void* const* d_in, const int* in_sizes, int n_in,
                              void* d_out, int out_size, void* d_ws, size_t ws_size,
                              hipStream_t stream) {
    const unsigned short* x = (const unsigned short*)d_in[0];
    const unsigned short* ckv = (const unsigned short*)d_in[1];
    const unsigned short* wq = (const unsigned short*)d_in[2];
    const unsigned short* wuk = (const unsigned short*)d_in[3];
    const unsigned short* wuv = (const unsigned short*)d_in[4];
    const unsigned short* wo = (const unsigned short*)d_in[5];
    const unsigned short* w1 = (const unsigned short*)d_in[7];
    const unsigned short* b1 = (const unsigned short*)d_in[8];
    const unsigned short* w2 = (const unsigned short*)d_in[9];
    const unsigned short* b2 = (const unsigned short*)d_in[10];
    unsigned char* ws = (unsigned char*)d_ws;
    k1<<<368, K1PF ? 320 : 256, 0, stream>>>(x, wq, w1, ws, wuk);
    k2<<<NCU, 512, 0, stream>>>(x, ckv, wuk, wuv, wo, b1, w2, b2, ws, (unsigned short*)d_out);
}
```

```cpp
#include <hip/hip_runtime.h>
#include <stdint.h>

#define GAS __attribute__((address_space(1)))
#define LAS __attribute__((address_space(3)))
typedef unsigned v4u __attribute__((ext_vector_type(4)));
typedef unsigned v2u __attribute__((ext_vector_type(2)));
typedef float f32x4 __attribute__((ext_vector_type(4)));
typedef float f32x2 __attribute__((ext_vector_type(2)));
typedef short bf16x8 __attribute__((ext_vector_type(8)));
typedef short s16x4 __attribute__((ext_vector_type(4)));
typedef __bf16 bf16x2_t __attribute__((ext_vector_type(2)));
typedef GAS unsigned gu32;
typedef GAS unsigned long long gu64;
#define RLX_AGENT __ATOMIC_RELAXED, __HIP_MEMORY_SCOPE_AGENT

#define STAMPS 0
#define WSEND 0
#define FBAR 1
#define CKV 0
#define FST 0
#define S2M 0
#define PFD 0
#define K1PF 0
#define WIN 16
#define E2D2 0
#define E2D 8
#define FILLM 0
#define QPM 0
#define E3D 4
#define ESD 2
#define S2P 2
#define SDLY(n) do { _Pragma("unroll") for (int i_ = 0; i_ < (n) / 4; ++i_) __builtin_amdgcn_s_sleep(16); if ((n) % 4) __builtin_amdgcn_s_sleep(4 * ((n) % 4)); } while (0)
#define PIN() __builtin_amdgcn_sched_barrier(0)

namespace {
constexpr int H = 16, SK = 4096, D = 256, M = 448, DC = 1024, F = 1792;
constexpr int NCU = 256;
constexpr int NKG = 32, KPG = 128;
constexpr int NC5 = 56;

constexpr size_t TMO_OFF = 0;
constexpr size_t QABS_OFF = 4096;
constexpr size_t ML_OFF = QABS_OFF + 131072;
constexpr size_t SP_OFF = ML_OFF + 32768;
constexpr size_t PCTX_OFF = SP_OFF + (size_t)256 * 8192;
constexpr size_t G4_OFF = PCTX_OFF + (size_t)2 * NKG * 16 * 1024 * 2;
constexpr size_t G4_BYTES = (size_t)256 * 512 * 4;
constexpr size_t G5_OFF = G4_OFF + G4_BYTES;
constexpr size_t G5_BYTES = (size_t)56 * 256 * 16 * 4;
constexpr size_t FILL_BYTES = G5_OFF + G5_BYTES;
constexpr size_t QP_OFF = FILL_BYTES;
constexpr size_t HP_OFF = QP_OFF + 4 * 2 * 4096 * 4;
constexpr size_t STAMP_OFF = HP_OFF + 65536;
constexpr size_t K1ST_OFF = STAMP_OFF + 256 * 16 * 8;
constexpr size_t WS_NEED = K1ST_OFF + 512 * 2 * 8;
static_assert(FILL_BYTES % 16 == 0, "fill");

constexpr int RS2 = 1040;
constexpr int L_CKV = 0;
constexpr int L_WK = 64 * RS2;
constexpr int LDS_DMA_BYTES = L_WK + 32 * RS2;
constexpr int L_SC = 0;
constexpr int L_QF = 8448;
constexpr int L_WO = L_QF + 16384;
constexpr int L_W2 = L_WO + 14336;
constexpr int L_QA = L_W2 + 7168;
constexpr int L_QS = L_QA + 2048;
constexpr int L_STAT = L_QS + 512;
constexpr int L_CC = L_STAT + 128;
constexpr int L_HM = L_CC + 512;
constexpr int L_X6 = L_HM + 128;
constexpr int LDS_SC_BYTES = L_X6 + 256;
static_assert(LDS_DMA_BYTES + LDS_SC_BYTES <= 160 * 1024, "lds");

constexpr unsigned kSpinCap = 1u << 18;
constexpr unsigned SENT = 0xFFFFFFFFu;

__device__ __forceinline__ float bf2f(unsigned short u) { return __uint_as_float(((unsigned)u) << 16); }
__device__ __forceinline__ float bflo(unsigned u) { return __uint_as_float(u << 16); }
__device__ __forceinline__ float bfhi(unsigned u) { return __uint_as_float(u & 0xffff0000u); }
__device__ __forceinline__ unsigned f2bf(float f) { unsigned u = __float_as_uint(f); return (u + 0x7fffu + ((u >> 16) & 1u)) >> 16; }
__device__ __forceinline__ unsigned pack2(float a, float b) { const f32x2 v = {a, b}; return __builtin_bit_cast(unsigned, __builtin_convertvector(v, bf16x2_t)); }
template <int C> __device__ __forceinline__ float dppf(float v) { return __int_as_float(__builtin_amdgcn_update_dpp(0, __float_as_int(v), C, 0xF, 0xF, true)); }
__device__ __forceinline__ float add16(float v) { auto r = __builtin_amdgcn_permlane16_swap(__float_as_uint(v), __float_as_uint(v), false, false); return __uint_as_float(r[0]) + __uint_as_float(r[1]); }
__device__ __forceinline__ float add32(float v) { auto r = __builtin_amdgcn_permlane32_swap(__float_as_uint(v), __float_as_uint(v), false, false); return __uint_as_float(r[0]) + __uint_as_float(r[1]); }
__device__ __forceinline__ float max16(float v) { auto r = __builtin_amdgcn_permlane16_swap(__float_as_uint(v), __float_as_uint(v), false, false); return fmaxf(__uint_as_float(r[0]), __uint_as_float(r[1])); }
__device__ __forceinline__ float max32(float v) { auto r = __builtin_amdgcn_permlane32_swap(__float_as_uint(v), __float_as_uint(v), false, false); return fmaxf(__uint_as_float(r[0]), __uint_as_float(r[1])); }
__device__ __forceinline__ float add_hi3(float v) { v += dppf<0x128>(v); v = add16(v); return add32(v); }
__device__ __forceinline__ float max_hi3(float v) { v = fmaxf(v, dppf<0x128>(v)); v = max16(v); return max32(v); }
__device__ __forceinline__ float add_lo5(float v) { v += dppf<0xB1>(v); v += dppf<0x4E>(v); v += dppf<0x141>(v); v += dppf<0x140>(v); return add16(v); }
__device__ __forceinline__ float max_lo5(float v) { v = fmaxf(v, dppf<0xB1>(v)); v = fmaxf(v, dppf<0x4E>(v)); v = fmaxf(v, dppf<0x141>(v)); v = fmaxf(v, dppf<0x140>(v)); return max16(v); }
__device__ __forceinline__ float wsum(float v) { return add32(add_lo5(v)); }
__device__ __forceinline__ unsigned long long rt() { return __builtin_amdgcn_s_memrealtime(); }

#define BAR() __builtin_amdgcn_s_barrier()
#define LGKM0() asm volatile("s_waitcnt lgkmcnt(0)" ::: "memory")
#define VM0() asm volatile("s_waitcnt vmcnt(0)" ::: "memory")
#define MFMA(a, b, c) __builtin_amdgcn_mfma_f32_16x16x32_bf16(__builtin_bit_cast(bf16x8, a), __builtin_bit_cast(bf16x8, b), c, 0, 0, 0)

__global__ void __launch_bounds__(320) k1(const unsigned short* __restrict__ x, const unsigned short* __restrict__ wq,
                                          const unsigned short* __restrict__ w1, unsigned char* __restrict__ ws) {
    __shared__ float xn[2][M];
    __shared__ float red[4][2][64];
    const int tid = threadIdx.x, wave = __builtin_amdgcn_readfirstlane(tid >> 6), lane = tid & 63;
    const int blk = blockIdx.x;
    const bool isq = blk < 256;
    if (wave == 4) {
        const v4u sn = {SENT, SENT, SENT, SENT};
        const unsigned tot16 = (unsigned)(FILL_BYTES / 16);
        const __amdgpu_buffer_rsrc_t rsf = __builtin_amdgcn_make_buffer_rsrc(ws, 0, (int)WS_NEED, 0x00020000);
        SDLY(PFD);
        for (unsigned i = blk * 64 + lane; i < tot16; i += gridDim.x * 64) __builtin_amdgcn_raw_buffer_store_b128(sn, rsf, (int)(i * 16), 0, FILLM);
        return;
    }
    const int idx = isq ? blk : blk - 256;
    const int mq = idx & 3, cg = idx >> 2;
    const int ncols = isq ? 4096 : F;
    const unsigned short* W = (isq ? wq : w1) + (size_t)(mq * 112) * ncols + cg * 64;
    const int r = tid >> 3, cch = tid & 7;
    unsigned xr[7];
#pragma unroll
    for (int i = 0; i < 7; ++i) asm volatile("global_load_ushort %0, %1, off" : "=v"(xr[i]) : "v"(x + (wave & 1) * M + lane + 64 * i) : "memory");
    v4u wv[4];
#pragma unroll
    for (int i = 0; i < 4; ++i) {
        const int row = (i < 3 || r < 16) ? r + 32 * i : 111;
        asm volatile("global_load_dwordx4 %0, %1, off nt" : "=v"(wv[i]) : "v"(W + (size_t)row * ncols + cch * 8) : "memory");
    }
    PIN();
    asm volatile("s_waitcnt vmcnt(4)" : "+v"(xr[0]), "+v"(xr[1]), "+v"(xr[2]), "+v"(xr[3]), "+v"(xr[4]), "+v"(xr[5]), "+v"(xr[6]) :: "memory");
    PIN();
    if (wave < 2) {
        float v[7]; float s = 0.f;
#pragma unroll
        for (int i = 0; i < 7; ++i) { v[i] = bf2f((unsigned short)xr[i]); s += v[i]; }
        s = wsum(s); const float mean = s * (1.0f / M);
        float ss = 0.f;
#pragma unroll
        for (int i = 0; i < 7; ++i) { const float d = v[i] - mean; ss += d * d; }
        ss = wsum(ss);
        const float rs = 1.0f / sqrtf(ss * (1.0f / M) + 1e-5f);
#pragma unroll
        for (int i = 0; i < 7; ++i) xn[wave][lane + 64 * i] = (v[i] - mean) * rs;
    }
    LGKM0(); BAR();
    asm volatile("s_waitcnt vmcnt(0)" : "+v"(wv[0]), "+v"(wv[1]), "+v"(wv[2]), "+v"(wv[3]) :: "memory");
    float a[2][8];
#pragma unroll
    for (int j = 0; j < 8; ++j) { a[0][j] = 0.f; a[1][j] = 0.f; }
#pragma unroll
    for (int i = 0; i < 4; ++i) {
        int row = mq * 112 + r + 32 * i; if (row >= M) row = M - 1;
        const bool ok = (i < 3 || r < 16);
        const float x0 = ok ? xn[0][row] : 0.f, x1 = ok ? xn[1][row] : 0.f;
#pragma unroll
        for (int j = 0; j < 4; ++j) {
            const float lo = bflo(wv[i][j]), hi = bfhi(wv[i][j]);
            a[0][2 * j] += x0 * lo; a[0][2 * j + 1] += x0 * hi;
            a[1][2 * j] += x1 * lo; a[1][2 * j + 1] += x1 * hi;
        }
    }
#pragma unroll
    for (int b = 0; b < 2; ++b)
#pragma unroll
        for (int j = 0; j < 8; ++j) a[b][j] = add_hi3(a[b][j]);
    if (lane < 8) {
#pragma unroll
        for (int b = 0; b < 2; ++b)
#pragma unroll
            for (int j = 0; j < 8; ++j) red[wave][b][lane * 8 + j] = a[b][j];
    }
    LGKM0(); BAR();
    if (tid < 128) {
        const int b = tid >> 6, c = tid & 63;
        const float s = red[0][b][c] + red[1][b][c] + red[2][b][c] + red[3][b][c];
        float* dst = (float*)(ws + (isq ? QP_OFF : HP_OFF));
        dst[((size_t)mq * 2 + b) * ncols + cg * 64 + c] = s;
    }
}

__device__ __forceinline__ bool spin_fail(unsigned& spins, gu32* tmo) {
    __builtin_amdgcn_s_sleep(1);
    if (++spins > kSpinCap) { __hip_atomic_store(tmo, 1u, RLX_AGENT); return true; }
    return (spins & 1023u) == 0u && __builtin_amdgcn_readfirstlane(__hip_atomic_load(tmo, RLX_AGENT)) == 1u;
}
__device__ __forceinline__ bool has_sent(v4u v) { return v[0] == SENT || v[1] == SENT || v[2] == SENT || v[3] == SENT; }
__device__ __forceinline__ unsigned canon(unsigned u) { return u == SENT ? 0x7FC00000u : u; }
__device__ __forceinline__ unsigned canonb(unsigned u) { return u == SENT ? 0xFFC0FFC0u : u; }

#define GLDS_NT(src, dst) __builtin_amdgcn_global_load_lds((const unsigned*)(src), (LAS unsigned*)(dst), 16, 0, 2)
#define STAMP(i) do {} while (0)
#define STAMPW(i, w) do {} while (0)

#define WUV_ROW(i) (64 * cs + 8 * wave + 2 * (i) + (lane >> 5))
#define STAGE2_B() do { \
        _Pragma("unroll") for (int i = 0; i < 4; ++i) \
            asm volatile("global_load_dwordx4 %0, %1, off nt" : "=v"(wuvv[i]) : "v"(wuv + ((size_t)WUV_ROW(i) * H + hh) * D + (lane & 31) * 8) : "memory"); \
    } while (0)
#define STAGE2_A() do { \
        const unsigned short* so = wo + ((size_t)hh * D + cs * 16) * M; \
        const int t2 = tid < 448 ? tid : 447; \
        asm volatile("global_load_dwordx4 %0, %1, off nt" : "=v"(wov[0]) : "v"(so + (size_t)t2 * 8) : "memory"); \
        asm volatile("global_load_dwordx4 %0, %1, off nt" : "=v"(wov[1]) : "v"(so + (size_t)(448 + t2) * 8) : "memory"); \
        asm volatile("global_load_dwordx4 %0, %1, off nt" : "=v"(w2v) : "v"(w2 + (size_t)cu * 7 * M + (size_t)(tid < 392 ? tid : 391) * 8) : "memory"); \
    } while (0)
#define XLD4(dst, off) asm volatile("buffer_load_dwordx4 %0, %1, %2, 0 offen sc1" : "=v"(dst) : "v"(off), "s"(rs_ws) : "memory")
#define XLD2(dst, off) asm volatile("buffer_load_dwordx2 %0, %1, %2, 0 offen sc1" : "=v"(dst) : "v"(off), "s"(rs_ws) : "memory")
__global__ void __launch_bounds__(512, 1) k2(const unsigned short* __restrict__ x, const unsigned short* __restrict__ ckv,
                                             const unsigned short* __restrict__ wuk, const unsigned short* __restrict__ wuv,
                                             const unsigned short* __restrict__ wo, const unsigned short* __restrict__ b1,
                                             const unsigned short* __restrict__ w2, const unsigned short* __restrict__ b2,
                                             unsigned char* __restrict__ ws, unsigned short* __restrict__ out) {
    __shared__ __attribute__((aligned(16))) unsigned char lds_dma[LDS_DMA_BYTES];
    __shared__ __attribute__((aligned(16))) unsigned char lds_sc[LDS_SC_BYTES];
    LAS unsigned char* L = (LAS unsigned char*)lds_dma;
    LAS unsigned char* S = (LAS unsigned char*)lds_sc;
    const int tid = threadIdx.x, wave = __builtin_amdgcn_readfirstlane(tid >> 6), lane = tid & 63;
    const int cu = blockIdx.x;
    const int hh = cu >> 4, cs = cu & 15;
    const int bb = cu >> 7, kg = (cu >> 2) & 31, cg = cu & 3;
    const int g4 = lane >> 4;
    gu32* tmo = (gu32*)(ws + TMO_OFF);
    const __amdgpu_buffer_rsrc_t rs_ws = __builtin_amdgcn_make_buffer_rsrc(ws, 0, (int)WS_NEED, 0x00020000);
    STAMP(0);

    float qp[4];
    {
        const float* p = (const float*)(ws + QP_OFF) + (size_t)(tid >> 8) * 4096 + hh * D + (tid & 255);
#pragma unroll
        for (int i = 0; i < 4; ++i) asm volatile("global_load_dword %0, %1, off" : "=v"(qp[i]) : "v"(p + (size_t)i * 8192) : "memory");
    }
    if (wave < 4) {
#pragma unroll
        for (int i = 0; i < 8; ++i) {
            const int r = 2 * i + (lane >> 5);
            const int ck = (lane & 31) ^ ((r & 7) << 2);
            GLDS_NT(wuk + ((size_t)(64 * cs + 16 * wave + r) * H + hh) * D + 8 * ck, L + L_WK + (8 * wave + i) * RS2);
        }
    }
    PIN();
    BAR();
    PIN();
    {
        const unsigned short* src = ckv + ((size_t)bb * SK + (size_t)kg * KPG + 16 * wave) * DC + 256 * cg;
#pragma unroll
        for (int i = 0; i < 8; ++i) {
            const int r = 2 * i + (lane >> 5);
            const int ck = (lane & 31) ^ ((r & 7) << 2);
            GLDS_NT(src + (size_t)r * DC + 8 * ck, L + L_CKV + (8 * wave + i) * RS2);
        }
    }
    PIN();
    if (wave < 4) asm volatile("s_waitcnt vmcnt(16)" ::: "memory");
    else asm volatile("s_waitcnt vmcnt(8)" ::: "memory");
    asm volatile("" : "+v"(qp[0]), "+v"(qp[1]), "+v"(qp[2]), "+v"(qp[3]));
    {
        float s = (qp[0] + qp[1]) + (qp[2] + qp[3]);
        s *= 0.0625f * 1.4426950408889634f;
        const unsigned hi = f2bf(s);
        const unsigned lo = f2bf(s - __uint_as_float(hi << 16));
        const int b = tid >> 8, d = tid & 255;
        ((LAS unsigned short*)(S + L_QA))[(2 * b) * 256 + d] = (unsigned short)hi;
        ((LAS unsigned short*)(S + L_QA))[(2 * b + 1) * 256 + d] = (unsigned short)lo;
    }
    LGKM0();
    BAR();
    STAMPW(1, 5);
    if (wave < 4) {
        v4u av[8], wk[8];
#pragma unroll
        for (int ks = 0; ks < 8; ++ks) av[ks] = *(const LAS v4u*)(S + L_QA + (lane & 3) * 512 + (32 * ks + 8 * g4) * 2);
        const bool keep = (lane & 15) < 4;
        asm volatile("s_waitcnt vmcnt(8)" ::: "memory");
        {
            const unsigned wa = (unsigned)(size_t)(L + L_WK + (8 * wave + ((lane & 15) >> 1)) * RS2 + (lane & 1) * 512 + g4 * 16);
#pragma unroll
            for (int ks = 0; ks < 8; ++ks) asm volatile("ds_read_b128 %0, %1" : "=v"(wk[ks]) : "v"(wa + ((ks ^ (lane & 7)) << 6)) : "memory");
            asm volatile("s_waitcnt lgkmcnt(0)" : "+v"(wk[0]), "+v"(wk[1]), "+v"(wk[2]), "+v"(wk[3]), "+v"(wk[4]), "+v"(wk[5]), "+v"(wk[6]), "+v"(wk[7]) :: "memory");
        }
        f32x4 acc = {0.f, 0.f, 0.f, 0.f};
#pragma unroll
        for (int ks = 0; ks < 8; ++ks) {
            v4u a = av[ks];
            a[0] = keep ? a[0] : 0u; a[1] = keep ? a[1] : 0u; a[2] = keep ? a[2] : 0u; a[3] = keep ? a[3] : 0u;
            acc = MFMA(a, wk[ks], acc);
        }
        LAS unsigned short* qs = (LAS unsigned short*)(S + L_QS) + wave * 64;
        if (lane < 16) {
            const float v0 = acc[0] + acc[1], v1 = acc[2] + acc[3];
            const unsigned h0 = f2bf(v0), h1 = f2bf(v1);
            qs[lane] = (unsigned short)h0;
            qs[16 + lane] = (unsigned short)f2bf(v0 - __uint_as_float(h0 << 16));
            qs[32 + lane] = (unsigned short)h1;
            qs[48 + lane] = (unsigned short)f2bf(v1 - __uint_as_float(h1 << 16));
        }
        LGKM0();
        if (lane < 8) {
            v4u v = *(const LAS v4u*)(S + L_QS + wave * 128 + lane * 16);
            v[0] = canonb(v[0]); v[1] = canonb(v[1]); v[2] = canonb(v[2]); v[3] = canonb(v[3]);
            __builtin_amdgcn_raw_buffer_store_b128(v, rs_ws, (int)QABS_OFF + (((((lane >> 2) * 4 + (cs >> 2)) * 2 + ((lane >> 1) & 1)) * 8 + ((2 * cs + (wave >> 1)) & 7)) * 64 + hh + 16 * (2 * (wave & 1) + (lane & 1))) * 16, 0, 16);
        }
        STAMP(2);
    }
    PIN();
    BAR();
    SDLY(E2D);
    PIN();
    v4u wuvv[4], wov[2], w2v;
    const int qoff = (int)QABS_OFF + ((bb * 4 + cg) * 2 * 8 + wave) * 1024 + lane * 16;
    v4u qa[2];
    XLD4(qa[0], qoff);
    XLD4(qa[1], qoff + 8192);
    STAMPW(14, 7);
    asm volatile("s_waitcnt vmcnt(2)" ::: "memory");
    STAMPW(13, 7);
    v4u kf[8];
    {
        const unsigned ka = (unsigned)(size_t)(L + L_CKV + (8 * wave + ((lane & 15) >> 1)) * RS2 + (lane & 1) * 512 + g4 * 16);
#pragma unroll
        for (int ks = 0; ks < 8; ++ks) asm volatile("ds_read_b128 %0, %1" : "=v"(kf[ks]) : "v"(ka + ((ks ^ (lane & 7)) << 6)) : "memory");
    }
    asm volatile("s_waitcnt vmcnt(0)" : "+v"(qa[0]), "+v"(qa[1]) :: "memory");
    for (unsigned spins = 0;;) {
        if (!__any(has_sent(qa[0]) || has_sent(qa[1]))) break;
        if (spin_fail(spins, tmo)) break;
        XLD4(qa[0], qoff);
        XLD4(qa[1], qoff + 8192);
        asm volatile("s_waitcnt vmcnt(0)" : "+v"(qa[0]), "+v"(qa[1]) :: "memory");
    }
    *(LAS v4u*)(S + L_QF + wave * 1024 + lane * 16) = qa[0];
    *(LAS v4u*)(S + L_QF + 8192 + wave * 1024 + lane * 16) = qa[1];
    asm volatile("s_waitcnt lgkmcnt(0)" : "+v"(kf[0]), "+v"(kf[1]), "+v"(kf[2]), "+v"(kf[3]), "+v"(kf[4]), "+v"(kf[5]), "+v"(kf[6]), "+v"(kf[7]) :: "memory");
    BAR();
    STAMPW(12, 7);
    f32x4 sown;
    {
        f32x4 acc0 = {0.f, 0.f, 0.f, 0.f}, acc1 = {0.f, 0.f, 0.f, 0.f};
#pragma unroll
        for (int ks = 0; ks < 8; ++ks) {
            const v4u ah = *(const LAS v4u*)(S + L_QF + ks * 1024 + lane * 16);
            const v4u al = *(const LAS v4u*)(S + L_QF + 8192 + ks * 1024 + lane * 16);
            acc0 = MFMA(ah, kf[ks], acc0);
            acc1 = MFMA(al, kf[ks], acc1);
        }
        sown = acc0 + acc1;
        v4u v; v[0] = canon(__float_as_uint(sown[0])); v[1] = canon(__float_as_uint(sown[1])); v[2] = canon(__float_as_uint(sown[2])); v[3] = canon(__float_as_uint(sown[3]));
        __builtin_amdgcn_raw_buffer_store_b128(v, rs_ws, (int)SP_OFF + cu * 8192 + wave * 1024 + lane * 16, 0, 16);
    }
    STAMP(3);
    PIN();
    v2u vf[16];
    {
        const int q = (lane & 15) >> 2, p = lane & 3;
#pragma unroll
        for (int u = 0; u < 2; ++u) {
            const unsigned va = (unsigned)(size_t)(L + L_CKV + (4 * g4 + 2 * u + (q >> 1)) * RS2 + (q & 1) * 512 + ((wave ^ (4 * u + q)) << 6) + 16 * p);
#pragma unroll
            for (int j = 0; j < 4; ++j)
#pragma unroll
                for (int t = 0; t < 2; ++t)
                    asm volatile("ds_read_b64_tr_b16 %0, %1 offset:%2" : "=v"(vf[(j * 2 + t) * 2 + u]) : "v"(va), "n"(j * 16 * RS2 + 8 * t) : "memory");
        }
    }
    SDLY(ESD);
    PIN();
    {
        v4u e[3];
        const int eb = (int)SP_OFF + (cu & ~3) * 8192 + wave * 1024 + lane * 16;
        const int eo0 = eb + (cg < 1 ? 1 : 0) * 8192, eo1 = eb + (cg < 2 ? 2 : 1) * 8192, eo2 = eb + (cg < 3 ? 3 : 2) * 8192;
        XLD4(e[0], eo0); XLD4(e[1], eo1); XLD4(e[2], eo2);
        asm volatile("s_waitcnt vmcnt(0)" : "+v"(e[0]), "+v"(e[1]), "+v"(e[2]) :: "memory");
        for (unsigned spins = 0;;) {
            if (!__any(has_sent(e[0]) || has_sent(e[1]) || has_sent(e[2]))) break;
            if (spin_fail(spins, tmo)) break;
            XLD4(e[0], eo0); XLD4(e[1], eo1); XLD4(e[2], eo2);
            asm volatile("s_waitcnt vmcnt(0)" : "+v"(e[0]), "+v"(e[1]), "+v"(e[2]) :: "memory");
        }
        STAMP(10);
        STAGE2_B(); STAGE2_A();
        LAS float* sc = (LAS float*)(S + L_SC);
        const bool lo = cg < 2;
#pragma unroll
        for (int r = 0; r < 4; ++r) {
            const float e0 = __uint_as_float(e[0][r]), e1 = __uint_as_float(e[1][r]), e2 = __uint_as_float(e[2][r]);
            const float s = (e0 + (lo ? sown[r] : e1)) + (e2 + (lo ? e1 : sown[r]));
            sc[(4 * g4 + r) * 132 + 16 * wave + (lane & 15)] = s;
        }
    }
    asm volatile("s_waitcnt lgkmcnt(0)" : "+v"(vf[0]), "+v"(vf[1]), "+v"(vf[2]), "+v"(vf[3]), "+v"(vf[4]), "+v"(vf[5]), "+v"(vf[6]), "+v"(vf[7]) :: "memory");
    asm volatile("" : "+v"(vf[8]), "+v"(vf[9]), "+v"(vf[10]), "+v"(vf[11]), "+v"(vf[12]), "+v"(vf[13]), "+v"(vf[14]), "+v"(vf[15]));
    BAR();
    {
        const LAS f32x4* sc4 = (const LAS f32x4*)(S + L_SC + (lane & 15) * 528 + g4 * 32);
        f32x4 sv[8];
#pragma unroll
        for (int j = 0; j < 4; ++j) { sv[2 * j] = sc4[8 * j]; sv[2 * j + 1] = sc4[8 * j + 1]; }
        float mx = sv[0][0];
#pragma unroll
        for (int i = 0; i < 8; ++i)
#pragma unroll
            for (int k = 0; k < 4; ++k) mx = fmaxf(mx, sv[i][k]);
        mx = max16(mx); mx = max32(mx);
        v4u pf[4];
        float l = 0.f;
#pragma unroll
        for (int i = 0; i < 8; ++i) {
            const unsigned p01 = pack2(__builtin_amdgcn_exp2f(sv[i][0] - mx), __builtin_amdgcn_exp2f(sv[i][1] - mx));
            const unsigned p23 = pack2(__builtin_amdgcn_exp2f(sv[i][2] - mx), __builtin_amdgcn_exp2f(sv[i][3] - mx));
            pf[i >> 1][(i & 1) * 2] = p01; pf[i >> 1][(i & 1) * 2 + 1] = p23;
            l += (bflo(p01) + bfhi(p01)) + (bflo(p23) + bfhi(p23));
        }
        f32x4 o0 = {0.f, 0.f, 0.f, 0.f}, o1 = {0.f, 0.f, 0.f, 0.f};
#pragma unroll
        for (int j = 0; j < 4; ++j) {
            const v4u a0 = {vf[(j * 2) * 2][0], vf[(j * 2) * 2][1], vf[(j * 2) * 2 + 1][0], vf[(j * 2) * 2 + 1][1]};
            const v4u a1 = {vf[(j * 2 + 1) * 2][0], vf[(j * 2 + 1) * 2][1], vf[(j * 2 + 1) * 2 + 1][0], vf[(j * 2 + 1) * 2 + 1][1]};
            o0 = MFMA(a0, pf[j], o0);
            o1 = MFMA(a1, pf[j], o1);
        }
        v4u v; v[0] = canonb(pack2(o0[0], o0[1])); v[1] = canonb(pack2(o0[2], o0[3])); v[2] = canonb(pack2(o1[0], o1[1])); v[3] = canonb(pack2(o1[2], o1[3]));
        __builtin_amdgcn_raw_buffer_store_b128(v, rs_ws, (int)PCTX_OFF + ((((bb * NKG + kg) * H + (lane & 15)) * DC) + 256 * cg + 32 * wave + 8 * g4) * 2, 0, 16);
        if (wave == 0) {
            l = add16(l); l = add32(l);
            if (lane < 16) __builtin_amdgcn_raw_buffer_store_b64((v2u){canon(__float_as_uint(mx)), canon(__float_as_uint(l))}, rs_ws, (int)ML_OFF + cu * 128 + lane * 8, 0, 16);
        }
    }
    STAMP(4);
    PIN();
    float xb = 0.f;
    {
        const int b = (tid >> 3) & 1, m = (cu < NC5 ? cu : 0) * 8 + (tid & 7);
        xb = bf2f(x[b * M + m]) + bf2f(b2[m]);
    }
    if (wave == 4 && lane < 14) {
        const int b = lane / 7, r = lane % 7, f = cu * 7 + r;
        const float* hp = (const float*)(ws + HP_OFF);
        float hv = bf2f(b1[f]);
#pragma unroll
        for (int p = 0; p < 4; ++p) hv += hp[((size_t)p * 2 + b) * F + f];
        const float t = tanhf(0.7978845608028654f * (hv + 0.044715f * hv * hv * hv));
        ((LAS float*)(S + L_HM))[b * 8 + r] = 0.5f * hv * (1.0f + t);
    }
    VM0();
    asm volatile("" : "+v"(wuvv[0]), "+v"(wuvv[1]), "+v"(wuvv[2]), "+v"(wuvv[3]), "+v"(wov[0]), "+v"(wov[1]), "+v"(w2v), "+v"(xb));
    if (tid < 448) { *(LAS v4u*)(S + L_WO + tid * 16) = wov[0]; *(LAS v4u*)(S + L_WO + (448 + tid) * 16) = wov[1]; }
    if (tid < 392) *(LAS v4u*)(S + L_W2 + tid * 16) = w2v;
    STAMP(15);
    PIN();
    {
        const int pr = tid >> 3, ch = tid & 7;
        const int po = (int)PCTX_OFF + ((pr * H + hh) * DC + 64 * cs + 8 * ch) * 2;
        const int mo = (int)ML_OFF + (pr * 4 + (cs >> 2)) * 128 + hh * 8;
        v4u pc; v2u ml;
        SDLY(E3D);
        for (unsigned spins = 0;;) {
            XLD4(pc, po); XLD2(ml, mo);
            asm volatile("s_waitcnt vmcnt(0)" : "+v"(pc), "+v"(ml) :: "memory");
            if (!__any(has_sent(pc) || ml[0] == SENT || ml[1] == SENT)) break;
            if (spin_fail(spins, tmo)) break;
        }
        STAMP(5);
        LAS float* red = (LAS float*)(S + L_QA);
        const float m0 = __uint_as_float(ml[0]), l0 = __uint_as_float(ml[1]);
        const float gm = max_hi3(m0);
        const float e0 = __builtin_amdgcn_exp2f(m0 - gm);
        const float ls = add_hi3(e0 * l0);
        float acc[8];
#pragma unroll
        for (int k = 0; k < 4; ++k) { acc[2 * k] = e0 * bflo(pc[k]); acc[2 * k + 1] = e0 * bfhi(pc[k]); }
#pragma unroll
        for (int k = 0; k < 8; ++k) acc[k] = add_hi3(acc[k]);
        if (lane < 8) {
            *(LAS f32x4*)(red + wave * 64 + lane * 8) = (f32x4){acc[0], acc[1], acc[2], acc[3]};
            *(LAS f32x4*)(red + wave * 64 + lane * 8 + 4) = (f32x4){acc[4], acc[5], acc[6], acc[7]};
        }
        if (lane == 0) ((LAS f32x2*)(S + L_STAT))[wave] = (f32x2){gm, ls};
    }
    LGKM0();
    BAR();
    if (tid < 128) {
        const int b = tid >> 6, c = tid & 63;
        const LAS float* red = (const LAS float*)(S + L_QA) + b * 256;
        const LAS f32x2* st = (const LAS f32x2*)(S + L_STAT) + b * 4;
        float gm = st[0][0];
#pragma unroll
        for (int w = 1; w < 4; ++w) gm = fmaxf(gm, st[w][0]);
        float s = 0.f, ls = 0.f;
#pragma unroll
        for (int w = 0; w < 4; ++w) {
            const f32x2 t = st[w];
            const float e = __builtin_amdgcn_exp2f(t[0] - gm);
            ls += e * t[1]; s += e * red[w * 64 + c];
        }
        ((LAS float*)(S + L_CC))[tid] = s / ls;
    }
    LGKM0();
    BAR();
    {
        f32x2 a[8];
#pragma unroll
        for (int j = 0; j < 8; ++j) a[j] = (f32x2){0.f, 0.f};
#pragma unroll
        for (int i = 0; i < 4; ++i) {
            const v4u w = wuvv[i];
            const LAS float* cc = (const LAS float*)(S + L_CC);
            const int c = 8 * wave + 2 * i + (lane >> 5);
            const f32x2 cv = {cc[c], cc[64 + c]};
#pragma unroll
            for (int j = 0; j < 4; ++j) { a[2 * j] += cv * bflo(w[j]); a[2 * j + 1] += cv * bfhi(w[j]); }
        }
#pragma unroll
        for (int j = 0; j < 8; ++j) { a[j][0] = add32(a[j][0]); a[j][1] = add32(a[j][1]); }
        if (lane < 32) {
            LAS f32x4* red = (LAS f32x4*)(S + L_QF);
#pragma unroll
            for (int b = 0; b < 2; ++b) {
                red[((wave * 2 + b) * 256 + lane * 8) / 4] = (f32x4){a[0][b], a[1][b], a[2][b], a[3][b]};
                red[((wave * 2 + b) * 256 + lane * 8) / 4 + 1] = (f32x4){a[4][b], a[5][b], a[6][b], a[7][b]};
            }
        }
    }
    LGKM0();
    BAR();
    if (tid < 128) {
        const LAS f32x4* red = (const LAS f32x4*)(S + L_QF);
        const int b = (tid >> 2) & 1, d = (tid >> 3) * 16 + (tid & 3) * 4;
        const int ridx = b * 64 + (d >> 2);
        f32x4 s = red[ridx];
#pragma unroll
        for (int w = 1; w < 8; ++w) s += red[w * 128 + ridx];
        v4u v; v[0] = canon(__float_as_uint(s[0])); v[1] = canon(__float_as_uint(s[1])); v[2] = canon(__float_as_uint(s[2])); v[3] = canon(__float_as_uint(s[3]));
        __builtin_amdgcn_raw_buffer_store_b128(v, rs_ws, (int)(G4_OFF + ((((size_t)hh * 16 + (d >> 4)) * 16 + cs) * 32 + b * 16 + (d & 15)) * 4), 0, 16);
        STAMP(6);
        v4u g;
        for (unsigned spins = 0;;) {
            g = __builtin_amdgcn_raw_buffer_load_b128(rs_ws, (int)(G4_OFF + (size_t)(hh * 16 + cs) * 2048 + tid * 16), 0, 16);
            if (!__any(has_sent(g))) break;
            if (spin_fail(spins, tmo)) break;
        }
        f32x4 t; t[0] = __uint_as_float(g[0]); t[1] = __uint_as_float(g[1]); t[2] = __uint_as_float(g[2]); t[3] = __uint_as_float(g[3]);
        t[0] = add_hi3(t[0]); t[1] = add_hi3(t[1]); t[2] = add_hi3(t[2]); t[3] = add_hi3(t[3]);
        if (lane < 8) *(LAS f32x4*)(S + L_X6 + wave * 128 + lane * 16) = t;
        STAMP(7);
    }
    LGKM0();
    BAR();
    if (tid < 224) {
        const int j = tid >> 1, b = tid & 1;
        const LAS f32x4* x6 = (const LAS f32x4*)(S + L_X6);
        const LAS float* hm = (const LAS float*)(S + L_HM) + b * 8;
        f32x4 a = {0.f, 0.f, 0.f, 0.f};
#pragma unroll
        for (int q4 = 0; q4 < 4; ++q4) {
            const f32x4 cx = x6[b * 4 + q4] + x6[8 + b * 4 + q4];
#pragma unroll
            for (int k = 0; k < 4; ++k) {
                const v2u w = *(const LAS v2u*)(S + L_WO + (q4 * 4 + k) * (M * 2) + j * 8);
                const float c = cx[k];
                a[0] += c * bflo(w[0]); a[1] += c * bfhi(w[0]); a[2] += c * bflo(w[1]); a[3] += c * bfhi(w[1]);
            }
        }
#pragma unroll
        for (int r = 0; r < 7; ++r) {
            const v2u w = *(const LAS v2u*)(S + L_W2 + r * (M * 2) + j * 8);
            const float c = hm[r];
            a[0] += c * bflo(w[0]); a[1] += c * bfhi(w[0]); a[2] += c * bflo(w[1]); a[3] += c * bfhi(w[1]);
        }
        v4u v; v[0] = canon(__float_as_uint(a[0])); v[1] = canon(__float_as_uint(a[1])); v[2] = canon(__float_as_uint(a[2])); v[3] = canon(__float_as_uint(a[3]));
        __builtin_amdgcn_raw_buffer_store_b128(v, rs_ws, (int)(G5_OFF + (((size_t)(j >> 1) * 256 + cu) * 16 + b * 8 + (j & 1) * 4) * 4), 0, 16);
    }
    STAMP(8);
    if (cu < NC5) {
        v4u v0 = {SENT, SENT, SENT, SENT}, v1 = {SENT, SENT, SENT, SENT};
        for (unsigned spins = 0;;) {
            if (has_sent(v0)) v0 = __builtin_amdgcn_raw_buffer_load_b128(rs_ws, (int)(G5_OFF + (size_t)cu * 16384 + tid * 16), 0, 16);
            if (has_sent(v1)) v1 = __builtin_amdgcn_raw_buffer_load_b128(rs_ws, (int)(G5_OFF + (size_t)cu * 16384 + 8192 + tid * 16), 0, 16);
            if (!__any(has_sent(v0) || has_sent(v1))) break;
            if (spin_fail(spins, tmo)) break;
        }
        f32x4 s; s[0] = __uint_as_float(v0[0]) + __uint_as_float(v1[0]); s[1] = __uint_as_float(v0[1]) + __uint_as_float(v1[1]);
        s[2] = __uint_as_float(v0[2]) + __uint_as_float(v1[2]); s[3] = __uint_as_float(v0[3]) + __uint_as_float(v1[3]);
#pragma unroll
        for (int k = 0; k < 4; ++k) { float v = s[k]; v += dppf<0x124>(v); s[k] = add_hi3(v); }
        LAS float* red = (LAS float*)(S + L_CC);
        if (lane < 4) *(LAS f32x4*)(red + wave * 16 + lane * 4) = s;
        LGKM0();
        BAR();
        if (tid < 16) {
            float t = 0.f;
#pragma unroll
            for (int w = 0; w < 8; ++w) t += red[w * 16 + tid];
            const int b = tid >> 3, m = cu * 8 + (tid & 7);
            t += xb;
            out[b * M + m] = (unsigned short)f2bf(t);
        }
        STAMP(9);
    }
}
}

extern "C" void kernel_launch(void* const* d_in, const int* in_sizes, int n_in,
                              void* d_out, int out_size, void* d_ws, size_t ws_size,
                              hipStream_t stream) {
    const unsigned short* x = (const unsigned short*)d_in[0];
    const unsigned short* ckv = (const unsigned short*)d_in[1];
    const unsigned short* wq = (const unsigned short*)d_in[2];
    const unsigned short* wuk = (const unsigned short*)d_in[3];
    const unsigned short* wuv = (const unsigned short*)d_in[4];
    const unsigned short* wo = (const unsigned short*)d_in[5];
    const unsigned short* w1 = (const unsigned short*)d_in[7];
    const unsigned short* b1 = (const unsigned short*)d_in[8];
    const unsigned short* w2 = (const unsigned short*)d_in[9];
    const unsigned short* b2 = (const unsigned short*)d_in[10];
    unsigned char* ws = (unsigned char*)d_ws;
    k1<<<368, 320, 0, stream>>>(x, wq, w1, ws);
    k2<<<NCU, 512, 0, stream>>>(x, ckv, wuk, wuv, wo, b1, w2, b2, ws, (unsigned short*)d_out);
}
```

```cpp
#include <hip/hip_runtime.h>
#include <stdint.h>

#define GAS __attribute__((address_space(1)))
#define LAS __attribute__((address_space(3)))
typedef unsigned v4u __attribute__((ext_vector_type(4)));
typedef unsigned v2u __attribute__((ext_vector_type(2)));
typedef float f32x4 __attribute__((ext_vector_type(4)));
typedef float f32x2 __attribute__((ext_vector_type(2)));
typedef short bf16x8 __attribute__((ext_vector_type(8)));
typedef short s16x4 __attribute__((ext_vector_type(4)));
typedef __bf16 bf16x2_t __attribute__((ext_vector_type(2)));
typedef GAS unsigned gu32;
typedef GAS unsigned long long gu64;
#define RLX_AGENT __ATOMIC_RELAXED, __HIP_MEMORY_SCOPE_AGENT

#define STAMPS 0
#define WSEND 0
#define FBAR 1
#define CKV 0
#define FST 0
#define S2M 0
#define PFD 0
#define K1PF 0
#define WIN 16
#define E2D2 0
#define E2D 8
#define FILLM 16
#define QPM 0
#define E3D 4
#define ESD 2
#define PU 4444
#define PO 555
#define E4D 0
#define SDLY(n) do { _Pragma("unroll") for (int i_ = 0; i_ < (n) / 4; ++i_) __builtin_amdgcn_s_sleep(16); if ((n) % 4) __builtin_amdgcn_s_sleep(4 * ((n) % 4)); } while (0)
#define PIN() __builtin_amdgcn_sched_barrier(0)

namespace {
constexpr int H = 16, SK = 4096, D = 256, M = 448, DC = 1024, F = 1792;
constexpr int NCU = 256;
constexpr int NKG = 32, KPG = 128;
constexpr int NC5 = 56;

constexpr size_t TMO_OFF = 0;
constexpr size_t QABS_OFF = 4096;
constexpr size_t ML_OFF = QABS_OFF + 131072;
constexpr size_t SP_OFF = ML_OFF + 32768;
constexpr size_t PCTX_OFF = SP_OFF + (size_t)256 * 8192;
constexpr size_t G4_OFF = PCTX_OFF + (size_t)2 * NKG * 16 * 1024 * 2;
constexpr size_t G4_BYTES = (size_t)256 * 512 * 4;
constexpr size_t G5_OFF = G4_OFF + G4_BYTES;
constexpr size_t G5_BYTES = (size_t)56 * 256 * 16 * 4;
constexpr size_t FILL_BYTES = G5_OFF + G5_BYTES;
constexpr size_t QP_OFF = FILL_BYTES;
constexpr size_t HP_OFF = QP_OFF + 4 * 2 * 4096 * 4;
constexpr size_t STAMP_OFF = HP_OFF + 65536;
constexpr size_t K1ST_OFF = STAMP_OFF + 256 * 16 * 8;
constexpr size_t WS_NEED = K1ST_OFF + 512 * 2 * 8;
static_assert(FILL_BYTES % 16 == 0, "fill");

constexpr int RS2 = 1040;
constexpr int L_CKV = 0;
constexpr int L_WK = 64 * RS2;
constexpr int L_WV = L_WK;
constexpr int L_WO = L_WK + 32 * RS2;
constexpr int LDS_DMA_BYTES = L_WO + 8 * 3072;
constexpr int L_SC = 0;
constexpr int L_QF = 8448;
constexpr int L_QA = L_QF + 16384;
constexpr int L_STAT = L_QA + 2048;
constexpr int L_CC = L_STAT + 128;
constexpr int L_HM = L_CC + 512;
constexpr int LDS_SC_BYTES = L_HM + 128;
static_assert(LDS_DMA_BYTES + LDS_SC_BYTES <= 160 * 1024, "lds");

constexpr unsigned kSpinCap = 1u << 18;
constexpr unsigned SENT = 0xFFFFFFFFu;

__device__ __forceinline__ float bf2f(unsigned short u) { return __uint_as_float(((unsigned)u) << 16); }
__device__ __forceinline__ float bflo(unsigned u) { return __uint_as_float(u << 16); }
__device__ __forceinline__ float bfhi(unsigned u) { return __uint_as_float(u & 0xffff0000u); }
__device__ __forceinline__ unsigned f2bf(float f) { unsigned u = __float_as_uint(f); return (u + 0x7fffu + ((u >> 16) & 1u)) >> 16; }
__device__ __forceinline__ unsigned pack2(float a, float b) { const f32x2 v = {a, b}; return __builtin_bit_cast(unsigned, __builtin_convertvector(v, bf16x2_t)); }
template <int C> __device__ __forceinline__ float dppf(float v) { return __int_as_float(__builtin_amdgcn_update_dpp(0, __float_as_int(v), C, 0xF, 0xF, true)); }
__device__ __forceinline__ float add16(float v) { auto r = __builtin_amdgcn_permlane16_swap(__float_as_uint(v), __float_as_uint(v), false, false); return __uint_as_float(r[0]) + __uint_as_float(r[1]); }
__device__ __forceinline__ float add32(float v) { auto r = __builtin_amdgcn_permlane32_swap(__float_as_uint(v), __float_as_uint(v), false, false); return __uint_as_float(r[0]) + __uint_as_float(r[1]); }
__device__ __forceinline__ float max16(float v) { auto r = __builtin_amdgcn_permlane16_swap(__float_as_uint(v), __float_as_uint(v), false, false); return fmaxf(__uint_as_float(r[0]), __uint_as_float(r[1])); }
__device__ __forceinline__ float max32(float v) { auto r = __builtin_amdgcn_permlane32_swap(__float_as_uint(v), __float_as_uint(v), false, false); return fmaxf(__uint_as_float(r[0]), __uint_as_float(r[1])); }
__device__ __forceinline__ float add_hi3(float v) { v += dppf<0x128>(v); v = add16(v); return add32(v); }
__device__ __forceinline__ float max_hi3(float v) { v = fmaxf(v, dppf<0x128>(v)); v = max16(v); return max32(v); }
__device__ __forceinline__ float add_lo5(float v) { v += dppf<0xB1>(v); v += dppf<0x4E>(v); v += dppf<0x141>(v); v += dppf<0x140>(v); return add16(v); }
__device__ __forceinline__ float max_lo5(float v) { v = fmaxf(v, dppf<0xB1>(v)); v = fmaxf(v, dppf<0x4E>(v)); v = fmaxf(v, dppf<0x141>(v)); v = fmaxf(v, dppf<0x140>(v)); return max16(v); }
__device__ __forceinline__ float wsum(float v) { return add32(add_lo5(v)); }
__device__ __forceinline__ unsigned long long rt() { return __builtin_amdgcn_s_memrealtime(); }

#define BAR() __builtin_amdgcn_s_barrier()
#define LGKM0() asm volatile("s_waitcnt lgkmcnt(0)" ::: "memory")
#define VM0() asm volatile("s_waitcnt vmcnt(0)" ::: "memory")
#define MFMA(a, b, c) __builtin_amdgcn_mfma_f32_16x16x32_bf16(__builtin_bit_cast(bf16x8, a), __builtin_bit_cast(bf16x8, b), c, 0, 0, 0)

__global__ void __launch_bounds__(320) k1(const unsigned short* __restrict__ x, const unsigned short* __restrict__ wq,
                                          const unsigned short* __restrict__ w1, unsigned char* __restrict__ ws) {
    __shared__ float xn[2][M];
    __shared__ float red[4][2][64];
    const int tid = threadIdx.x, wave = __builtin_amdgcn_readfirstlane(tid >> 6), lane = tid & 63;
    const int blk = blockIdx.x;
    const bool isq = blk < 256;
    if (wave == 4) {
        const v4u sn = {SENT, SENT, SENT, SENT};
        const unsigned tot16 = (unsigned)(FILL_BYTES / 16);
        const __amdgpu_buffer_rsrc_t rsf = __builtin_amdgcn_make_buffer_rsrc(ws, 0, (int)WS_NEED, 0x00020000);
        SDLY(PFD);
        for (unsigned i = blk * 64 + lane; i < tot16; i += gridDim.x * 64) __builtin_amdgcn_raw_buffer_store_b128(sn, rsf, (int)(i * 16), 0, FILLM);
        return;
    }
    const int idx = isq ? blk : blk - 256;
    const int mq = idx & 3, cg = idx >> 2;
    const int ncols = isq ? 4096 : F;
    const unsigned short* W = (isq ? wq : w1) + (size_t)(mq * 112) * ncols + cg * 64;
    const int r = tid >> 3, cch = tid & 7;
    unsigned xr[7];
#pragma unroll
    for (int i = 0; i < 7; ++i) asm volatile("global_load_ushort %0, %1, off" : "=v"(xr[i]) : "v"(x + (wave & 1) * M + lane + 64 * i) : "memory");
    v4u wv[4];
#pragma unroll
    for (int i = 0; i < 4; ++i) {
        const int row = (i < 3 || r < 16) ? r + 32 * i : 111;
        asm volatile("global_load_dwordx4 %0, %1, off nt" : "=v"(wv[i]) : "v"(W + (size_t)row * ncols + cch * 8) : "memory");
    }
    PIN();
    asm volatile("s_waitcnt vmcnt(4)" : "+v"(xr[0]), "+v"(xr[1]), "+v"(xr[2]), "+v"(xr[3]), "+v"(xr[4]), "+v"(xr[5]), "+v"(xr[6]) :: "memory");
    PIN();
    if (wave < 2) {
        float v[7]; float s = 0.f;
#pragma unroll
        for (int i = 0; i < 7; ++i) { v[i] = bf2f((unsigned short)xr[i]); s += v[i]; }
        s = wsum(s); const float mean = s * (1.0f / M);
        float ss = 0.f;
#pragma unroll
        for (int i = 0; i < 7; ++i) { const float d = v[i] - mean; ss += d * d; }
        ss = wsum(ss);
        const float rs = 1.0f / sqrtf(ss * (1.0f / M) + 1e-5f);
#pragma unroll
        for (int i = 0; i < 7; ++i) xn[wave][lane + 64 * i] = (v[i] - mean) * rs;
    }
    LGKM0(); BAR();
    asm volatile("s_waitcnt vmcnt(0)" : "+v"(wv[0]), "+v"(wv[1]), "+v"(wv[2]), "+v"(wv[3]) :: "memory");
    float a[2][8];
#pragma unroll
    for (int j = 0; j < 8; ++j) { a[0][j] = 0.f; a[1][j] = 0.f; }
#pragma unroll
    for (int i = 0; i < 4; ++i) {
        int row = mq * 112 + r + 32 * i; if (row >= M) row = M - 1;
        const bool ok = (i < 3 || r < 16);
        const float x0 = ok ? xn[0][row] : 0.f, x1 = ok ? xn[1][row] : 0.f;
#pragma unroll
        for (int j = 0; j < 4; ++j) {
            const float lo = bflo(wv[i][j]), hi = bfhi(wv[i][j]);
            a[0][2 * j] += x0 * lo; a[0][2 * j + 1] += x0 * hi;
            a[1][2 * j] += x1 * lo; a[1][2 * j + 1] += x1 * hi;
        }
    }
#pragma unroll
    for (int b = 0; b < 2; ++b)
#pragma unroll
        for (int j = 0; j < 8; ++j) a[b][j] = add_hi3(a[b][j]);
    if (lane < 8) {
#pragma unroll
        for (int b = 0; b < 2; ++b)
#pragma unroll
            for (int j = 0; j < 8; ++j) red[wave][b][lane * 8 + j] = a[b][j];
    }
    LGKM0(); BAR();
    if (tid < 128) {
        const int b = tid >> 6, c = tid & 63;
        const float s = red[0][b][c] + red[1][b][c] + red[2][b][c] + red[3][b][c];
        float* dst = (float*)(ws + (isq ? QP_OFF : HP_OFF));
        dst[((size_t)mq * 2 + b) * ncols + cg * 64 + c] = s;
    }
}

__device__ __forceinline__ bool spin_fail(unsigned& spins, gu32* tmo) {
    __builtin_amdgcn_s_sleep(1);
    if (++spins > kSpinCap) { __hip_atomic_store(tmo, 1u, RLX_AGENT); return true; }
    return (spins & 1023u) == 0u && __builtin_amdgcn_readfirstlane(__hip_atomic_load(tmo, RLX_AGENT)) == 1u;
}
__device__ __forceinline__ bool has_sent(v4u v) { return v[0] == SENT || v[1] == SENT || v[2] == SENT || v[3] == SENT; }
__device__ __forceinline__ unsigned canon(unsigned u) { return u == SENT ? 0x7FC00000u : u; }
__device__ __forceinline__ unsigned canonb(unsigned u) { return u == SENT ? 0xFFC0FFC0u : u; }

#define GLDS_NT(src, dst) __builtin_amdgcn_global_load_lds((const unsigned*)(src), (LAS unsigned*)(dst), 16, 0, 2)
#define STAMP(i) do {} while (0)
#define STAMPW(i, w) do {} while (0)

constexpr int dig(int v, int i) { return i == 0 ? v % 10 : dig(v / 10, i - 1); }
constexpr int cnt_at(int p) { int n = 0; for (int i = 0; i < 4; ++i) n += dig(PU, i) == p; for (int i = 0; i < 3; ++i) n += dig(PO, i) == p; return n; }
constexpr int cnt_after_u(int p) {
    int lastp = 0; for (int i = 0; i < 4; ++i) if (dig(PU, i) > lastp) lastp = dig(PU, i);
    int n = 0; for (int i = 0; i < 3; ++i) n += (dig(PO, i) >= lastp && dig(PO, i) <= p); return n; }
constexpr bool OEARLY = cnt_at(6) == 0 && cnt_at(7) == 0;
#define BULK(p) do { \
        _Pragma("unroll") for (int i = 0; i < 4; ++i) if (dig(PU, i) == (p)) \
            GLDS_NT(wuv + ((size_t)(64 * cs + 16 * i + (lane >> 2)) * H + hh) * D + 32 * wave + (lane & 3) * 8, L + L_WV + wave * 4096 + i * 1024); \
        _Pragma("unroll") for (int i = 0; i < 3; ++i) if (dig(PO, i) == (p)) { \
            const int row = 8 * i + (lane >> 3); \
            const unsigned short* sp = row < 16 ? wo + ((size_t)hh * D + cs * 16 + row) * M : w2 + ((size_t)cu * 7 + (row < 23 ? row - 16 : 6)) * M; \
            GLDS_NT(sp + 64 * (wave < 7 ? wave : 6) + (lane & 7) * 8, L + L_WO + wave * 3072 + i * 1024); \
        } \
        PIN(); \
    } while (0)
#define VMW(n) asm volatile("s_waitcnt vmcnt(%0)" :: "n"(n) : "memory")
#define XLD4(dst, off) asm volatile("buffer_load_dwordx4 %0, %1, %2, 0 offen sc1" : "=v"(dst) : "v"(off), "s"(rs_ws) : "memory")
#define XLD2(dst, off) asm volatile("buffer_load_dwordx2 %0, %1, %2, 0 offen sc1" : "=v"(dst) : "v"(off), "s"(rs_ws) : "memory")
__global__ void __launch_bounds__(512, 1) k2(const unsigned short* __restrict__ x, const unsigned short* __restrict__ ckv,
                                             const unsigned short* __restrict__ wuk, const unsigned short* __restrict__ wuv,
                                             const unsigned short* __restrict__ wo, const unsigned short* __restrict__ b1,
                                             const unsigned short* __restrict__ w2, const unsigned short* __restrict__ b2,
                                             unsigned char* __restrict__ ws, unsigned short* __restrict__ out) {
    __shared__ __attribute__((aligned(16))) unsigned char lds_dma[LDS_DMA_BYTES];
    __shared__ __attribute__((aligned(16))) unsigned char lds_sc[LDS_SC_BYTES];
    LAS unsigned char* L = (LAS unsigned char*)lds_dma;
    LAS unsigned char* S = (LAS unsigned char*)lds_sc;
    const int tid = threadIdx.x, wave = __builtin_amdgcn_readfirstlane(tid >> 6), lane = tid & 63;
    const int cu = blockIdx.x;
    const int hh = cu >> 4, cs = cu & 15;
    const int bb = cu >> 7, kg = (cu >> 2) & 31, cg = cu & 3;
    const int g4 = lane >> 4;
    gu32* tmo = (gu32*)(ws + TMO_OFF);
    const __amdgpu_buffer_rsrc_t rs_ws = __builtin_amdgcn_make_buffer_rsrc(ws, 0, (int)WS_NEED, 0x00020000);
    STAMP(0);

    float qp[4];
    {
        const float* p = (const float*)(ws + QP_OFF) + (size_t)(tid >> 8) * 4096 + hh * D + (tid & 255);
#pragma unroll
        for (int i = 0; i < 4; ++i) asm volatile("global_load_dword %0, %1, off" : "=v"(qp[i]) : "v"(p + (size_t)i * 8192) : "memory");
    }
    if (wave < 4) {
#pragma unroll
        for (int i = 0; i < 8; ++i) {
            const int r = 2 * i + (lane >> 5);
            const int ck = (lane & 31) ^ ((r & 7) << 2);
            GLDS_NT(wuk + ((size_t)(64 * cs + 16 * wave + r) * H + hh) * D + 8 * ck, L + L_WK + (8 * wave + i) * RS2);
        }
    }
    PIN();
    BAR();
    PIN();
    {
        const unsigned short* src = ckv + ((size_t)bb * SK + (size_t)kg * KPG + 16 * wave) * DC + 256 * cg;
#pragma unroll
        for (int i = 0; i < 8; ++i) {
            const int r = 2 * i + (lane >> 5);
            const int ck = (lane & 31) ^ ((r & 7) << 2);
            GLDS_NT(src + (size_t)r * DC + 8 * ck, L + L_CKV + (8 * wave + i) * RS2);
        }
    }
    PIN();
    if (wave < 4) asm volatile("s_waitcnt vmcnt(16)" ::: "memory");
    else asm volatile("s_waitcnt vmcnt(8)" ::: "memory");
    asm volatile("" : "+v"(qp[0]), "+v"(qp[1]), "+v"(qp[2]), "+v"(qp[3]));
    {
        float s = (qp[0] + qp[1]) + (qp[2] + qp[3]);
        s *= 0.0625f * 1.4426950408889634f;
        const unsigned hi = f2bf(s);
        const unsigned lo = f2bf(s - __uint_as_float(hi << 16));
        const int b = tid >> 8, d = tid & 255;
        ((LAS unsigned short*)(S + L_QA))[(2 * b) * 256 + d] = (unsigned short)hi;
        ((LAS unsigned short*)(S + L_QA))[(2 * b + 1) * 256 + d] = (unsigned short)lo;
    }
    LGKM0();
    BAR();
    STAMPW(1, 5);
    if (wave < 4) {
        v4u av[8], wk[8];
#pragma unroll
        for (int ks = 0; ks < 8; ++ks) av[ks] = *(const LAS v4u*)(S + L_QA + (lane & 3) * 512 + (32 * ks + 8 * g4) * 2);
        asm volatile("s_waitcnt vmcnt(8)" ::: "memory");
        {
            const unsigned wa = (unsigned)(size_t)(L + L_WK + (8 * wave + ((lane & 15) >> 1)) * RS2 + (lane & 1) * 512 + g4 * 16);
#pragma unroll
            for (int ks = 0; ks < 8; ++ks) asm volatile("ds_read_b128 %0, %1" : "=v"(wk[ks]) : "v"(wa + ((ks ^ (lane & 7)) << 6)) : "memory");
            asm volatile("s_waitcnt lgkmcnt(0)" : "+v"(wk[0]), "+v"(wk[1]), "+v"(wk[2]), "+v"(wk[3]), "+v"(wk[4]), "+v"(wk[5]), "+v"(wk[6]), "+v"(wk[7]) :: "memory");
        }
        f32x4 acc0 = {0.f, 0.f, 0.f, 0.f}, acc1 = {0.f, 0.f, 0.f, 0.f};
#pragma unroll
        for (int ks = 0; ks < 8; ks += 2) { acc0 = MFMA(wk[ks], av[ks], acc0); acc1 = MFMA(wk[ks + 1], av[ks + 1], acc1); }
        acc0 += acc1;
        unsigned hb[4];
#pragma unroll
        for (int r = 0; r < 4; ++r) {
            const float v = acc0[r] + dppf<0xB1>(acc0[r]);
            const unsigned h0 = f2bf(v);
            hb[r] = (lane & 1) ? f2bf(v - __uint_as_float(h0 << 16)) : h0;
        }
        if ((lane & 15) < 4) {
            const v2u v = {canonb(hb[0] | (hb[1] << 16)), canonb(hb[2] | (hb[3] << 16))};
            __builtin_amdgcn_raw_buffer_store_b64(v, rs_ws, (int)QABS_OFF + ((((((lane >> 1) & 1) * 4 + (cs >> 2)) * 2 + (lane & 1)) * 8 + ((2 * cs + (wave >> 1)) & 7)) * 64 + hh + 16 * (2 * (wave & 1) + (g4 >> 1))) * 16 + (g4 & 1) * 8, 0, 16);
        }
        STAMP(2);
    }
    PIN();
    BAR();
    SDLY(E2D);
    PIN();
    const int qoff = (int)QABS_OFF + ((bb * 4 + cg) * 2 * 8 + wave) * 1024 + lane * 16;
    v4u qa[2];
    XLD4(qa[0], qoff);
    XLD4(qa[1], qoff + 8192);
    STAMPW(14, 7);
    asm volatile("s_waitcnt vmcnt(2)" ::: "memory");
    STAMPW(13, 7);
    v4u kf[8];
    {
        const unsigned ka = (unsigned)(size_t)(L + L_CKV + (8 * wave + ((lane & 15) >> 1)) * RS2 + (lane & 1) * 512 + g4 * 16);
#pragma unroll
        for (int ks = 0; ks < 8; ++ks) asm volatile("ds_read_b128 %0, %1" : "=v"(kf[ks]) : "v"(ka + ((ks ^ (lane & 7)) << 6)) : "memory");
    }
    BULK(1);
    asm volatile("s_waitcnt vmcnt(%2)" : "+v"(qa[0]), "+v"(qa[1]) : "n"(cnt_at(1)) : "memory");
    for (unsigned spins = 0;;) {
        if (!__any(has_sent(qa[0]) || has_sent(qa[1]))) break;
        if (spin_fail(spins, tmo)) break;
        XLD4(qa[0], qoff);
        XLD4(qa[1], qoff + 8192);
        asm volatile("s_waitcnt vmcnt(0)" : "+v"(qa[0]), "+v"(qa[1]) :: "memory");
    }
    *(LAS v4u*)(S + L_QF + wave * 1024 + lane * 16) = qa[0];
    *(LAS v4u*)(S + L_QF + 8192 + wave * 1024 + lane * 16) = qa[1];
    asm volatile("s_waitcnt lgkmcnt(0)" : "+v"(kf[0]), "+v"(kf[1]), "+v"(kf[2]), "+v"(kf[3]), "+v"(kf[4]), "+v"(kf[5]), "+v"(kf[6]), "+v"(kf[7]) :: "memory");
    BAR();
    STAMPW(12, 7);
    f32x4 sown;
    {
        f32x4 acc0 = {0.f, 0.f, 0.f, 0.f}, acc1 = {0.f, 0.f, 0.f, 0.f};
#pragma unroll
        for (int ks = 0; ks < 8; ++ks) {
            const v4u ah = *(const LAS v4u*)(S + L_QF + ks * 1024 + lane * 16);
            const v4u al = *(const LAS v4u*)(S + L_QF + 8192 + ks * 1024 + lane * 16);
            acc0 = MFMA(ah, kf[ks], acc0);
            acc1 = MFMA(al, kf[ks], acc1);
        }
        sown = acc0 + acc1;
        v4u v; v[0] = canon(__float_as_uint(sown[0])); v[1] = canon(__float_as_uint(sown[1])); v[2] = canon(__float_as_uint(sown[2])); v[3] = canon(__float_as_uint(sown[3]));
        __builtin_amdgcn_raw_buffer_store_b128(v, rs_ws, (int)SP_OFF + cu * 8192 + wave * 1024 + lane * 16, 0, 16);
    }
    STAMP(3);
    PIN();
    BULK(2);
    v2u vf[16];
    {
        const int q = (lane & 15) >> 2, p = lane & 3;
#pragma unroll
        for (int u = 0; u < 2; ++u) {
            const unsigned va = (unsigned)(size_t)(L + L_CKV + (4 * g4 + 2 * u + (q >> 1)) * RS2 + (q & 1) * 512 + ((wave ^ (4 * u + q)) << 6) + 16 * p);
#pragma unroll
            for (int j = 0; j < 4; ++j)
#pragma unroll
                for (int t = 0; t < 2; ++t)
                    asm volatile("ds_read_b64_tr_b16 %0, %1 offset:%2" : "=v"(vf[(j * 2 + t) * 2 + u]) : "v"(va), "n"(j * 16 * RS2 + 8 * t) : "memory");
        }
    }
    SDLY(ESD);
    PIN();
    {
        v4u e[3];
        const int eb = (int)SP_OFF + (cu & ~3) * 8192 + wave * 1024 + lane * 16;
        const int eo0 = eb + (cg < 1 ? 1 : 0) * 8192, eo1 = eb + (cg < 2 ? 2 : 1) * 8192, eo2 = eb + (cg < 3 ? 3 : 2) * 8192;
        XLD4(e[0], eo0); XLD4(e[1], eo1); XLD4(e[2], eo2);
        BULK(3);
        asm volatile("s_waitcnt vmcnt(%3)" : "+v"(e[0]), "+v"(e[1]), "+v"(e[2]) : "n"(cnt_at(3)) : "memory");
        for (unsigned spins = 0;;) {
            if (!__any(has_sent(e[0]) || has_sent(e[1]) || has_sent(e[2]))) break;
            if (spin_fail(spins, tmo)) break;
            XLD4(e[0], eo0); XLD4(e[1], eo1); XLD4(e[2], eo2);
            asm volatile("s_waitcnt vmcnt(0)" : "+v"(e[0]), "+v"(e[1]), "+v"(e[2]) :: "memory");
        }
        STAMP(10);
        BULK(4);
        LAS float* sc = (LAS float*)(S + L_SC);
        const bool lo = cg < 2;
#pragma unroll
        for (int r = 0; r < 4; ++r) {
            const float e0 = __uint_as_float(e[0][r]), e1 = __uint_as_float(e[1][r]), e2 = __uint_as_float(e[2][r]);
            const float s = (e0 + (lo ? sown[r] : e1)) + (e2 + (lo ? e1 : sown[r]));
            sc[(4 * g4 + r) * 132 + 16 * wave + (lane & 15)] = s;
        }
    }
    asm volatile("s_waitcnt lgkmcnt(0)" : "+v"(vf[0]), "+v"(vf[1]), "+v"(vf[2]), "+v"(vf[3]), "+v"(vf[4]), "+v"(vf[5]), "+v"(vf[6]), "+v"(vf[7]) :: "memory");
    asm volatile("" : "+v"(vf[8]), "+v"(vf[9]), "+v"(vf[10]), "+v"(vf[11]), "+v"(vf[12]), "+v"(vf[13]), "+v"(vf[14]), "+v"(vf[15]));
    BAR();
    {
        const LAS f32x4* sc4 = (const LAS f32x4*)(S + L_SC + (lane & 15) * 528 + g4 * 32);
        f32x4 sv[8];
#pragma unroll
        for (int j = 0; j < 4; ++j) { sv[2 * j] = sc4[8 * j]; sv[2 * j + 1] = sc4[8 * j + 1]; }
        float mx = sv[0][0];
#pragma unroll
        for (int i = 0; i < 8; ++i)
#pragma unroll
            for (int k = 0; k < 4; ++k) mx = fmaxf(mx, sv[i][k]);
        mx = max16(mx); mx = max32(mx);
        v4u pf[4];
        float l = 0.f;
#pragma unroll
        for (int i = 0; i < 8; ++i) {
            const unsigned p01 = pack2(__builtin_amdgcn_exp2f(sv[i][0] - mx), __builtin_amdgcn_exp2f(sv[i][1] - mx));
            const unsigned p23 = pack2(__builtin_amdgcn_exp2f(sv[i][2] - mx), __builtin_amdgcn_exp2f(sv[i][3] - mx));
            pf[i >> 1][(i & 1) * 2] = p01; pf[i >> 1][(i & 1) * 2 + 1] = p23;
            l += (bflo(p01) + bfhi(p01)) + (bflo(p23) + bfhi(p23));
        }
        f32x4 o0 = {0.f, 0.f, 0.f, 0.f}, o1 = {0.f, 0.f, 0.f, 0.f};
#pragma unroll
        for (int j = 0; j < 4; ++j) {
            const v4u a0 = {vf[(j * 2) * 2][0], vf[(j * 2) * 2][1], vf[(j * 2) * 2 + 1][0], vf[(j * 2) * 2 + 1][1]};
            const v4u a1 = {vf[(j * 2 + 1) * 2][0], vf[(j * 2 + 1) * 2][1], vf[(j * 2 + 1) * 2 + 1][0], vf[(j * 2 + 1) * 2 + 1][1]};
            o0 = MFMA(a0, pf[j], o0);
            o1 = MFMA(a1, pf[j], o1);
        }
        v4u v; v[0] = canonb(pack2(o0[0], o0[1])); v[1] = canonb(pack2(o0[2], o0[3])); v[2] = canonb(pack2(o1[0], o1[1])); v[3] = canonb(pack2(o1[2], o1[3]));
        __builtin_amdgcn_raw_buffer_store_b128(v, rs_ws, (int)PCTX_OFF + ((((bb * NKG + kg) * H + (lane & 15)) * DC) + 256 * cg + 32 * wave + 8 * g4) * 2, 0, 16);
        if (wave == 0) {
            l = add16(l); l = add32(l);
            if (lane < 16) __builtin_amdgcn_raw_buffer_store_b64((v2u){canon(__float_as_uint(mx)), canon(__float_as_uint(l))}, rs_ws, (int)ML_OFF + cu * 128 + lane * 8, 0, 16);
        }
    }
    STAMP(4);
    PIN();
    BULK(5);
    unsigned xr0, xr1, hb1; float hp4[4];
    {
        const int b = (tid >> 3) & 1, m = (cu < NC5 ? cu : 0) * 8 + (tid & 7);
        asm volatile("global_load_ushort %0, %1, off" : "=v"(xr0) : "v"(x + b * M + m) : "memory");
        asm volatile("global_load_ushort %0, %1, off" : "=v"(xr1) : "v"(b2 + m) : "memory");
    }
    if (wave == 7) {
        const int l2 = lane < 14 ? lane : 13;
        const int b = l2 / 7, r = l2 % 7, f = cu * 7 + r;
        const float* hp = (const float*)(ws + HP_OFF);
        asm volatile("global_load_ushort %0, %1, off" : "=v"(hb1) : "v"(b1 + f) : "memory");
#pragma unroll
        for (int p = 0; p < 4; ++p) asm volatile("global_load_dword %0, %1, off" : "=v"(hp4[p]) : "v"(hp + ((size_t)p * 2 + b) * F + f) : "memory");
    }
    PIN();
    float xb;
    v2u uf[8], of[8];
    {
        const int pr = tid >> 3, ch = tid & 7;
        const int po = (int)PCTX_OFF + ((pr * H + hh) * DC + 64 * cs + 8 * ch) * 2;
        const int mo = (int)ML_OFF + (pr * 4 + (cs >> 2)) * 128 + hh * 8;
        v4u pc; v2u ml;
        SDLY(E3D);
        XLD4(pc, po); XLD2(ml, mo);
        BULK(6);
        VMW(2 + cnt_at(6));
        asm volatile("" : "+v"(xr0), "+v"(xr1));
        xb = bf2f((unsigned short)xr0) + bf2f((unsigned short)xr1);
        if (wave == 7) {
            asm volatile("" : "+v"(hb1), "+v"(hp4[0]), "+v"(hp4[1]), "+v"(hp4[2]), "+v"(hp4[3]));
            const float hv = bf2f((unsigned short)hb1) + hp4[0] + hp4[1] + hp4[2] + hp4[3];
            const float t = tanhf(0.7978845608028654f * (hv + 0.044715f * hv * hv * hv));
            if (lane < 16) ((LAS float*)(S + L_HM))[lane < 14 ? (lane / 7) * 8 + lane % 7 : (lane - 14) * 8 + 7] = lane < 14 ? 0.5f * hv * (1.0f + t) : 0.f;
        }
        {
            const int q = (lane & 15) >> 2, p = lane & 3;
            const unsigned ua = (unsigned)(size_t)(L + L_WV + wave * 4096 + (8 * g4 + q) * 64 + 8 * p);
#pragma unroll
            for (int j = 0; j < 2; ++j)
#pragma unroll
                for (int t = 0; t < 2; ++t)
#pragma unroll
                    for (int u = 0; u < 2; ++u)
                        asm volatile("ds_read_b64_tr_b16 %0, %1 offset:%2" : "=v"(uf[(j * 2 + t) * 2 + u]) : "v"(ua), "n"(j * 2048 + u * 256 + t * 32) : "memory");
        }
        if (OEARLY) {
        {
            const int q = (lane & 15) >> 2, p = lane & 3;
            const unsigned oa = (unsigned)(size_t)(L + L_WO + wave * 3072 + (8 * (g4 < 2 ? g4 : 2) + q) * 128 + 8 * p);
#pragma unroll
            for (int t = 0; t < 4; ++t)
#pragma unroll
                for (int u = 0; u < 2; ++u)
                    asm volatile("ds_read_b64_tr_b16 %0, %1 offset:%2" : "=v"(of[t * 2 + u]) : "v"(oa), "n"(u * 512 + t * 32) : "memory");
        }
        }
        STAMP(15);
        asm volatile("s_waitcnt vmcnt(%2)" : "+v"(pc), "+v"(ml) : "n"(cnt_at(6)) : "memory");
        for (unsigned spins = 0;;) {
            if (!__any(has_sent(pc) || ml[0] == SENT || ml[1] == SENT)) break;
            if (spin_fail(spins, tmo)) break;
            XLD4(pc, po); XLD2(ml, mo);
            asm volatile("s_waitcnt vmcnt(0)" : "+v"(pc), "+v"(ml) :: "memory");
        }
        STAMP(5);
        BULK(7);
        LAS float* red = (LAS float*)(S + L_QA);
        const float m0 = __uint_as_float(ml[0]), l0 = __uint_as_float(ml[1]);
        const float gm = max_hi3(m0);
        const float e0 = __builtin_amdgcn_exp2f(m0 - gm);
        const float ls = add_hi3(e0 * l0);
        float acc[8];
#pragma unroll
        for (int k = 0; k < 4; ++k) { acc[2 * k] = e0 * bflo(pc[k]); acc[2 * k + 1] = e0 * bfhi(pc[k]); }
#pragma unroll
        for (int k = 0; k < 8; ++k) acc[k] = add_hi3(acc[k]);
        if (lane < 8) {
            *(LAS f32x4*)(red + wave * 64 + lane * 8) = (f32x4){acc[0], acc[1], acc[2], acc[3]};
            *(LAS f32x4*)(red + wave * 64 + lane * 8 + 4) = (f32x4){acc[4], acc[5], acc[6], acc[7]};
        }
        if (lane == 0) ((LAS f32x2*)(S + L_STAT))[wave] = (f32x2){gm, ls};
    }
    asm volatile("s_waitcnt lgkmcnt(0)" : "+v"(uf[0]), "+v"(uf[1]), "+v"(uf[2]), "+v"(uf[3]), "+v"(uf[4]), "+v"(uf[5]), "+v"(uf[6]), "+v"(uf[7]) :: "memory");
    if (OEARLY) asm volatile("" : "+v"(of[0]), "+v"(of[1]), "+v"(of[2]), "+v"(of[3]), "+v"(of[4]), "+v"(of[5]), "+v"(of[6]), "+v"(of[7]));
    BAR();
    STAMP(11);
    {
        const int b = (lane >> 1) & 1;
        const LAS f32x4* red4 = (const LAS f32x4*)(S + L_QA + b * 1024 + g4 * 32);
        const LAS f32x4* st4 = (const LAS f32x4*)(S + L_STAT + b * 32);
        const f32x4 s01 = st4[0], s23 = st4[1];
        f32x4 rv[4][4];
#pragma unroll
        for (int w = 0; w < 4; ++w)
#pragma unroll
            for (int i = 0; i < 4; ++i) rv[w][i] = red4[w * 16 + (i >> 1) * 8 + (i & 1)];
        const float gm = fmaxf(fmaxf(s01[0], s01[2]), fmaxf(s23[0], s23[2]));
        float e[4] = {__builtin_amdgcn_exp2f(s01[0] - gm), __builtin_amdgcn_exp2f(s01[2] - gm), __builtin_amdgcn_exp2f(s23[0] - gm), __builtin_amdgcn_exp2f(s23[2] - gm)};
        const float ls = (e[0] * s01[1] + e[1] * s01[3]) + (e[2] * s23[1] + e[3] * s23[3]);
        const float inv = 1.0f / ls;
        v4u bf[2];
#pragma unroll
        for (int i = 0; i < 4; ++i) {
            const f32x4 c4 = ((rv[0][i] * e[0] + rv[1][i] * e[1]) + (rv[2][i] * e[2] + rv[3][i] * e[3])) * inv;
            unsigned h01 = pack2(c4[0], c4[1]), h23 = pack2(c4[2], c4[3]);
            const unsigned l01 = pack2(c4[0] - bflo(h01), c4[1] - bfhi(h01)), l23 = pack2(c4[2] - bflo(h23), c4[3] - bfhi(h23));
            bf[i >> 1][(i & 1) * 2] = (lane & 1) ? l01 : h01;
            bf[i >> 1][(i & 1) * 2 + 1] = (lane & 1) ? l23 : h23;
        }
        f32x4 o0 = {0.f, 0.f, 0.f, 0.f}, o1 = {0.f, 0.f, 0.f, 0.f};
#pragma unroll
        for (int j = 0; j < 2; ++j) {
            const v4u a0 = {uf[(j * 2) * 2][0], uf[(j * 2) * 2][1], uf[(j * 2) * 2 + 1][0], uf[(j * 2) * 2 + 1][1]};
            const v4u a1 = {uf[(j * 2 + 1) * 2][0], uf[(j * 2 + 1) * 2][1], uf[(j * 2 + 1) * 2 + 1][0], uf[(j * 2 + 1) * 2 + 1][1]};
            o0 = MFMA(a0, bf[j], o0);
            o1 = MFMA(a1, bf[j], o1);
        }
        v4u v;
#pragma unroll
        for (int r = 0; r < 4; ++r) {
            const float t0 = o0[r] + dppf<0xB1>(o0[r]), t1 = o1[r] + dppf<0xB1>(o1[r]);
            v[r] = canon(__float_as_uint((lane & 1) ? t1 : t0));
        }
        if ((lane & 15) < 4)
            __builtin_amdgcn_raw_buffer_store_b128(v, rs_ws, (int)G4_OFF + ((((hh * 16 + 2 * wave + (lane & 1)) * 16 + cs) * 32) + b * 16 + 4 * g4) * 4, 0, 16);
    }
    STAMP(6);
    PIN();
    if (wave < 7) {
        if (!OEARLY) {
            VM0();
        {
            const int q = (lane & 15) >> 2, p = lane & 3;
            const unsigned oa = (unsigned)(size_t)(L + L_WO + wave * 3072 + (8 * (g4 < 2 ? g4 : 2) + q) * 128 + 8 * p);
#pragma unroll
            for (int t = 0; t < 4; ++t)
#pragma unroll
                for (int u = 0; u < 2; ++u)
                    asm volatile("ds_read_b64_tr_b16 %0, %1 offset:%2" : "=v"(of[t * 2 + u]) : "v"(oa), "n"(u * 512 + t * 32) : "memory");
        }
        }
        const int b = (lane >> 1) & 1;
        const f32x4 hm0 = *(const LAS f32x4*)(S + L_HM + b * 32), hm1 = *(const LAS f32x4*)(S + L_HM + b * 32 + 16);
        const int csx = (lane & 1) | (((lane >> 2) & 3) << 1) | ((lane >> 5) << 3);
        const int go = (int)G4_OFF + (cu * 16 + csx) * 128 + b * 64 + (g4 & 1) * 32;
        v4u g0, g1;
        SDLY(E4D);
        for (unsigned spins = 0;;) {
            XLD4(g0, go); XLD4(g1, go + 16);
            asm volatile("s_waitcnt vmcnt(0)" : "+v"(g0), "+v"(g1) :: "memory");
            if (!__any(has_sent(g0) || has_sent(g1))) break;
            if (spin_fail(spins, tmo)) break;
        }
        STAMP(7);
        float t[8];
#pragma unroll
        for (int k = 0; k < 8; ++k) {
            float v = __uint_as_float(k < 4 ? g0[k & 3] : g1[k & 3]);
            v += dppf<0xB1>(v); v += dppf<0x124>(v); v += dppf<0x128>(v); v = add32(v);
            const float h = k < 4 ? hm0[k & 3] : hm1[k & 3];
            t[k] = g4 < 2 ? v : (g4 == 2 ? h : 0.f);
        }
        v4u bfr;
#pragma unroll
        for (int k = 0; k < 4; ++k) {
            const unsigned h = pack2(t[2 * k], t[2 * k + 1]);
            const unsigned l = pack2(t[2 * k] - bflo(h), t[2 * k + 1] - bfhi(h));
            bfr[k] = (lane & 1) ? l : h;
        }
        asm volatile("s_waitcnt lgkmcnt(0)" : "+v"(of[0]), "+v"(of[1]), "+v"(of[2]), "+v"(of[3]), "+v"(of[4]), "+v"(of[5]), "+v"(of[6]), "+v"(of[7]) :: "memory");
        f32x4 o[4];
#pragma unroll
        for (int tt = 0; tt < 4; ++tt) {
            const v4u a = {of[2 * tt][0], of[2 * tt][1], of[2 * tt + 1][0], of[2 * tt + 1][1]};
            o[tt] = (f32x4){0.f, 0.f, 0.f, 0.f};
            o[tt] = MFMA(a, bfr, o[tt]);
        }
#pragma unroll
        for (int pp = 0; pp < 2; ++pp) {
            v4u v;
#pragma unroll
            for (int r = 0; r < 4; ++r) {
                const float t0 = o[2 * pp][r] + dppf<0xB1>(o[2 * pp][r]), t1 = o[2 * pp + 1][r] + dppf<0xB1>(o[2 * pp + 1][r]);
                v[r] = canon(__float_as_uint((lane & 1) ? t1 : t0));
            }
            if ((lane & 15) < 4)
                __builtin_amdgcn_raw_buffer_store_b128(v, rs_ws, (int)G5_OFF + (((8 * wave + 2 * (2 * pp + (lane & 1)) + (g4 >> 1)) * 256 + cu) * 16 + b * 8 + (g4 & 1) * 4) * 4, 0, 16);
        }
    }
    STAMP(8);
    if (cu < NC5) {
        v4u v0 = {SENT, SENT, SENT, SENT}, v1 = {SENT, SENT, SENT, SENT};
        for (unsigned spins = 0;;) {
            if (has_sent(v0)) v0 = __builtin_amdgcn_raw_buffer_load_b128(rs_ws, (int)(G5_OFF + (size_t)cu * 16384 + tid * 16), 0, 16);
            if (has_sent(v1)) v1 = __builtin_amdgcn_raw_buffer_load_b128(rs_ws, (int)(G5_OFF + (size_t)cu * 16384 + 8192 + tid * 16), 0, 16);
            if (!__any(has_sent(v0) || has_sent(v1))) break;
            if (spin_fail(spins, tmo)) break;
        }
        f32x4 s; s[0] = __uint_as_float(v0[0]) + __uint_as_float(v1[0]); s[1] = __uint_as_float(v0[1]) + __uint_as_float(v1[1]);
        s[2] = __uint_as_float(v0[2]) + __uint_as_float(v1[2]); s[3] = __uint_as_float(v0[3]) + __uint_as_float(v1[3]);
#pragma unroll
        for (int k = 0; k < 4; ++k) { float v = s[k]; v += dppf<0x124>(v); s[k] = add_hi3(v); }
        LAS float* red = (LAS float*)(S + L_CC);
        if (lane < 4) *(LAS f32x4*)(red + wave * 16 + lane * 4) = s;
        LGKM0();
        BAR();
        if (tid < 16) {
            float t = 0.f;
#pragma unroll
            for (int w = 0; w < 8; ++w) t += red[w * 16 + tid];
            const int b = tid >> 3, m = cu * 8 + (tid & 7);
            t += xb;
            out[b * M + m] = (unsigned short)f2bf(t);
        }
        STAMP(9);
    }
}
}

extern "C" void kernel_launch(void* const* d_in, const int* in_sizes, int n_in,
                              void* d_out, int out_size, void* d_ws, size_t ws_size,
                              hipStream_t stream) {
    const unsigned short* x = (const unsigned short*)d_in[0];
    const unsigned short* ckv = (const unsigned short*)d_in[1];
    const unsigned short* wq = (const unsigned short*)d_in[2];
    const unsigned short* wuk = (const unsigned short*)d_in[3];
    const unsigned short* wuv = (const unsigned short*)d_in[4];
    const unsigned short* wo = (const unsigned short*)d_in[5];
    const unsigned short* w1 = (const unsigned short*)d_in[7];
    const unsigned short* b1 = (const unsigned short*)d_in[8];
    const unsigned short* w2 = (const unsigned short*)d_in[9];
    const unsigned short* b2 = (const unsigned short*)d_in[10];
    unsigned char* ws = (unsigned char*)d_ws;
    k1<<<368, 320, 0, stream>>>(x, wq, w1, ws);
    k2<<<NCU, 512, 0, stream>>>(x, ckv, wuk, wuv, wo, b1, w2, b2, ws, (unsigned short*)d_out);
}
```

```cpp
#include <hip/hip_runtime.h>
#include <stdint.h>

#define GAS __attribute__((address_space(1)))
#define LAS __attribute__((address_space(3)))
typedef unsigned v4u __attribute__((ext_vector_type(4)));
typedef unsigned v2u __attribute__((ext_vector_type(2)));
typedef float f32x4 __attribute__((ext_vector_type(4)));
typedef float f32x2 __attribute__((ext_vector_type(2)));
typedef short bf16x8 __attribute__((ext_vector_type(8)));
typedef short s16x4 __attribute__((ext_vector_type(4)));
typedef __bf16 bf16x2_t __attribute__((ext_vector_type(2)));
typedef GAS unsigned gu32;
typedef GAS unsigned long long gu64;
#define RLX_AGENT __ATOMIC_RELAXED, __HIP_MEMORY_SCOPE_AGENT

#define STAMPS 0
#define WSEND 0
#define FBAR 1
#define CKV 0
#define FST 0
#define S2M 0
#define PFD 0
#define K1PF 0
#define WIN 16
#define E2D2 0
#define E2D 8
#define FILLM 0
#define QPM 0
#define E3D 0
#define ESD 2
#define PU 4444
#define PO 555555555
#define ROT 2
#define MRT 1
#define E3O 0
#define E4D 0
#define SDLY(n) do { _Pragma("unroll") for (int i_ = 0; i_ < (n) / 4; ++i_) __builtin_amdgcn_s_sleep(16); if ((n) % 4) __builtin_amdgcn_s_sleep(4 * ((n) % 4)); } while (0)
#define PIN() __builtin_amdgcn_sched_barrier(0)

namespace {
constexpr int H = 16, SK = 4096, D = 256, M = 448, DC = 1024, F = 1792;
constexpr int NCU = 256;
constexpr int NKG = 32, KPG = 128;
constexpr int NC5 = 56;

constexpr size_t TMO_OFF = 0;
constexpr size_t QABS_OFF = 4096;
constexpr size_t ML_OFF = QABS_OFF + 131072;
constexpr size_t SP_OFF = ML_OFF + 32768;
constexpr size_t PCTX_OFF = SP_OFF + (size_t)256 * 8192;
constexpr size_t G5_OFF = PCTX_OFF + (size_t)2 * NKG * 16 * 1024 * 2;
constexpr size_t G5_BYTES = (size_t)56 * 256 * 16 * 4;
constexpr size_t FILL_BYTES = G5_OFF + G5_BYTES;
constexpr size_t QP_OFF = FILL_BYTES;
constexpr size_t HP_OFF = QP_OFF + 4 * 2 * 4096 * 4;
constexpr size_t STAMP_OFF = HP_OFF + 65536;
constexpr size_t K1ST_OFF = STAMP_OFF + 256 * 16 * 8;
constexpr size_t WS_NEED = K1ST_OFF + 512 * 2 * 8;
static_assert(FILL_BYTES % 16 == 0, "fill");

constexpr int RS2 = 1040;
constexpr int L_CKV = 0;
constexpr int L_WK = 64 * RS2;
constexpr int L_WV = L_WK;
constexpr int L_WO = L_WK + 32 * RS2;
constexpr int L_WO2 = L_CKV;
constexpr int LDS_DMA_BYTES = L_WO + 7 * 5120;
constexpr int L_SC = 0;
constexpr int L_QF = 8448;
constexpr int L_CB = L_QF;
constexpr int L_QA = L_QF + 16384;
constexpr int L_CC = L_QA + 2048;
constexpr int L_HM = L_CC + 512;
constexpr int LDS_SC_BYTES = L_HM + 64;
static_assert(LDS_DMA_BYTES + LDS_SC_BYTES <= 160 * 1024, "lds");

constexpr unsigned kSpinCap = 1u << 18;
constexpr unsigned SENT = 0xFFFFFFFFu;

__device__ __forceinline__ float bf2f(unsigned short u) { return __uint_as_float(((unsigned)u) << 16); }
__device__ __forceinline__ float bflo(unsigned u) { return __uint_as_float(u << 16); }
__device__ __forceinline__ float bfhi(unsigned u) { return __uint_as_float(u & 0xffff0000u); }
__device__ __forceinline__ unsigned f2bf(float f) { unsigned u = __float_as_uint(f); return (u + 0x7fffu + ((u >> 16) & 1u)) >> 16; }
__device__ __forceinline__ unsigned pack2(float a, float b) { const f32x2 v = {a, b}; return __builtin_bit_cast(unsigned, __builtin_convertvector(v, bf16x2_t)); }
template <int C> __device__ __forceinline__ float dppf(float v) { return __int_as_float(__builtin_amdgcn_update_dpp(0, __float_as_int(v), C, 0xF, 0xF, true)); }
__device__ __forceinline__ float add16(float v) { auto r = __builtin_amdgcn_permlane16_swap(__float_as_uint(v), __float_as_uint(v), false, false); return __uint_as_float(r[0]) + __uint_as_float(r[1]); }
__device__ __forceinline__ float add32(float v) { auto r = __builtin_amdgcn_permlane32_swap(__float_as_uint(v), __float_as_uint(v), false, false); return __uint_as_float(r[0]) + __uint_as_float(r[1]); }
__device__ __forceinline__ float max16(float v) { auto r = __builtin_amdgcn_permlane16_swap(__float_as_uint(v), __float_as_uint(v), false, false); return fmaxf(__uint_as_float(r[0]), __uint_as_float(r[1])); }
__device__ __forceinline__ float max32(float v) { auto r = __builtin_amdgcn_permlane32_swap(__float_as_uint(v), __float_as_uint(v), false, false); return fmaxf(__uint_as_float(r[0]), __uint_as_float(r[1])); }
__device__ __forceinline__ float add_hi3(float v) { v += dppf<0x128>(v); v = add16(v); return add32(v); }
__device__ __forceinline__ float max_hi3(float v) { v = fmaxf(v, dppf<0x128>(v)); v = max16(v); return max32(v); }
__device__ __forceinline__ float add_lo5(float v) { v += dppf<0xB1>(v); v += dppf<0x4E>(v); v += dppf<0x141>(v); v += dppf<0x140>(v); return add16(v); }
__device__ __forceinline__ float max_lo5(float v) { v = fmaxf(v, dppf<0xB1>(v)); v = fmaxf(v, dppf<0x4E>(v)); v = fmaxf(v, dppf<0x141>(v)); v = fmaxf(v, dppf<0x140>(v)); return max16(v); }
__device__ __forceinline__ float wsum(float v) { return add32(add_lo5(v)); }
__device__ __forceinline__ unsigned long long rt() { return __builtin_amdgcn_s_memrealtime(); }

#define BAR() __builtin_amdgcn_s_barrier()
#define LGKM0() asm volatile("s_waitcnt lgkmcnt(0)" ::: "memory")
#define VM0() asm volatile("s_waitcnt vmcnt(0)" ::: "memory")
#define MFMA(a, b, c) __builtin_amdgcn_mfma_f32_16x16x32_bf16(__builtin_bit_cast(bf16x8, a), __builtin_bit_cast(bf16x8, b), c, 0, 0, 0)

__global__ void __launch_bounds__(320) k1(const unsigned short* __restrict__ x, const unsigned short* __restrict__ wq,
                                          const unsigned short* __restrict__ w1, unsigned char* __restrict__ ws) {
    __shared__ float xn[2][M];
    __shared__ float red[4][2][64];
    const int tid = threadIdx.x, wave = __builtin_amdgcn_readfirstlane(tid >> 6), lane = tid & 63;
    const int blk = blockIdx.x;
    const bool isq = blk < 256;
    if (wave == 4) {
        const v4u sn = {SENT, SENT, SENT, SENT};
        const unsigned tot16 = (unsigned)(FILL_BYTES / 16);
        const __amdgpu_buffer_rsrc_t rsf = __builtin_amdgcn_make_buffer_rsrc(ws, 0, (int)WS_NEED, 0x00020000);
        SDLY(PFD);
        for (unsigned i = blk * 64 + lane; i < tot16; i += gridDim.x * 64) __builtin_amdgcn_raw_buffer_store_b128(sn, rsf, (int)(i * 16), 0, FILLM);
        return;
    }
    const int idx = isq ? blk : blk - 256;
    const int mq = idx & 3, cg = idx >> 2;
    const int ncols = isq ? 4096 : F;
    const unsigned short* W = (isq ? wq : w1) + (size_t)(mq * 112) * ncols + cg * 64;
    const int r = tid >> 3, cch = tid & 7;
    unsigned xr[7];
#pragma unroll
    for (int i = 0; i < 7; ++i) asm volatile("global_load_ushort %0, %1, off" : "=v"(xr[i]) : "v"(x + (wave & 1) * M + lane + 64 * i) : "memory");
    v4u wv[4];
#pragma unroll
    for (int i = 0; i < 4; ++i) {
        const int row = (i < 3 || r < 16) ? r + 32 * i : 111;
        asm volatile("global_load_dwordx4 %0, %1, off nt" : "=v"(wv[i]) : "v"(W + (size_t)row * ncols + cch * 8) : "memory");
    }
    PIN();
    asm volatile("s_waitcnt vmcnt(4)" : "+v"(xr[0]), "+v"(xr[1]), "+v"(xr[2]), "+v"(xr[3]), "+v"(xr[4]), "+v"(xr[5]), "+v"(xr[6]) :: "memory");
    PIN();
    if (wave < 2) {
        float v[7]; float s = 0.f;
#pragma unroll
        for (int i = 0; i < 7; ++i) { v[i] = bf2f((unsigned short)xr[i]); s += v[i]; }
        s = wsum(s); const float mean = s * (1.0f / M);
        float ss = 0.f;
#pragma unroll
        for (int i = 0; i < 7; ++i) { const float d = v[i] - mean; ss += d * d; }
        ss = wsum(ss);
        const float rs = 1.0f / sqrtf(ss * (1.0f / M) + 1e-5f);
#pragma unroll
        for (int i = 0; i < 7; ++i) xn[wave][lane + 64 * i] = (v[i] - mean) * rs;
    }
    LGKM0(); BAR();
    asm volatile("s_waitcnt vmcnt(0)" : "+v"(wv[0]), "+v"(wv[1]), "+v"(wv[2]), "+v"(wv[3]) :: "memory");
    float a[2][8];
#pragma unroll
    for (int j = 0; j < 8; ++j) { a[0][j] = 0.f; a[1][j] = 0.f; }
#pragma unroll
    for (int i = 0; i < 4; ++i) {
        int row = mq * 112 + r + 32 * i; if (row >= M) row = M - 1;
        const bool ok = (i < 3 || r < 16);
        const float x0 = ok ? xn[0][row] : 0.f, x1 = ok ? xn[1][row] : 0.f;
#pragma unroll
        for (int j = 0; j < 4; ++j) {
            const float lo = bflo(wv[i][j]), hi = bfhi(wv[i][j]);
            a[0][2 * j] += x0 * lo; a[0][2 * j + 1] += x0 * hi;
            a[1][2 * j] += x1 * lo; a[1][2 * j + 1] += x1 * hi;
        }
    }
#pragma unroll
    for (int b = 0; b < 2; ++b)
#pragma unroll
        for (int j = 0; j < 8; ++j) a[b][j] = add_hi3(a[b][j]);
    if (lane < 8) {
#pragma unroll
        for (int b = 0; b < 2; ++b)
#pragma unroll
            for (int j = 0; j < 8; ++j) red[wave][b][lane * 8 + j] = a[b][j];
    }
    LGKM0(); BAR();
    if (tid < 128) {
        const int b = tid >> 6, c = tid & 63;
        const float s = red[0][b][c] + red[1][b][c] + red[2][b][c] + red[3][b][c];
        float* dst = (float*)(ws + (isq ? QP_OFF : HP_OFF));
        dst[((size_t)mq * 2 + b) * ncols + cg * 64 + c] = s;
    }
}

__device__ __forceinline__ bool spin_fail(unsigned& spins, gu32* tmo) {
    __builtin_amdgcn_s_sleep(1);
    if (++spins > kSpinCap) { __hip_atomic_store(tmo, 1u, RLX_AGENT); return true; }
    return (spins & 1023u) == 0u && __builtin_amdgcn_readfirstlane(__hip_atomic_load(tmo, RLX_AGENT)) == 1u;
}
__device__ __forceinline__ unsigned umax(unsigned a, unsigned b) { return a > b ? a : b; }
__device__ __forceinline__ unsigned smax(v4u v) { return umax(umax(umax(v[0], v[1]), v[2]), v[3]); }
__device__ __forceinline__ bool has_sent(v4u v) { return smax(v) == SENT; }
__device__ __forceinline__ unsigned canon(unsigned u) { return u == SENT ? 0x7FC00000u : u; }
__device__ __forceinline__ unsigned canonb(unsigned u) { return u == SENT ? 0xFFC0FFC0u : u; }

#define GLDS_NT(src, dst) __builtin_amdgcn_global_load_lds((const unsigned*)(src), (LAS unsigned*)(dst), 16, 0, 2)
#define OAUX 0
#define GLDS_O(src, dst) __builtin_amdgcn_global_load_lds((const unsigned*)(src), (LAS unsigned*)(dst), 16, 0, OAUX)
#define STAMP(i) do {} while (0)
#define STAMPW(i, w) do {} while (0)

constexpr int dig(int v, int i) { return i == 0 ? v % 10 : dig(v / 10, i - 1); }
constexpr int rank_of(int p) { return p == 1 ? 0 : (p == 9 ? 1 : (p == 8 ? 5 : (p >= 5 ? p + 1 : p))); }
constexpr int cnt_u(int p) { int n = 0; for (int i = 0; i < 4; ++i) n += dig(PU, i) == p; return n; }
constexpr int cnt_o(int p) { int n = 0; for (int i = 0; i < 9; ++i) n += dig(PO, i) == p; return n; }
constexpr int last_u() { int l = 0; for (int i = 0; i < 4; ++i) if (rank_of(dig(PU, i)) > l) l = rank_of(dig(PU, i)); return l; }
constexpr int o_after_u(int p) {
    int n = 0; for (int i = 0; i < 9; ++i) n += (rank_of(dig(PO, i)) >= last_u() && rank_of(dig(PO, i)) <= rank_of(p)); return n; }
static_assert(last_u() <= rank_of(5), "w_uv");
static_assert(rank_of(dig(PO, 5)) >= 5 && rank_of(dig(PO, 6)) >= 5 && rank_of(dig(PO, 7)) >= 5 && rank_of(dig(PO, 8)) >= 5, "w_o late part");
#define BULK(p) do { \
        _Pragma("unroll") for (int i = 0; i < 4; ++i) if (dig(PU, i) == (p)) \
            GLDS_NT(wuv + ((size_t)(256 * cq + 32 * wave + 8 * i + (lane >> 3)) * H + th) * D + 64 * dq + (((lane & 7) ^ ((lane >> 3) & 2) ^ ((i & 1) << 2)) * 8), L + L_WV + wave * 4096 + i * 1024); \
        if (cnt_o(p) && wave < 7) { \
            _Pragma("unroll") for (int i = 0; i < 9; ++i) if (dig(PO, i) == (p)) { \
                const int row = lane >> 3; \
                const unsigned short* sp = i > 0 ? wo + ((size_t)th * D + 64 * dq + 8 * ((i - 1 + ROT * cq) & 7) + row) * M : w2 + ((size_t)cu * 7 + (row < 7 ? row : 6)) * M; \
                GLDS_O(sp + 64 * wave + (((lane & 7) ^ (row & 2) ^ (i > 0 ? ((i - 1) & 1) << 2 : 0)) * 8), L + (i < 5 ? L_WO + wave * 5120 + i * 1024 : L_WO2 + wave * 4096 + (i - 5) * 1024)); \
            } \
        } \
        PIN(); \
    } while (0)
#define VMW2(a, b) do { if ((b) > 0 && wave == 7) VMW(a); else VMW((a) + (b)); } while (0)
#define VMW(n) asm volatile("s_waitcnt vmcnt(%0)" :: "n"(n) : "memory")
#define XB 0
#define XBITS "sc1"
#define XLD4(dst, off) asm volatile("buffer_load_dwordx4 %0, %1, %2, 0 offen " XBITS : "=v"(dst) : "v"(off), "s"(rs_ws) : "memory")
#define XLD4R(dst, off) asm volatile("buffer_load_dwordx4 %0, %1, %2, 0 offen " XBITS : "+v"(dst) : "v"(off), "s"(rs_ws) : "memory")
#define XLD2(dst, off) asm volatile("buffer_load_dwordx2 %0, %1, %2, 0 offen sc1" : "=v"(dst) : "v"(off), "s"(rs_ws) : "memory")
__global__ void __launch_bounds__(512, 1) k2(const unsigned short* __restrict__ x, const unsigned short* __restrict__ ckv,
                                             const unsigned short* __restrict__ wuk, const unsigned short* __restrict__ wuv,
                                             const unsigned short* __restrict__ wo, const unsigned short* __restrict__ b1,
                                             const unsigned short* __restrict__ w2, const unsigned short* __restrict__ b2,
                                             unsigned char* __restrict__ ws, unsigned short* __restrict__ out) {
    __shared__ __attribute__((aligned(16))) unsigned char lds_dma[LDS_DMA_BYTES];
    __shared__ __attribute__((aligned(16))) unsigned char lds_sc[LDS_SC_BYTES];
    LAS unsigned char* L = (LAS unsigned char*)lds_dma;
    LAS unsigned char* S = (LAS unsigned char*)lds_sc;
    const int tid = threadIdx.x, wave = __builtin_amdgcn_readfirstlane(tid >> 6), lane = tid & 63;
    const int cu = blockIdx.x;
    const int hh = cu >> 4, cs = cu & 15;
    const int bb = cu >> 7, kg = (cu >> 2) & 31, cg = cu & 3;
    const int th = (cu & 7) * 2 + (cu >> 7), dq = (cu >> 5) & 3, cq = (cu >> 3) & 3;
    const int g4 = lane >> 4;
    gu32* tmo = (gu32*)(ws + TMO_OFF);
    const __amdgpu_buffer_rsrc_t rs_ws = __builtin_amdgcn_make_buffer_rsrc(ws, 0, (int)WS_NEED, 0x00020000);
    STAMP(0);

    float qp[4];
    {
        const float* p = (const float*)(ws + QP_OFF) + (size_t)(tid >> 8) * 4096 + hh * D + (tid & 255);
#pragma unroll
        for (int i = 0; i < 4; ++i) asm volatile("global_load_dword %0, %1, off" : "=v"(qp[i]) : "v"(p + (size_t)i * 8192) : "memory");
    }
    unsigned xr0, xr1, hb1; float hp4[4];
    {
        const int b = (tid >> 3) & 1, m = (cu < NC5 ? cu : 0) * 8 + (tid & 7);
        asm volatile("global_load_ushort %0, %1, off" : "=v"(xr0) : "v"(x + b * M + m) : "memory");
        asm volatile("global_load_ushort %0, %1, off" : "=v"(xr1) : "v"(b2 + m) : "memory");
    }
    if (wave == 7) {
        const int l2 = lane < 14 ? lane : 13;
        const int b = l2 / 7, r = l2 % 7, f = cu * 7 + r;
        const float* hp = (const float*)(ws + HP_OFF);
        asm volatile("global_load_ushort %0, %1, off" : "=v"(hb1) : "v"(b1 + f) : "memory");
#pragma unroll
        for (int p = 0; p < 4; ++p) asm volatile("global_load_dword %0, %1, off" : "=v"(hp4[p]) : "v"(hp + ((size_t)p * 2 + b) * F + f) : "memory");
    }
    if (wave < 4) {
#pragma unroll
        for (int i = 0; i < 8; ++i) {
            const int r = 2 * i + (lane >> 5);
            const int ck = (lane & 31) ^ ((r & 7) << 2);
            GLDS_NT(wuk + ((size_t)(64 * cs + 16 * wave + r) * H + hh) * D + 8 * ck, L + L_WK + (8 * wave + i) * RS2);
        }
    }
    PIN();
    BAR();
    PIN();
    {
        const unsigned short* src = ckv + ((size_t)bb * SK + (size_t)kg * KPG + 16 * wave) * DC + 256 * cg;
#pragma unroll
        for (int i = 0; i < 8; ++i) {
            const int r = 2 * i + (lane >> 5);
            const int ck = (lane & 31) ^ ((r & 7) << 2);
            GLDS_NT(src + (size_t)r * DC + 8 * ck, L + L_CKV + (8 * wave + i) * RS2);
        }
    }
    PIN();
    if (wave < 4) asm volatile("s_waitcnt vmcnt(16)" ::: "memory");
    else asm volatile("s_waitcnt vmcnt(8)" ::: "memory");
    asm volatile("" : "+v"(qp[0]), "+v"(qp[1]), "+v"(qp[2]), "+v"(qp[3]), "+v"(xr0), "+v"(xr1));
    const float xb = bf2f((unsigned short)xr0) + bf2f((unsigned short)xr1);
    if (wave == 7) {
        asm volatile("" : "+v"(hb1), "+v"(hp4[0]), "+v"(hp4[1]), "+v"(hp4[2]), "+v"(hp4[3]));
        const float hv = bf2f((unsigned short)hb1) + hp4[0] + hp4[1] + hp4[2] + hp4[3];
        const float t = tanhf(0.7978845608028654f * (hv + 0.044715f * hv * hv * hv));
        const float gl = lane < 14 ? 0.5f * hv * (1.0f + t) : 0.f;
        const unsigned hi = f2bf(gl), lo = f2bf(gl - __uint_as_float(hi << 16));
        const int ix = lane < 14 ? (lane / 7) * 16 + lane % 7 : (lane - 14) * 16 + 7;
        if (lane < 16) { ((LAS unsigned short*)(S + L_HM))[ix] = (unsigned short)hi; ((LAS unsigned short*)(S + L_HM))[ix + 8] = (unsigned short)lo; }
    }
    {
        float s = (qp[0] + qp[1]) + (qp[2] + qp[3]);
        s *= 0.0625f * 1.4426950408889634f;
        const unsigned hi = f2bf(s);
        const unsigned lo = f2bf(s - __uint_as_float(hi << 16));
        const int b = tid >> 8, d = tid & 255;
        ((LAS unsigned short*)(S + L_QA))[(2 * b) * 256 + d] = (unsigned short)hi;
        ((LAS unsigned short*)(S + L_QA))[(2 * b + 1) * 256 + d] = (unsigned short)lo;
    }
    LGKM0();
    BAR();
    STAMPW(1, 5);
    if (wave < 4) {
        v4u av[8], wk[8];
#pragma unroll
        for (int ks = 0; ks < 8; ++ks) av[ks] = *(const LAS v4u*)(S + L_QA + (lane & 3) * 512 + (32 * ks + 8 * g4) * 2);
        asm volatile("s_waitcnt vmcnt(8)" ::: "memory");
        {
            const unsigned wa = (unsigned)(size_t)(L + L_WK + (8 * wave + ((lane & 15) >> 1)) * RS2 + (lane & 1) * 512 + g4 * 16);
#pragma unroll
            for (int ks = 0; ks < 8; ++ks) asm volatile("ds_read_b128 %0, %1" : "=v"(wk[ks]) : "v"(wa + ((ks ^ (lane & 7)) << 6)) : "memory");
            asm volatile("s_waitcnt lgkmcnt(0)" : "+v"(wk[0]), "+v"(wk[1]), "+v"(wk[2]), "+v"(wk[3]), "+v"(wk[4]), "+v"(wk[5]), "+v"(wk[6]), "+v"(wk[7]) :: "memory");
        }
        f32x4 acc0 = {0.f, 0.f, 0.f, 0.f}, acc1 = {0.f, 0.f, 0.f, 0.f};
#pragma unroll
        for (int ks = 0; ks < 8; ks += 2) { acc0 = MFMA(wk[ks], av[ks], acc0); acc1 = MFMA(wk[ks + 1], av[ks + 1], acc1); }
        acc0 += acc1;
        unsigned hb[4];
#pragma unroll
        for (int r = 0; r < 4; ++r) {
            const float v = acc0[r] + dppf<0xB1>(acc0[r]);
            const unsigned h0 = f2bf(v);
            hb[r] = (lane & 1) ? f2bf(v - __uint_as_float(h0 << 16)) : h0;
        }
        if ((lane & 15) < 4) {
            const v2u v = {canonb(hb[0] | (hb[1] << 16)), canonb(hb[2] | (hb[3] << 16))};
            __builtin_amdgcn_raw_buffer_store_b64(v, rs_ws, (int)QABS_OFF + ((((((lane >> 1) & 1) * 4 + (cs >> 2)) * 2 + (lane & 1)) * 8 + ((2 * cs + (wave >> 1)) & 7)) * 64 + hh + 16 * (2 * (wave & 1) + (g4 >> 1))) * 16 + (g4 & 1) * 8, 0, 16);
        }
        STAMP(2);
    }
    PIN();
    BAR();
    SDLY(E2D);
    PIN();
    const int qoff = (int)QABS_OFF + ((bb * 4 + cg) * 2 * 8 + wave) * 1024 + lane * 16;
    v4u qa[2];
    XLD4(qa[0], qoff);
    XLD4(qa[1], qoff + 8192);
    asm volatile("s_waitcnt vmcnt(2)" ::: "memory");
    STAMPW(13, 7);
    v4u kf[8];
    {
        const unsigned ka = (unsigned)(size_t)(L + L_CKV + (8 * wave + ((lane & 15) >> 1)) * RS2 + (lane & 1) * 512 + g4 * 16);
#pragma unroll
        for (int ks = 0; ks < 8; ++ks) asm volatile("ds_read_b128 %0, %1" : "=v"(kf[ks]) : "v"(ka + ((ks ^ (lane & 7)) << 6)) : "memory");
    }
    BULK(1);
    VMW2(cnt_u(1), cnt_o(1));
    asm volatile("" : "+v"(qa[0]), "+v"(qa[1]));
    for (unsigned spins = 0;;) {
        if (!__any(umax(smax(qa[0]), smax(qa[1])) == SENT)) break;
        if (spin_fail(spins, tmo)) break;
        XLD4(qa[0], qoff);
        XLD4(qa[1], qoff + 8192);
        asm volatile("s_waitcnt vmcnt(0)" : "+v"(qa[0]), "+v"(qa[1]) :: "memory");
    }
    *(LAS v4u*)(S + L_QF + wave * 1024 + lane * 16) = qa[0];
    *(LAS v4u*)(S + L_QF + 8192 + wave * 1024 + lane * 16) = qa[1];
    asm volatile("s_waitcnt lgkmcnt(0)" : "+v"(kf[0]), "+v"(kf[1]), "+v"(kf[2]), "+v"(kf[3]), "+v"(kf[4]), "+v"(kf[5]), "+v"(kf[6]), "+v"(kf[7]) :: "memory");
    BAR();
    STAMPW(12, 7);
    BULK(9);
    f32x4 sown;
    {
        f32x4 acc0 = {0.f, 0.f, 0.f, 0.f}, acc1 = {0.f, 0.f, 0.f, 0.f};
#pragma unroll
        for (int ks = 0; ks < 8; ++ks) {
            const v4u ah = *(const LAS v4u*)(S + L_QF + ks * 1024 + lane * 16);
            const v4u al = *(const LAS v4u*)(S + L_QF + 8192 + ks * 1024 + lane * 16);
            acc0 = MFMA(ah, kf[ks], acc0);
            acc1 = MFMA(al, kf[ks], acc1);
        }
        sown = acc0 + acc1;
        v4u v; v[0] = canon(__float_as_uint(sown[0])); v[1] = canon(__float_as_uint(sown[1])); v[2] = canon(__float_as_uint(sown[2])); v[3] = canon(__float_as_uint(sown[3]));
        __builtin_amdgcn_raw_buffer_store_b128(v, rs_ws, (int)SP_OFF + cu * 8192 + wave * 1024 + lane * 16, 0, 16);
    }
    STAMP(3);
    PIN();
    BULK(2);
    v2u vf[16];
    {
        const int q = (lane & 15) >> 2, p = lane & 3;
#pragma unroll
        for (int u = 0; u < 2; ++u) {
            const unsigned va = (unsigned)(size_t)(L + L_CKV + (4 * g4 + 2 * u + (q >> 1)) * RS2 + (q & 1) * 512 + ((wave ^ (4 * u + q)) << 6) + 16 * p);
#pragma unroll
            for (int j = 0; j < 4; ++j)
#pragma unroll
                for (int t = 0; t < 2; ++t)
                    asm volatile("ds_read_b64_tr_b16 %0, %1 offset:%2" : "=v"(vf[(j * 2 + t) * 2 + u]) : "v"(va), "n"(j * 16 * RS2 + 8 * t) : "memory");
        }
    }
    SDLY(ESD);
    PIN();
    {
        v4u e[3];
        const int eb = (int)SP_OFF + (cu & ~3) * 8192 + wave * 1024 + lane * 16;
        const int eo0 = eb + (cg < 1 ? 1 : 0) * 8192, eo1 = eb + (cg < 2 ? 2 : 1) * 8192, eo2 = eb + (cg < 3 ? 3 : 2) * 8192;
        XLD4(e[0], eo0); XLD4(e[1], eo1); XLD4(e[2], eo2);
        BULK(3);
        VMW2(cnt_u(3), cnt_o(3));
        asm volatile("" : "+v"(e[0]), "+v"(e[1]), "+v"(e[2]));
        for (unsigned spins = 0;;) {
            if (!__any(umax(umax(smax(e[0]), smax(e[1])), smax(e[2])) == SENT)) break;
            if (spin_fail(spins, tmo)) break;
            XLD4(e[0], eo0); XLD4(e[1], eo1); XLD4(e[2], eo2);
            asm volatile("s_waitcnt vmcnt(0)" : "+v"(e[0]), "+v"(e[1]), "+v"(e[2]) :: "memory");
        }
        STAMP(10);
        BULK(4);
        LAS float* sc = (LAS float*)(S + L_SC);
        const bool lo = cg < 2;
#pragma unroll
        for (int r = 0; r < 4; ++r) {
            const float e0 = __uint_as_float(e[0][r]), e1 = __uint_as_float(e[1][r]), e2 = __uint_as_float(e[2][r]);
            const float s = (e0 + (lo ? sown[r] : e1)) + (e2 + (lo ? e1 : sown[r]));
            sc[(4 * g4 + r) * 132 + 16 * wave + (lane & 15)] = s;
        }
    }
    asm volatile("s_waitcnt lgkmcnt(0)" : "+v"(vf[0]), "+v"(vf[1]), "+v"(vf[2]), "+v"(vf[3]), "+v"(vf[4]), "+v"(vf[5]), "+v"(vf[6]), "+v"(vf[7]) :: "memory");
    asm volatile("" : "+v"(vf[8]), "+v"(vf[9]), "+v"(vf[10]), "+v"(vf[11]), "+v"(vf[12]), "+v"(vf[13]), "+v"(vf[14]), "+v"(vf[15]));
    BAR();
    BULK(8);
    {
        const LAS f32x4* sc4 = (const LAS f32x4*)(S + L_SC + (lane & 15) * 528 + g4 * 32);
        f32x4 sv[8];
#pragma unroll
        for (int j = 0; j < 4; ++j) { sv[2 * j] = sc4[8 * j]; sv[2 * j + 1] = sc4[8 * j + 1]; }
        float mx = sv[0][0];
#pragma unroll
        for (int i = 0; i < 8; ++i)
#pragma unroll
            for (int k = 0; k < 4; ++k) mx = fmaxf(mx, sv[i][k]);
        mx = max16(mx); mx = max32(mx);
        v4u pf[4];
        float l = 0.f;
#pragma unroll
        for (int i = 0; i < 8; ++i) {
            const unsigned p01 = pack2(__builtin_amdgcn_exp2f(sv[i][0] - mx), __builtin_amdgcn_exp2f(sv[i][1] - mx));
            const unsigned p23 = pack2(__builtin_amdgcn_exp2f(sv[i][2] - mx), __builtin_amdgcn_exp2f(sv[i][3] - mx));
            pf[i >> 1][(i & 1) * 2] = p01; pf[i >> 1][(i & 1) * 2 + 1] = p23;
            l += (bflo(p01) + bfhi(p01)) + (bflo(p23) + bfhi(p23));
        }
        f32x4 o0 = {0.f, 0.f, 0.f, 0.f}, o1 = {0.f, 0.f, 0.f, 0.f};
#pragma unroll
        for (int j = 0; j < 4; ++j) {
            const v4u a0 = {vf[(j * 2) * 2][0], vf[(j * 2) * 2][1], vf[(j * 2) * 2 + 1][0], vf[(j * 2) * 2 + 1][1]};
            const v4u a1 = {vf[(j * 2 + 1) * 2][0], vf[(j * 2 + 1) * 2][1], vf[(j * 2 + 1) * 2 + 1][0], vf[(j * 2 + 1) * 2 + 1][1]};
            o0 = MFMA(a0, pf[j], o0);
            o1 = MFMA(a1, pf[j], o1);
        }
        v4u v; v[0] = canonb(pack2(o0[0], o0[1])); v[1] = canonb(pack2(o0[2], o0[3])); v[2] = canonb(pack2(o1[0], o1[1])); v[3] = canonb(pack2(o1[2], o1[3]));
        __builtin_amdgcn_raw_buffer_store_b128(v, rs_ws, (int)PCTX_OFF + ((((lane & 15) * 4 + cg) * 64 + bb * NKG + kg) * 256 + 32 * wave + 8 * g4) * 2, 0, 16);
        if (wave == 0) {
            l = add16(l); l = add32(l);
            if (lane < 16) __builtin_amdgcn_raw_buffer_store_b64((v2u){canon(__float_as_uint(mx)), canon(__float_as_uint(l))}, rs_ws, (int)ML_OFF + (((cg * 2 + bb) * 16 + lane) * 32 + kg) * 8, 0, 16);
        }
    }
    STAMP(4);
    PIN();
    BULK(5);
    {
        const int mt = wave & 3, kh = wave >> 2;
        const int po = (int)PCTX_OFF + (((th * 4 + cq) * 64 + 16 * mt + (lane & 15)) * 256 + 128 * kh + 8 * g4) * 2;
        const int mo = (int)ML_OFF + (((cq * 2 + (mt >> 1)) * 16 + th) * 32 + 16 * (mt & 1) + 4 * g4) * 8;
        const int mo2 = mo ^ 128;
        const int q = (lane & 15) >> 2, p = lane & 3;
        const int swz = ((q & 2) | ((g4 & 1) << 2)) << 4;
        v4u pa[4], ml[4];
        v2u uf[32], of[24];
#define E3POLL() do { XLD4(pa[0], po); XLD4(pa[1], po + 64); XLD4(pa[2], po + 128); XLD4(pa[3], po + 192); \
                      XLD4(ml[0], mo); XLD4(ml[1], mo + 16); XLD4(ml[2], mo2); XLD4(ml[3], mo2 + 16); } while (0)
        VMW2(0, o_after_u(5));
        STAMP(7);
        BAR();
        STAMP(14);
        {
            const unsigned ua = (unsigned)(size_t)(L + L_WV + kh * 16384 + (8 * g4 + q) * 128 + 8 * p);
#pragma unroll
            for (int nt = 0; nt < 4; ++nt) {
                const unsigned a = ua + ((nt * 32) ^ swz);
#pragma unroll
                for (int ks = 0; ks < 4; ++ks)
#pragma unroll
                    for (int u = 0; u < 2; ++u)
                        asm volatile("ds_read_b64_tr_b16 %0, %1 offset:%2" : "=v"(uf[(nt * 4 + ks) * 2 + u]) : "v"(a), "n"(ks * 4096 + u * 512) : "memory");
            }
        }
        SDLY(E3D);
        E3POLL();
        BULK(6);
        STAMP(15);
        asm volatile("s_waitcnt lgkmcnt(0)" : "+v"(uf[0]), "+v"(uf[1]), "+v"(uf[2]), "+v"(uf[3]), "+v"(uf[4]), "+v"(uf[5]), "+v"(uf[6]), "+v"(uf[7]) :: "memory");
        asm volatile("" : "+v"(uf[8]), "+v"(uf[9]), "+v"(uf[10]), "+v"(uf[11]), "+v"(uf[12]), "+v"(uf[13]), "+v"(uf[14]), "+v"(uf[15]));
        asm volatile("" : "+v"(uf[16]), "+v"(uf[17]), "+v"(uf[18]), "+v"(uf[19]), "+v"(uf[20]), "+v"(uf[21]), "+v"(uf[22]), "+v"(uf[23]));
        asm volatile("" : "+v"(uf[24]), "+v"(uf[25]), "+v"(uf[26]), "+v"(uf[27]), "+v"(uf[28]), "+v"(uf[29]), "+v"(uf[30]), "+v"(uf[31]));
        VMW2(0, cnt_o(6));
        asm volatile("" : "+v"(pa[0]), "+v"(pa[1]), "+v"(pa[2]), "+v"(pa[3]), "+v"(ml[0]), "+v"(ml[1]), "+v"(ml[2]), "+v"(ml[3]));
        for (unsigned spins = 0;;) {
            if (!__any(umax(umax(umax(smax(pa[0]), smax(pa[1])), umax(smax(pa[2]), smax(pa[3]))), umax(umax(smax(ml[0]), smax(ml[1])), umax(smax(ml[2]), smax(ml[3])))) == SENT)) break;
            if (spin_fail(spins, tmo)) break;
            if (has_sent(pa[0])) XLD4R(pa[0], po);
            if (has_sent(pa[1])) XLD4R(pa[1], po + 64);
            if (has_sent(pa[2])) XLD4R(pa[2], po + 128);
            if (has_sent(pa[3])) XLD4R(pa[3], po + 192);
            if (has_sent(ml[0])) XLD4R(ml[0], mo);
            if (has_sent(ml[1])) XLD4R(ml[1], mo + 16);
            if (has_sent(ml[2])) XLD4R(ml[2], mo2);
            if (has_sent(ml[3])) XLD4R(ml[3], mo2 + 16);
            asm volatile("s_waitcnt vmcnt(0)" : "+v"(pa[0]), "+v"(pa[1]), "+v"(pa[2]), "+v"(pa[3]), "+v"(ml[0]), "+v"(ml[1]), "+v"(ml[2]), "+v"(ml[3]) :: "memory");
        }
        STAMP(5);
        BULK(7);
        f32x4 T[4];
#pragma unroll
        for (int nt = 0; nt < 4; ++nt) T[nt] = (f32x4){0.f, 0.f, 0.f, 0.f};
#pragma unroll
        for (int ks = 0; ks < 4; ++ks)
#pragma unroll
            for (int nt = 0; nt < 4; ++nt) {
                const v4u bq = {uf[(nt * 4 + ks) * 2][0], uf[(nt * 4 + ks) * 2][1], uf[(nt * 4 + ks) * 2 + 1][0], uf[(nt * 4 + ks) * 2 + 1][1]};
                T[nt] = MFMA(pa[ks], bq, T[nt]);
            }
        if (wave < 7) {
            if (cnt_o(6) + cnt_o(7)) VM0();
            const unsigned oa = (unsigned)(size_t)(L + L_WO + wave * 5120 + (8 * g4 + q) * 128 + 8 * p);
            const unsigned ob = (unsigned)(size_t)(L + L_WO2 + wave * 4096 + (8 * g4 + q) * 128 + 8 * p);
#pragma unroll
            for (int tt = 0; tt < 4; ++tt) {
                const unsigned a = oa + ((tt * 32) ^ swz), a2 = ob + ((tt * 32) ^ swz);
#pragma unroll
                for (int u = 0; u < 2; ++u) {
                    asm volatile("ds_read_b64_tr_b16 %0, %1 offset:%2" : "=v"(of[tt * 6 + u]) : "v"(a), "n"(u * 512) : "memory");
                    asm volatile("ds_read_b64_tr_b16 %0, %1 offset:%2" : "=v"(of[tt * 6 + 2 + u]) : "v"(a), "n"(1024 + u * 512) : "memory");
                    asm volatile("ds_read_b64_tr_b16 %0, %1 offset:%2" : "=v"(of[tt * 6 + 4 + u]) : "v"(a2), "n"(u * 512) : "memory");
                }
            }
        }
        float mv[8], lv[8];
#pragma unroll
        for (int i = 0; i < 4; ++i) { mv[2 * i] = __uint_as_float(ml[i][0]); lv[2 * i] = __uint_as_float(ml[i][1]); mv[2 * i + 1] = __uint_as_float(ml[i][2]); lv[2 * i + 1] = __uint_as_float(ml[i][3]); }
        float gm = fmaxf(fmaxf(fmaxf(mv[0], mv[1]), fmaxf(mv[2], mv[3])), fmaxf(fmaxf(mv[4], mv[5]), fmaxf(mv[6], mv[7])));
        gm = max16(gm); gm = max32(gm);
#pragma unroll
        for (int i = 0; i < 8; ++i) mv[i] = __builtin_amdgcn_exp2f(mv[i] - gm);
        float ls = ((mv[0] * lv[0] + mv[1] * lv[1]) + (mv[2] * lv[2] + mv[3] * lv[3])) + ((mv[4] * lv[4] + mv[5] * lv[5]) + (mv[6] * lv[6] + mv[7] * lv[7]));
        ls = add16(ls); ls = add32(ls);
        const float inv = 1.0f / ls;
        float sv[4];
        {
            float wr[4];
#pragma unroll
            for (int r = 0; r < 4; ++r) wr[r] = mv[r] * inv;
#pragma unroll
            for (int nt = 0; nt < 4; ++nt) sv[nt] = (T[nt][0] * wr[0] + T[nt][1] * wr[1]) + (T[nt][2] * wr[2] + T[nt][3] * wr[3]);
        }
        const auto r0 = __builtin_amdgcn_permlane32_swap(__float_as_uint(sv[0]), __float_as_uint(sv[2]), false, false);
        const auto r1 = __builtin_amdgcn_permlane32_swap(__float_as_uint(sv[1]), __float_as_uint(sv[3]), false, false);
        const float x0 = __uint_as_float(r0[0]) + __uint_as_float(r0[1]), x1 = __uint_as_float(r1[0]) + __uint_as_float(r1[1]);
        const auto r2 = __builtin_amdgcn_permlane16_swap(__float_as_uint(x0), __float_as_uint(x1), false, false);
        const float cv = __uint_as_float(r2[0]) + __uint_as_float(r2[1]);
        {
            const unsigned hi = f2bf(cv), lo = f2bf(cv - __uint_as_float(hi << 16));
            LAS unsigned short* cb = (LAS unsigned short*)(S + L_CB + ((mt << 1) | (kh << 3)) * 144) + ((lane - 8 * ROT * cq) & 63);
            cb[0] = (unsigned short)hi; cb[72] = (unsigned short)lo;
        }
        v4u hb = {0u, 0u, 0u, 0u};
        if (g4 == 0 && (lane & 10) == 0) hb = *(const LAS v4u*)(S + L_HM + ((lane >> 2) * 2 + (lane & 1)) * 16);
        asm volatile("s_waitcnt lgkmcnt(0)" : "+v"(of[0]), "+v"(of[1]), "+v"(of[2]), "+v"(of[3]), "+v"(of[4]), "+v"(of[5]), "+v"(of[6]), "+v"(of[7]) :: "memory");
        asm volatile("" : "+v"(of[8]), "+v"(of[9]), "+v"(of[10]), "+v"(of[11]), "+v"(of[12]), "+v"(of[13]), "+v"(of[14]), "+v"(of[15]));
        asm volatile("" : "+v"(of[16]), "+v"(of[17]), "+v"(of[18]), "+v"(of[19]), "+v"(of[20]), "+v"(of[21]), "+v"(of[22]), "+v"(of[23]));
        STAMP(6);
        BAR();
        STAMP(11);
        if (wave < 7) {
            const v4u b0 = *(const LAS v4u*)(S + L_CB + (lane & 15) * 144 + g4 * 16);
            const v4u b1v = *(const LAS v4u*)(S + L_CB + (lane & 15) * 144 + 64 + g4 * 16);
            f32x4 o[4];
#pragma unroll
            for (int tt = 0; tt < 4; ++tt) {
                const v4u a0 = {of[tt * 6][0], of[tt * 6][1], of[tt * 6 + 1][0], of[tt * 6 + 1][1]};
                o[tt] = (f32x4){0.f, 0.f, 0.f, 0.f};
                o[tt] = MFMA(a0, hb, o[tt]);
            }
#pragma unroll
            for (int tt = 0; tt < 4; ++tt) {
                const v4u a1 = {of[tt * 6 + 2][0], of[tt * 6 + 2][1], of[tt * 6 + 3][0], of[tt * 6 + 3][1]};
                o[tt] = MFMA(a1, b0, o[tt]);
            }
#pragma unroll
            for (int tt = 0; tt < 4; ++tt) {
                const v4u a2 = {of[tt * 6 + 4][0], of[tt * 6 + 4][1], of[tt * 6 + 5][0], of[tt * 6 + 5][1]};
                o[tt] = MFMA(a2, b1v, o[tt]);
            }
            const bool l0 = lane & 1, l1 = lane & 2;
            v4u v;
#pragma unroll
            for (int r = 0; r < 4; ++r) {
                const float y0 = (l0 ? o[1][r] : o[0][r]) + dppf<0xB1>(l0 ? o[0][r] : o[1][r]);
                const float y1 = (l0 ? o[3][r] : o[2][r]) + dppf<0xB1>(l0 ? o[2][r] : o[3][r]);
                float z = (l1 ? y1 : y0) + dppf<0x4E>(l1 ? y0 : y1);
                z += dppf<0x128>(z);
                v[r] = canon(__float_as_uint(z));
            }
            if ((lane & 8) == 0)
                __builtin_amdgcn_raw_buffer_store_b128(v, rs_ws, (int)G5_OFF + (((8 * wave + 2 * (lane & 3) + (g4 >> 1)) * 256 + cu) * 16 + ((lane >> 2) & 1) * 8 + (g4 & 1) * 4) * 4, 0, 16);
        }
    }
    STAMP(8);
    if (cu < NC5) {
        v4u v0 = {SENT, SENT, SENT, SENT}, v1 = {SENT, SENT, SENT, SENT};
        for (unsigned spins = 0;;) {
            if (has_sent(v0)) v0 = __builtin_amdgcn_raw_buffer_load_b128(rs_ws, (int)(G5_OFF + (size_t)cu * 16384 + tid * 16), 0, 16);
            if (has_sent(v1)) v1 = __builtin_amdgcn_raw_buffer_load_b128(rs_ws, (int)(G5_OFF + (size_t)cu * 16384 + 8192 + tid * 16), 0, 16);
            if (!__any(umax(smax(v0), smax(v1)) == SENT)) break;
            if (spin_fail(spins, tmo)) break;
        }
        f32x4 s; s[0] = __uint_as_float(v0[0]) + __uint_as_float(v1[0]); s[1] = __uint_as_float(v0[1]) + __uint_as_float(v1[1]);
        s[2] = __uint_as_float(v0[2]) + __uint_as_float(v1[2]); s[3] = __uint_as_float(v0[3]) + __uint_as_float(v1[3]);
#pragma unroll
        for (int k = 0; k < 4; ++k) { float v = s[k]; v += dppf<0x124>(v); s[k] = add_hi3(v); }
        LAS float* red = (LAS float*)(S + L_CC);
        if (lane < 4) *(LAS f32x4*)(red + wave * 16 + lane * 4) = s;
        LGKM0();
        BAR();
        if (tid < 16) {
            float t = 0.f;
#pragma unroll
            for (int w = 0; w < 8; ++w) t += red[w * 16 + tid];
            const int b = tid >> 3, m = cu * 8 + (tid & 7);
            t += xb;
            out[b * M + m] = (unsigned short)f2bf(t);
        }
        STAMP(9);
    }
}
}

extern "C" void kernel_launch(void* const* d_in, const int* in_sizes, int n_in,
                              void* d_out, int out_size, void* d_ws, size_t ws_size,
                              hipStream_t stream) {
    const unsigned short* x = (const unsigned short*)d_in[0];
    const unsigned short* ckv = (const unsigned short*)d_in[1];
    const unsigned short* wq = (const unsigned short*)d_in[2];
    const unsigned short* wuk = (const unsigned short*)d_in[3];
    const unsigned short* wuv = (const unsigned short*)d_in[4];
    const unsigned short* wo = (const unsigned short*)d_in[5];
    const unsigned short* w1 = (const unsigned short*)d_in[7];
    const unsigned short* b1 = (const unsigned short*)d_in[8];
    const unsigned short* w2 = (const unsigned short*)d_in[9];
    const unsigned short* b2 = (const unsigned short*)d_in[10];
    unsigned char* ws = (unsigned char*)d_ws;
    k1<<<368, 320, 0, stream>>>(x, wq, w1, ws);
    k2<<<NCU, 512, 0, stream>>>(x, ckv, wuk, wuv, wo, b1, w2, b2, ws, (unsigned short*)d_out);
}
```

```cpp
#define PROBE 0
#include <hip/hip_runtime.h>
#include <stdint.h>

#define GAS __attribute__((address_space(1)))
#define LAS __attribute__((address_space(3)))
typedef unsigned v4u __attribute__((ext_vector_type(4)));
typedef unsigned v2u __attribute__((ext_vector_type(2)));
typedef float f32x4 __attribute__((ext_vector_type(4)));
typedef float f32x2 __attribute__((ext_vector_type(2)));
typedef short bf16x8 __attribute__((ext_vector_type(8)));
typedef short s16x4 __attribute__((ext_vector_type(4)));
typedef __bf16 bf16x2_t __attribute__((ext_vector_type(2)));
typedef GAS unsigned gu32;
typedef GAS unsigned long long gu64;
#define RLX_AGENT __ATOMIC_RELAXED, __HIP_MEMORY_SCOPE_AGENT

#define STAMPS 0
#define WSEND 0
#define FBAR 1
#define CKV 0
#define FST 0
#define S2M 0
#define PFD 0
#define K1PF 0
#define WIN 16
#define E2D2 0
#define E2D 8
#define FILLM 16
#define QPM 0
#define E3D 0
#define ESD 2
#define PU 4444
#define PO 555555555
#define ROT 2
#define MRT 1
#define E5D 6
#define E3O 0
#define E4D 0
#define SDLY(n) do { _Pragma("unroll") for (int i_ = 0; i_ < (n) / 4; ++i_) __builtin_amdgcn_s_sleep(16); if ((n) % 4) __builtin_amdgcn_s_sleep(4 * ((n) % 4)); } while (0)
#define PIN() __builtin_amdgcn_sched_barrier(0)

namespace {
constexpr int H = 16, SK = 4096, D = 256, M = 448, DC = 1024, F = 1792;
constexpr int NCU = 256;
constexpr int NKG = 32, KPG = 128;
constexpr int NC5 = 56;

constexpr size_t TMO_OFF = 0;
constexpr size_t QABS_OFF = 4096;
constexpr size_t ML_OFF = QABS_OFF + 131072;
constexpr size_t SP_OFF = ML_OFF + 32768;
constexpr size_t PCTX_OFF = SP_OFF + (size_t)256 * 8192;
constexpr size_t G5_OFF = PCTX_OFF + (size_t)2 * NKG * 16 * 1024 * 2;
constexpr size_t G5_BYTES = (size_t)56 * 256 * 16 * 4;
constexpr size_t FILL_BYTES = G5_OFF + G5_BYTES;
constexpr size_t QP_OFF = FILL_BYTES;
constexpr size_t HP_OFF = QP_OFF + 4 * 2 * 4096 * 4;
constexpr size_t STAMP_OFF = HP_OFF + 65536;
constexpr size_t K1ST_OFF = STAMP_OFF + 256 * 16 * 8;
constexpr size_t WS_NEED = K1ST_OFF + 512 * 2 * 8;
static_assert(FILL_BYTES % 16 == 0, "fill");

constexpr int RS2 = 1040;
constexpr int L_CKV = 0;
constexpr int L_WK = 64 * RS2;
constexpr int L_WV = L_WK;
constexpr int L_WO = L_WK + 32 * RS2;
constexpr int L_WO2 = L_CKV;
constexpr int L_PG = L_CKV + 7 * 4096;
constexpr int LDS_DMA_BYTES = L_WO + 7 * 5120;
static_assert(L_PG + 8 * 4096 <= L_WK, "lds");
constexpr int L_SC = 0;
constexpr int L_QF = 8448;
constexpr int L_CB = L_QF;
constexpr int L_QA = L_QF + 16384;
constexpr int L_CC = L_QA + 2048;
constexpr int L_HM = L_CC + 512;
constexpr int LDS_SC_BYTES = L_HM + 64;
static_assert(LDS_DMA_BYTES + LDS_SC_BYTES <= 160 * 1024, "lds");

constexpr unsigned kSpinCap = 1u << 18;
constexpr unsigned SENT = 0xFFFFFFFFu;

__device__ __forceinline__ float bf2f(unsigned short u) { return __uint_as_float(((unsigned)u) << 16); }
__device__ __forceinline__ float bflo(unsigned u) { return __uint_as_float(u << 16); }
__device__ __forceinline__ float bfhi(unsigned u) { return __uint_as_float(u & 0xffff0000u); }
__device__ __forceinline__ unsigned f2bf(float f) { unsigned u = __float_as_uint(f); return (u + 0x7fffu + ((u >> 16) & 1u)) >> 16; }
__device__ __forceinline__ unsigned pack2(float a, float b) { const f32x2 v = {a, b}; return __builtin_bit_cast(unsigned, __builtin_convertvector(v, bf16x2_t)); }
template <int C> __device__ __forceinline__ float dppf(float v) { return __int_as_float(__builtin_amdgcn_update_dpp(0, __float_as_int(v), C, 0xF, 0xF, true)); }
__device__ __forceinline__ float add16(float v) { auto r = __builtin_amdgcn_permlane16_swap(__float_as_uint(v), __float_as_uint(v), false, false); return __uint_as_float(r[0]) + __uint_as_float(r[1]); }
__device__ __forceinline__ float add32(float v) { auto r = __builtin_amdgcn_permlane32_swap(__float_as_uint(v), __float_as_uint(v), false, false); return __uint_as_float(r[0]) + __uint_as_float(r[1]); }
__device__ __forceinline__ float max16(float v) { auto r = __builtin_amdgcn_permlane16_swap(__float_as_uint(v), __float_as_uint(v), false, false); return fmaxf(__uint_as_float(r[0]), __uint_as_float(r[1])); }
__device__ __forceinline__ float max32(float v) { auto r = __builtin_amdgcn_permlane32_swap(__float_as_uint(v), __float_as_uint(v), false, false); return fmaxf(__uint_as_float(r[0]), __uint_as_float(r[1])); }
__device__ __forceinline__ float add_hi3(float v) { v += dppf<0x128>(v); v = add16(v); return add32(v); }
__device__ __forceinline__ float max_hi3(float v) { v = fmaxf(v, dppf<0x128>(v)); v = max16(v); return max32(v); }
__device__ __forceinline__ float add_lo5(float v) { v += dppf<0xB1>(v); v += dppf<0x4E>(v); v += dppf<0x141>(v); v += dppf<0x140>(v); return add16(v); }
__device__ __forceinline__ float max_lo5(float v) { v = fmaxf(v, dppf<0xB1>(v)); v = fmaxf(v, dppf<0x4E>(v)); v = fmaxf(v, dppf<0x141>(v)); v = fmaxf(v, dppf<0x140>(v)); return max16(v); }
__device__ __forceinline__ float wsum(float v) { return add32(add_lo5(v)); }
__device__ __forceinline__ unsigned long long rt() { return __builtin_amdgcn_s_memrealtime(); }

#define BAR() __builtin_amdgcn_s_barrier()
#define LGKM0() asm volatile("s_waitcnt lgkmcnt(0)" ::: "memory")
#define VM0() asm volatile("s_waitcnt vmcnt(0)" ::: "memory")
#define MFMA(a, b, c) __builtin_amdgcn_mfma_f32_16x16x32_bf16(__builtin_bit_cast(bf16x8, a), __builtin_bit_cast(bf16x8, b), c, 0, 0, 0)

__global__ void __launch_bounds__(320) k1(const unsigned short* __restrict__ x, const unsigned short* __restrict__ wq,
                                          const unsigned short* __restrict__ w1, unsigned char* __restrict__ ws) {
    __shared__ float xn[2][M];
    __shared__ float red[4][2][64];
    const int tid = threadIdx.x, wave = __builtin_amdgcn_readfirstlane(tid >> 6), lane = tid & 63;
    const int blk = blockIdx.x;
    if (tid == 0) ((gu64*)(ws + K1ST_OFF))[blk * 2] = rt();
    const bool isq = blk < 256;
    if (wave == 4) {
        const v4u sn = {SENT, SENT, SENT, SENT};
        const unsigned tot16 = (unsigned)(FILL_BYTES / 16);
        const __amdgpu_buffer_rsrc_t rsf = __builtin_amdgcn_make_buffer_rsrc(ws, 0, (int)WS_NEED, 0x00020000);
        SDLY(PFD);
        for (unsigned i = blk * 64 + lane; i < tot16; i += gridDim.x * 64) __builtin_amdgcn_raw_buffer_store_b128(sn, rsf, (int)(i * 16), 0, FILLM);
        return;
    }
    const int idx = isq ? blk : blk - 256;
    const int mq = idx & 3, cg = idx >> 2;
    const int ncols = isq ? 4096 : F;
    const unsigned short* W = (isq ? wq : w1) + (size_t)(mq * 112) * ncols + cg * 64;
    const int r = tid >> 3, cch = tid & 7;
    unsigned xr[7];
#pragma unroll
    for (int i = 0; i < 7; ++i) asm volatile("global_load_ushort %0, %1, off" : "=v"(xr[i]) : "v"(x + (wave & 1) * M + lane + 64 * i) : "memory");
    v4u wv[4];
#pragma unroll
    for (int i = 0; i < 4; ++i) {
        const int row = (i < 3 || r < 16) ? r + 32 * i : 111;
        asm volatile("global_load_dwordx4 %0, %1, off nt" : "=v"(wv[i]) : "v"(W + (size_t)row * ncols + cch * 8) : "memory");
    }
    PIN();
    asm volatile("s_waitcnt vmcnt(4)" : "+v"(xr[0]), "+v"(xr[1]), "+v"(xr[2]), "+v"(xr[3]), "+v"(xr[4]), "+v"(xr[5]), "+v"(xr[6]) :: "memory");
    PIN();
    if (wave < 2) {
        float v[7]; float s = 0.f;
#pragma unroll
        for (int i = 0; i < 7; ++i) { v[i] = bf2f((unsigned short)xr[i]); s += v[i]; }
        s = wsum(s); const float mean = s * (1.0f / M);
        float ss = 0.f;
#pragma unroll
        for (int i = 0; i < 7; ++i) { const float d = v[i] - mean; ss += d * d; }
        ss = wsum(ss);
        const float rs = 1.0f / sqrtf(ss * (1.0f / M) + 1e-5f);
#pragma unroll
        for (int i = 0; i < 7; ++i) xn[wave][lane + 64 * i] = (v[i] - mean) * rs;
    }
    LGKM0(); BAR();
    asm volatile("s_waitcnt vmcnt(0)" : "+v"(wv[0]), "+v"(wv[1]), "+v"(wv[2]), "+v"(wv[3]) :: "memory");
    float a[2][8];
#pragma unroll
    for (int j = 0; j < 8; ++j) { a[0][j] = 0.f; a[1][j] = 0.f; }
#pragma unroll
    for (int i = 0; i < 4; ++i) {
        int row = mq * 112 + r + 32 * i; if (row >= M) row = M - 1;
        const bool ok = (i < 3 || r < 16);
        const float x0 = ok ? xn[0][row] : 0.f, x1 = ok ? xn[1][row] : 0.f;
#pragma unroll
        for (int j = 0; j < 4; ++j) {
            const float lo = bflo(wv[i][j]), hi = bfhi(wv[i][j]);
            a[0][2 * j] += x0 * lo; a[0][2 * j + 1] += x0 * hi;
            a[1][2 * j] += x1 * lo; a[1][2 * j + 1] += x1 * hi;
        }
    }
#pragma unroll
    for (int b = 0; b < 2; ++b)
#pragma unroll
        for (int j = 0; j < 8; ++j) a[b][j] = add_hi3(a[b][j]);
    if (lane < 8) {
#pragma unroll
        for (int b = 0; b < 2; ++b)
#pragma unroll
            for (int j = 0; j < 8; ++j) red[wave][b][lane * 8 + j] = a[b][j];
    }
    LGKM0(); BAR();
    if (tid < 128) {
        const int b = tid >> 6, c = tid & 63;
        const float s = red[0][b][c] + red[1][b][c] + red[2][b][c] + red[3][b][c];
        float* dst = (float*)(ws + (isq ? QP_OFF : HP_OFF));
        dst[((size_t)mq * 2 + b) * ncols + cg * 64 + c] = s;
    }
    if (tid == 0) ((gu64*)(ws + K1ST_OFF))[blk * 2 + 1] = rt();
}

__device__ __forceinline__ bool spin_fail(unsigned& spins, gu32* tmo) {
    __builtin_amdgcn_s_sleep(1);
    if (++spins > kSpinCap) { __hip_atomic_store(tmo, 1u, RLX_AGENT); return true; }
    return (spins & 1023u) == 0u && __builtin_amdgcn_readfirstlane(__hip_atomic_load(tmo, RLX_AGENT)) == 1u;
}
__device__ __forceinline__ unsigned umax(unsigned a, unsigned b) { return a > b ? a : b; }
__device__ __forceinline__ unsigned smax(v4u v) { return umax(umax(umax(v[0], v[1]), v[2]), v[3]); }
__device__ __forceinline__ bool has_sent(v4u v) { return smax(v) == SENT; }
__device__ __forceinline__ unsigned canon(unsigned u) { return u == SENT ? 0x7FC00000u : u; }
__device__ __forceinline__ unsigned canonb(unsigned u) { return u == SENT ? 0xFFC0FFC0u : u; }

#define GLDS_NT(src, dst) __builtin_amdgcn_global_load_lds((const unsigned*)(src), (LAS unsigned*)(dst), 16, 0, 2)
#define OAUX 0
#define GLDS_O(src, dst) __builtin_amdgcn_global_load_lds((const unsigned*)(src), (LAS unsigned*)(dst), 16, 0, OAUX)
#define STAMP(i) do { pt[i] = (unsigned)rt(); } while (0)
#define STAMPW(i, w) do { pt[i] = (unsigned)rt(); } while (0)

constexpr int dig(int v, int i) { return i == 0 ? v % 10 : dig(v / 10, i - 1); }
constexpr int rank_of(int p) { return p == 1 ? 0 : (p == 9 ? 1 : (p == 8 ? 5 : (p >= 5 ? p + 1 : p))); }
constexpr int cnt_u(int p) { int n = 0; for (int i = 0; i < 4; ++i) n += dig(PU, i) == p; return n; }
constexpr int cnt_o(int p) { int n = 0; for (int i = 0; i < 9; ++i) n += dig(PO, i) == p; return n; }
constexpr int last_u() { int l = 0; for (int i = 0; i < 4; ++i) if (rank_of(dig(PU, i)) > l) l = rank_of(dig(PU, i)); return l; }
constexpr int o_after_u(int p) {
    int n = 0; for (int i = 0; i < 9; ++i) n += (rank_of(dig(PO, i)) >= last_u() && rank_of(dig(PO, i)) <= rank_of(p)); return n; }
#define OE 1
constexpr bool OEARLY = OE && cnt_o(6) + cnt_o(7) == 0;
static_assert(last_u() <= rank_of(5), "w_uv");
static_assert(rank_of(dig(PO, 5)) >= 5 && rank_of(dig(PO, 6)) >= 5 && rank_of(dig(PO, 7)) >= 5 && rank_of(dig(PO, 8)) >= 5, "w_o late part");
#define BULK(p) do { \
        _Pragma("unroll") for (int i = 0; i < 4; ++i) if (dig(PU, i) == (p)) \
            GLDS_NT(wuv + ((size_t)(256 * cq + 32 * wave + 8 * i + (lane >> 3)) * H + th) * D + 64 * dq + (((lane & 7) ^ ((lane >> 3) & 2) ^ ((i & 1) << 2)) * 8), L + L_WV + wave * 4096 + i * 1024); \
        if (cnt_o(p) && wave < 7) { \
            _Pragma("unroll") for (int i = 0; i < 9; ++i) if (dig(PO, i) == (p)) { \
                const int row = lane >> 3; \
                const unsigned short* sp = i > 0 ? wo + ((size_t)th * D + 64 * dq + 8 * ((i - 1 + ROT * cq) & 7) + row) * M : w2 + ((size_t)cu * 7 + (row < 7 ? row : 6)) * M; \
                GLDS_O(sp + 64 * wave + (((lane & 7) ^ (row & 2) ^ (i > 0 ? ((i - 1) & 1) << 2 : 0)) * 8), L + (i < 5 ? L_WO + wave * 5120 + i * 1024 : L_WO2 + wave * 4096 + (i - 5) * 1024)); \
            } \
        } \
        PIN(); \
    } while (0)
#define VMW2(a, b) do { if ((b) > 0 && wave == 7) VMW(a); else VMW((a) + (b)); } while (0)
#define VMW(n) asm volatile("s_waitcnt vmcnt(%0)" :: "n"(n) : "memory")
#define XB 0
#define XBITS "sc1"
#define XLD4(dst, off) asm volatile("buffer_load_dwordx4 %0, %1, %2, 0 offen " XBITS : "=v"(dst) : "v"(off), "s"(rs_ws) : "memory")
#define XLD4R(dst, off) asm volatile("buffer_load_dwordx4 %0, %1, %2, 0 offen " XBITS : "+v"(dst) : "v"(off), "s"(rs_ws) : "memory")
#define XLD2(dst, off) asm volatile("buffer_load_dwordx2 %0, %1, %2, 0 offen sc1" : "=v"(dst) : "v"(off), "s"(rs_ws) : "memory")
__global__ void __launch_bounds__(512, 1) k2(const unsigned short* __restrict__ x, const unsigned short* __restrict__ ckv,
                                             const unsigned short* __restrict__ wuk, const unsigned short* __restrict__ wuv,
                                             const unsigned short* __restrict__ wo, const unsigned short* __restrict__ b1,
                                             const unsigned short* __restrict__ w2, const unsigned short* __restrict__ b2,
                                             unsigned char* __restrict__ ws, unsigned short* __restrict__ out) {
    __shared__ __attribute__((aligned(16))) unsigned char lds_dma[LDS_DMA_BYTES];
    __shared__ __attribute__((aligned(16))) unsigned char lds_sc[LDS_SC_BYTES];
    LAS unsigned char* L = (LAS unsigned char*)lds_dma;
    LAS unsigned char* S = (LAS unsigned char*)lds_sc;
    const int tid = threadIdx.x, wave = __builtin_amdgcn_readfirstlane(tid >> 6), lane = tid & 63;
    const int cu = blockIdx.x;
    const int hh = cu >> 4, cs = cu & 15;
    const int bb = cu >> 7, kg = (cu >> 2) & 31, cg = cu & 3;
    const int th = (cu & 7) * 2 + (cu >> 7), dq = (cu >> 5) & 3, cq = (cu >> 3) & 3;
    const int g4 = lane >> 4;
    gu32* tmo = (gu32*)(ws + TMO_OFF);
    const __amdgpu_buffer_rsrc_t rs_ws = __builtin_amdgcn_make_buffer_rsrc(ws, 0, (int)WS_NEED, 0x00020000);
    unsigned pt[16];
    for (int i = 0; i < 16; ++i) pt[i] = 0;
    STAMP(0);

    float qp[4];
    {
        const float* p = (const float*)(ws + QP_OFF) + (size_t)(tid >> 8) * 4096 + hh * D + (tid & 255);
#pragma unroll
        for (int i = 0; i < 4; ++i) asm volatile("global_load_dword %0, %1, off" : "=v"(qp[i]) : "v"(p + (size_t)i * 8192) : "memory");
    }
    unsigned xr0, xr1, hb1; float hp4[4];
    {
        const int b = (tid >> 3) & 1, m = (cu < NC5 ? cu : 0) * 8 + (tid & 7);
        asm volatile("global_load_ushort %0, %1, off" : "=v"(xr0) : "v"(x + b * M + m) : "memory");
        asm volatile("global_load_ushort %0, %1, off" : "=v"(xr1) : "v"(b2 + m) : "memory");
    }
    if (wave == 7) {
        const int l2 = lane < 14 ? lane : 13;
        const int b = l2 / 7, r = l2 % 7, f = cu * 7 + r;
        const float* hp = (const float*)(ws + HP_OFF);
        asm volatile("global_load_ushort %0, %1, off" : "=v"(hb1) : "v"(b1 + f) : "memory");
#pragma unroll
        for (int p = 0; p < 4; ++p) asm volatile("global_load_dword %0, %1, off" : "=v"(hp4[p]) : "v"(hp + ((size_t)p * 2 + b) * F + f) : "memory");
    }
    if (wave < 4) {
#pragma unroll
        for (int i = 0; i < 8; ++i) {
            const int r = 2 * i + (lane >> 5);
            const int ck = (lane & 31) ^ ((r & 7) << 2);
            GLDS_NT(wuk + ((size_t)(64 * cs + 16 * wave + r) * H + hh) * D + 8 * ck, L + L_WK + (8 * wave + i) * RS2);
        }
    }
    PIN();
    BAR();
    PIN();
    {
        const unsigned short* src = ckv + ((size_t)bb * SK + (size_t)kg * KPG + 16 * wave) * DC + 256 * cg;
#pragma unroll
        for (int i = 0; i < 8; ++i) {
            const int r = 2 * i + (lane >> 5);
            const int ck = (lane & 31) ^ ((r & 7) << 2);
            GLDS_NT(src + (size_t)r * DC + 8 * ck, L + L_CKV + (8 * wave + i) * RS2);
        }
    }
    PIN();
    if (wave < 4) asm volatile("s_waitcnt vmcnt(16)" ::: "memory");
    else asm volatile("s_waitcnt vmcnt(8)" ::: "memory");
    asm volatile("" : "+v"(qp[0]), "+v"(qp[1]), "+v"(qp[2]), "+v"(qp[3]), "+v"(xr0), "+v"(xr1));
    const float xb = bf2f((unsigned short)xr0) + bf2f((unsigned short)xr1);
    if (wave == 7) {
        asm volatile("" : "+v"(hb1), "+v"(hp4[0]), "+v"(hp4[1]), "+v"(hp4[2]), "+v"(hp4[3]));
        const float hv = bf2f((unsigned short)hb1) + hp4[0] + hp4[1] + hp4[2] + hp4[3];
        const float t = tanhf(0.7978845608028654f * (hv + 0.044715f * hv * hv * hv));
        const float gl = lane < 14 ? 0.5f * hv * (1.0f + t) : 0.f;
        const unsigned hi = f2bf(gl), lo = f2bf(gl - __uint_as_float(hi << 16));
        const int ix = lane < 14 ? (lane / 7) * 16 + lane % 7 : (lane - 14) * 16 + 7;
        if (lane < 16) { ((LAS unsigned short*)(S + L_HM))[ix] = (unsigned short)hi; ((LAS unsigned short*)(S + L_HM))[ix + 8] = (unsigned short)lo; }
    }
    {
        float s = (qp[0] + qp[1]) + (qp[2] + qp[3]);
        s *= 0.0625f * 1.4426950408889634f;
        const unsigned hi = f2bf(s);
        const unsigned lo = f2bf(s - __uint_as_float(hi << 16));
        const int b = tid >> 8, d = tid & 255;
        ((LAS unsigned short*)(S + L_QA))[(2 * b) * 256 + d] = (unsigned short)hi;
        ((LAS unsigned short*)(S + L_QA))[(2 * b + 1) * 256 + d] = (unsigned short)lo;
    }
    LGKM0();
    BAR();
    STAMPW(1, 5);
    if (wave < 4) {
        v4u av[8], wk[8];
#pragma unroll
        for (int ks = 0; ks < 8; ++ks) av[ks] = *(const LAS v4u*)(S + L_QA + (lane & 3) * 512 + (32 * ks + 8 * g4) * 2);
        asm volatile("s_waitcnt vmcnt(8)" ::: "memory");
        {
            const unsigned wa = (unsigned)(size_t)(L + L_WK + (8 * wave + ((lane & 15) >> 1)) * RS2 + (lane & 1) * 512 + g4 * 16);
#pragma unroll
            for (int ks = 0; ks < 8; ++ks) asm volatile("ds_read_b128 %0, %1" : "=v"(wk[ks]) : "v"(wa + ((ks ^ (lane & 7)) << 6)) : "memory");
            asm volatile("s_waitcnt lgkmcnt(0)" : "+v"(wk[0]), "+v"(wk[1]), "+v"(wk[2]), "+v"(wk[3]), "+v"(wk[4]), "+v"(wk[5]), "+v"(wk[6]), "+v"(wk[7]) :: "memory");
        }
        f32x4 acc0 = {0.f, 0.f, 0.f, 0.f}, acc1 = {0.f, 0.f, 0.f, 0.f};
#pragma unroll
        for (int ks = 0; ks < 8; ks += 2) { acc0 = MFMA(wk[ks], av[ks], acc0); acc1 = MFMA(wk[ks + 1], av[ks + 1], acc1); }
        acc0 += acc1;
        unsigned hb[4];
#pragma unroll
        for (int r = 0; r < 4; ++r) {
            const float v = acc0[r] + dppf<0xB1>(acc0[r]);
            const unsigned h0 = f2bf(v);
            hb[r] = (lane & 1) ? f2bf(v - __uint_as_float(h0 << 16)) : h0;
        }
        if ((lane & 15) < 4) {
            const v2u v = {canonb(hb[0] | (hb[1] << 16)), canonb(hb[2] | (hb[3] << 16))};
            __builtin_amdgcn_raw_buffer_store_b64(v, rs_ws, (int)QABS_OFF + ((((((lane >> 1) & 1) * 4 + (cs >> 2)) * 2 + (lane & 1)) * 8 + ((2 * cs + (wave >> 1)) & 7)) * 64 + hh + 16 * (2 * (wave & 1) + (g4 >> 1))) * 16 + (g4 & 1) * 8, 0, 16);
        }
        STAMP(2);
    }
    PIN();
    BAR();
    SDLY(E2D);
    PIN();
    const int qoff = (int)QABS_OFF + ((bb * 4 + cg) * 2 * 8 + wave) * 1024 + lane * 16;
    v4u qa[2];
    XLD4(qa[0], qoff);
    XLD4(qa[1], qoff + 8192);
    asm volatile("s_waitcnt vmcnt(2)" ::: "memory");
    STAMPW(13, 7);
    v4u kf[8];
    {
        const unsigned ka = (unsigned)(size_t)(L + L_CKV + (8 * wave + ((lane & 15) >> 1)) * RS2 + (lane & 1) * 512 + g4 * 16);
#pragma unroll
        for (int ks = 0; ks < 8; ++ks) asm volatile("ds_read_b128 %0, %1" : "=v"(kf[ks]) : "v"(ka + ((ks ^ (lane & 7)) << 6)) : "memory");
    }
    BULK(1);
    VMW2(cnt_u(1), cnt_o(1));
    asm volatile("" : "+v"(qa[0]), "+v"(qa[1]));
    for (unsigned spins = 0;;) {
        if (!__any(umax(smax(qa[0]), smax(qa[1])) == SENT)) break;
        if (spin_fail(spins, tmo)) break;
        XLD4(qa[0], qoff);
        XLD4(qa[1], qoff + 8192);
        asm volatile("s_waitcnt vmcnt(0)" : "+v"(qa[0]), "+v"(qa[1]) :: "memory");
    }
    *(LAS v4u*)(S + L_QF + wave * 1024 + lane * 16) = qa[0];
    *(LAS v4u*)(S + L_QF + 8192 + wave * 1024 + lane * 16) = qa[1];
    asm volatile("s_waitcnt lgkmcnt(0)" : "+v"(kf[0]), "+v"(kf[1]), "+v"(kf[2]), "+v"(kf[3]), "+v"(kf[4]), "+v"(kf[5]), "+v"(kf[6]), "+v"(kf[7]) :: "memory");
    BAR();
    STAMPW(12, 7);
    BULK(9);
    f32x4 sown;
    {
        f32x4 acc0 = {0.f, 0.f, 0.f, 0.f}, acc1 = {0.f, 0.f, 0.f, 0.f};
#pragma unroll
        for (int ks = 0; ks < 8; ++ks) {
            const v4u ah = *(const LAS v4u*)(S + L_QF + ks * 1024 + lane * 16);
            const v4u al = *(const LAS v4u*)(S + L_QF + 8192 + ks * 1024 + lane * 16);
            acc0 = MFMA(ah, kf[ks], acc0);
            acc1 = MFMA(al, kf[ks], acc1);
        }
        sown = acc0 + acc1;
        v4u v; v[0] = canon(__float_as_uint(sown[0])); v[1] = canon(__float_as_uint(sown[1])); v[2] = canon(__float_as_uint(sown[2])); v[3] = canon(__float_as_uint(sown[3]));
        __builtin_amdgcn_raw_buffer_store_b128(v, rs_ws, (int)SP_OFF + cu * 8192 + wave * 1024 + lane * 16, 0, 16);
    }
    STAMP(3);
    PIN();
    BULK(2);
    v2u vf[16];
    {
        const int q = (lane & 15) >> 2, p = lane & 3;
#pragma unroll
        for (int u = 0; u < 2; ++u) {
            const unsigned va = (unsigned)(size_t)(L + L_CKV + (4 * g4 + 2 * u + (q >> 1)) * RS2 + (q & 1) * 512 + ((wave ^ (4 * u + q)) << 6) + 16 * p);
#pragma unroll
            for (int j = 0; j < 4; ++j)
#pragma unroll
                for (int t = 0; t < 2; ++t)
                    asm volatile("ds_read_b64_tr_b16 %0, %1 offset:%2" : "=v"(vf[(j * 2 + t) * 2 + u]) : "v"(va), "n"(j * 16 * RS2 + 8 * t) : "memory");
        }
    }
    SDLY(ESD);
    PIN();
    {
        v4u e[3];
        const int eb = (int)SP_OFF + (cu & ~3) * 8192 + wave * 1024 + lane * 16;
        const int eo0 = eb + (cg < 1 ? 1 : 0) * 8192, eo1 = eb + (cg < 2 ? 2 : 1) * 8192, eo2 = eb + (cg < 3 ? 3 : 2) * 8192;
        XLD4(e[0], eo0); XLD4(e[1], eo1); XLD4(e[2], eo2);
        BULK(3);
        VMW2(cnt_u(3), cnt_o(3));
        asm volatile("" : "+v"(e[0]), "+v"(e[1]), "+v"(e[2]));
        for (unsigned spins = 0;;) {
            if (!__any(umax(umax(smax(e[0]), smax(e[1])), smax(e[2])) == SENT)) break;
            if (spin_fail(spins, tmo)) break;
            XLD4(e[0], eo0); XLD4(e[1], eo1); XLD4(e[2], eo2);
            asm volatile("s_waitcnt vmcnt(0)" : "+v"(e[0]), "+v"(e[1]), "+v"(e[2]) :: "memory");
        }
        STAMP(10);
        BULK(4);
        LAS float* sc = (LAS float*)(S + L_SC);
        const bool lo = cg < 2;
#pragma unroll
        for (int r = 0; r < 4; ++r) {
            const float e0 = __uint_as_float(e[0][r]), e1 = __uint_as_float(e[1][r]), e2 = __uint_as_float(e[2][r]);
            const float s = (e0 + (lo ? sown[r] : e1)) + (e2 + (lo ? e1 : sown[r]));
            sc[(4 * g4 + r) * 132 + 16 * wave + (lane & 15)] = s;
        }
    }
    asm volatile("s_waitcnt lgkmcnt(0)" : "+v"(vf[0]), "+v"(vf[1]), "+v"(vf[2]), "+v"(vf[3]), "+v"(vf[4]), "+v"(vf[5]), "+v"(vf[6]), "+v"(vf[7]) :: "memory");
    asm volatile("" : "+v"(vf[8]), "+v"(vf[9]), "+v"(vf[10]), "+v"(vf[11]), "+v"(vf[12]), "+v"(vf[13]), "+v"(vf[14]), "+v"(vf[15]));
    BAR();
    BULK(8);
    {
        const LAS f32x4* sc4 = (const LAS f32x4*)(S + L_SC + (lane & 15) * 528 + g4 * 32);
        f32x4 sv[8];
#pragma unroll
        for (int j = 0; j < 4; ++j) { sv[2 * j] = sc4[8 * j]; sv[2 * j + 1] = sc4[8 * j + 1]; }
        float mx = sv[0][0];
#pragma unroll
        for (int i = 0; i < 8; ++i)
#pragma unroll
            for (int k = 0; k < 4; ++k) mx = fmaxf(mx, sv[i][k]);
        mx = max16(mx); mx = max32(mx);
        v4u pf[4];
        float l = 0.f;
#pragma unroll
        for (int i = 0; i < 8; ++i) {
            const unsigned p01 = pack2(__builtin_amdgcn_exp2f(sv[i][0] - mx), __builtin_amdgcn_exp2f(sv[i][1] - mx));
            const unsigned p23 = pack2(__builtin_amdgcn_exp2f(sv[i][2] - mx), __builtin_amdgcn_exp2f(sv[i][3] - mx));
            pf[i >> 1][(i & 1) * 2] = p01; pf[i >> 1][(i & 1) * 2 + 1] = p23;
            l += (bflo(p01) + bfhi(p01)) + (bflo(p23) + bfhi(p23));
        }
        f32x4 o0 = {0.f, 0.f, 0.f, 0.f}, o1 = {0.f, 0.f, 0.f, 0.f};
#pragma unroll
        for (int j = 0; j < 4; ++j) {
            const v4u a0 = {vf[(j * 2) * 2][0], vf[(j * 2) * 2][1], vf[(j * 2) * 2 + 1][0], vf[(j * 2) * 2 + 1][1]};
            const v4u a1 = {vf[(j * 2 + 1) * 2][0], vf[(j * 2 + 1) * 2][1], vf[(j * 2 + 1) * 2 + 1][0], vf[(j * 2 + 1) * 2 + 1][1]};
            o0 = MFMA(a0, pf[j], o0);
            o1 = MFMA(a1, pf[j], o1);
        }
        v4u v; v[0] = canonb(pack2(o0[0], o0[1])); v[1] = canonb(pack2(o0[2], o0[3])); v[2] = canonb(pack2(o1[0], o1[1])); v[3] = canonb(pack2(o1[2], o1[3]));
        __builtin_amdgcn_raw_buffer_store_b128(v, rs_ws, (int)PCTX_OFF + ((((lane & 15) * 4 + cg) * 64 + bb * NKG + kg) * 256 + 32 * wave + 8 * g4) * 2, 0, 16);
        if (wave == 0) {
            l = add16(l); l = add32(l);
            if (lane < 16) __builtin_amdgcn_raw_buffer_store_b64((v2u){canon(__float_as_uint(mx)), canon(__float_as_uint(l))}, rs_ws, (int)ML_OFF + (((cg * 2 + bb) * 16 + lane) * 32 + kg) * 8, 0, 16);
        }
    }
    STAMP(4);
    PIN();
    BULK(5);
    {
        const int mt = wave & 3, kh = wave >> 2;
        const unsigned char* pg = ws + PCTX_OFF + (size_t)(((th * 4 + cq) * 64 + 16 * mt + g4) * 256 + 128 * kh) * 2;
        const unsigned pr = (unsigned)(size_t)(L + L_PG + wave * 4096 + (lane & 15) * 256);
        const int mo = (int)ML_OFF + (((cq * 2 + (mt >> 1)) * 16 + th) * 32 + 16 * (mt & 1) + 4 * g4) * 8;
        const int mo2 = mo ^ 128;
        const int q = (lane & 15) >> 2, p = lane & 3;
        const int swz = ((q & 2) | ((g4 & 1) << 2)) << 4;
        v4u pa[4], ml[4];
        v2u uf[32], of[24];
#define OFRD() do { \
            const unsigned oa = (unsigned)(size_t)(L + L_WO + wave * 5120 + (8 * g4 + q) * 128 + 8 * p); \
            const unsigned ob = (unsigned)(size_t)(L + L_WO2 + wave * 4096 + (8 * g4 + q) * 128 + 8 * p); \
            _Pragma("unroll") for (int tt = 0; tt < 4; ++tt) { \
                const unsigned a = oa + ((tt * 32) ^ swz), a2 = ob + ((tt * 32) ^ swz); \
                _Pragma("unroll") for (int u = 0; u < 2; ++u) { \
                    asm volatile("ds_read_b64_tr_b16 %0, %1 offset:%2" : "=v"(of[tt * 6 + u]) : "v"(a), "n"(u * 512) : "memory"); \
                    asm volatile("ds_read_b64_tr_b16 %0, %1 offset:%2" : "=v"(of[tt * 6 + 2 + u]) : "v"(a), "n"(1024 + u * 512) : "memory"); \
                    asm volatile("ds_read_b64_tr_b16 %0, %1 offset:%2" : "=v"(of[tt * 6 + 4 + u]) : "v"(a2), "n"(u * 512) : "memory"); \
                } \
            } } while (0)
#define PGLD(j) __builtin_amdgcn_global_load_lds((const unsigned*)(pg + (j) * 2048 + (((lane & 15) ^ (4 * (j) + g4)) << 4)), (LAS unsigned*)(L + L_PG + wave * 4096 + (j) * 1024), 16, 0, 16)
#define PGRD() do { _Pragma("unroll") for (int ks = 0; ks < 4; ++ks) asm volatile("ds_read_b128 %0, %1" : "=v"(pa[ks]) : "v"(pr + (((4 * ks + g4) ^ (lane & 15)) << 4)) : "memory"); \
                    asm volatile("s_waitcnt lgkmcnt(0)" : "+v"(pa[0]), "+v"(pa[1]), "+v"(pa[2]), "+v"(pa[3]) :: "memory"); } while (0)
#define E3POLL() do { PGLD(0); PGLD(1); PGLD(2); PGLD(3); \
                      XLD4(ml[0], mo); XLD4(ml[1], mo + 16); XLD4(ml[2], mo2); XLD4(ml[3], mo2 + 16); } while (0)
        VMW2(0, o_after_u(5));
        STAMP(7);
        BAR();
        STAMP(14);
        {
            const unsigned ua = (unsigned)(size_t)(L + L_WV + kh * 16384 + (8 * g4 + q) * 128 + 8 * p);
#pragma unroll
            for (int nt = 0; nt < 4; ++nt) {
                const unsigned a = ua + ((nt * 32) ^ swz);
#pragma unroll
                for (int ks = 0; ks < 4; ++ks)
#pragma unroll
                    for (int u = 0; u < 2; ++u)
                        asm volatile("ds_read_b64_tr_b16 %0, %1 offset:%2" : "=v"(uf[(nt * 4 + ks) * 2 + u]) : "v"(a), "n"(ks * 4096 + u * 512) : "memory");
            }
        }
        SDLY(E3D);
        E3POLL();
        BULK(6);
        STAMP(15);
        if (OEARLY) { if (wave < 7) { VMW(8); OFRD(); } }
        asm volatile("s_waitcnt lgkmcnt(0)" : "+v"(uf[0]), "+v"(uf[1]), "+v"(uf[2]), "+v"(uf[3]), "+v"(uf[4]), "+v"(uf[5]), "+v"(uf[6]), "+v"(uf[7]) :: "memory");
        asm volatile("" : "+v"(uf[8]), "+v"(uf[9]), "+v"(uf[10]), "+v"(uf[11]), "+v"(uf[12]), "+v"(uf[13]), "+v"(uf[14]), "+v"(uf[15]));
        asm volatile("" : "+v"(uf[16]), "+v"(uf[17]), "+v"(uf[18]), "+v"(uf[19]), "+v"(uf[20]), "+v"(uf[21]), "+v"(uf[22]), "+v"(uf[23]));
        asm volatile("" : "+v"(uf[24]), "+v"(uf[25]), "+v"(uf[26]), "+v"(uf[27]), "+v"(uf[28]), "+v"(uf[29]), "+v"(uf[30]), "+v"(uf[31]));
        VMW2(0, cnt_o(6));
        asm volatile("" : "+v"(ml[0]), "+v"(ml[1]), "+v"(ml[2]), "+v"(ml[3]));
        for (unsigned spins = 0;;) {
            PGRD();
            if (!__any(umax(umax(umax(smax(pa[0]), smax(pa[1])), umax(smax(pa[2]), smax(pa[3]))), umax(umax(smax(ml[0]), smax(ml[1])), umax(smax(ml[2]), smax(ml[3])))) == SENT)) break;
            if (spin_fail(spins, tmo)) break;
            {
                const unsigned long long bm = __ballot(umax(umax(smax(pa[0]), smax(pa[1])), umax(smax(pa[2]), smax(pa[3]))) == SENT);
                const unsigned rows = ((unsigned)bm | (unsigned)(bm >> 16) | (unsigned)(bm >> 32) | (unsigned)(bm >> 48)) >> g4;
                if (rows & 1u) PGLD(0);
                if (rows & 16u) PGLD(1);
                if (rows & 256u) PGLD(2);
                if (rows & 4096u) PGLD(3);
            }
            if (has_sent(ml[0])) XLD4R(ml[0], mo);
            if (has_sent(ml[1])) XLD4R(ml[1], mo + 16);
            if (has_sent(ml[2])) XLD4R(ml[2], mo2);
            if (has_sent(ml[3])) XLD4R(ml[3], mo2 + 16);
            asm volatile("s_waitcnt vmcnt(0)" : "+v"(ml[0]), "+v"(ml[1]), "+v"(ml[2]), "+v"(ml[3]) :: "memory");
        }
        STAMP(5);
        BULK(7);
        f32x4 T[4];
#pragma unroll
        for (int nt = 0; nt < 4; ++nt) T[nt] = (f32x4){0.f, 0.f, 0.f, 0.f};
#pragma unroll
        for (int ks = 0; ks < 4; ++ks)
#pragma unroll
            for (int nt = 0; nt < 4; ++nt) {
                const v4u bq = {uf[(nt * 4 + ks) * 2][0], uf[(nt * 4 + ks) * 2][1], uf[(nt * 4 + ks) * 2 + 1][0], uf[(nt * 4 + ks) * 2 + 1][1]};
                T[nt] = MFMA(pa[ks], bq, T[nt]);
            }
        if (!OEARLY) { if (wave < 7) { VM0(); OFRD(); } }
        float mv[8], lv[8];
#pragma unroll
        for (int i = 0; i < 4; ++i) { mv[2 * i] = __uint_as_float(ml[i][0]); lv[2 * i] = __uint_as_float(ml[i][1]); mv[2 * i + 1] = __uint_as_float(ml[i][2]); lv[2 * i + 1] = __uint_as_float(ml[i][3]); }
        float gm = fmaxf(fmaxf(fmaxf(mv[0], mv[1]), fmaxf(mv[2], mv[3])), fmaxf(fmaxf(mv[4], mv[5]), fmaxf(mv[6], mv[7])));
        gm = max16(gm); gm = max32(gm);
#pragma unroll
        for (int i = 0; i < 8; ++i) mv[i] = __builtin_amdgcn_exp2f(mv[i] - gm);
        float ls = ((mv[0] * lv[0] + mv[1] * lv[1]) + (mv[2] * lv[2] + mv[3] * lv[3])) + ((mv[4] * lv[4] + mv[5] * lv[5]) + (mv[6] * lv[6] + mv[7] * lv[7]));
        ls = add16(ls); ls = add32(ls);
        const float inv = 1.0f / ls;
        float sv[4];
        {
            float wr[4];
#pragma unroll
            for (int r = 0; r < 4; ++r) wr[r] = mv[r] * inv;
#pragma unroll
            for (int nt = 0; nt < 4; ++nt) sv[nt] = (T[nt][0] * wr[0] + T[nt][1] * wr[1]) + (T[nt][2] * wr[2] + T[nt][3] * wr[3]);
        }
        const auto r0 = __builtin_amdgcn_permlane32_swap(__float_as_uint(sv[0]), __float_as_uint(sv[2]), false, false);
        const auto r1 = __builtin_amdgcn_permlane32_swap(__float_as_uint(sv[1]), __float_as_uint(sv[3]), false, false);
        const float x0 = __uint_as_float(r0[0]) + __uint_as_float(r0[1]), x1 = __uint_as_float(r1[0]) + __uint_as_float(r1[1]);
        const auto r2 = __builtin_amdgcn_permlane16_swap(__float_as_uint(x0), __float_as_uint(x1), false, false);
        const float cv = __uint_as_float(r2[0]) + __uint_as_float(r2[1]);
        {
            const unsigned hi = f2bf(cv), lo = f2bf(cv - __uint_as_float(hi << 16));
            LAS unsigned short* cb = (LAS unsigned short*)(S + L_CB + ((mt << 1) | (kh << 3)) * 144) + ((lane - 8 * ROT * cq) & 63);
            cb[0] = (unsigned short)hi; cb[72] = (unsigned short)lo;
        }
        v4u hb = {0u, 0u, 0u, 0u};
        if (g4 == 0 && (lane & 10) == 0) hb = *(const LAS v4u*)(S + L_HM + ((lane >> 2) * 2 + (lane & 1)) * 16);
        asm volatile("s_waitcnt lgkmcnt(0)" : "+v"(of[0]), "+v"(of[1]), "+v"(of[2]), "+v"(of[3]), "+v"(of[4]), "+v"(of[5]), "+v"(of[6]), "+v"(of[7]) :: "memory");
        asm volatile("" : "+v"(of[8]), "+v"(of[9]), "+v"(of[10]), "+v"(of[11]), "+v"(of[12]), "+v"(of[13]), "+v"(of[14]), "+v"(of[15]));
        asm volatile("" : "+v"(of[16]), "+v"(of[17]), "+v"(of[18]), "+v"(of[19]), "+v"(of[20]), "+v"(of[21]), "+v"(of[22]), "+v"(of[23]));
        STAMP(6);
        BAR();
        STAMP(11);
        if (wave < 7) {
            const v4u b0 = *(const LAS v4u*)(S + L_CB + (lane & 15) * 144 + g4 * 16);
            const v4u b1v = *(const LAS v4u*)(S + L_CB + (lane & 15) * 144 + 64 + g4 * 16);
            f32x4 o[4];
#pragma unroll
            for (int tt = 0; tt < 4; ++tt) {
                const v4u a0 = {of[tt * 6][0], of[tt * 6][1], of[tt * 6 + 1][0], of[tt * 6 + 1][1]};
                o[tt] = (f32x4){0.f, 0.f, 0.f, 0.f};
                o[tt] = MFMA(a0, hb, o[tt]);
            }
#pragma unroll
            for (int tt = 0; tt < 4; ++tt) {
                const v4u a1 = {of[tt * 6 + 2][0], of[tt * 6 + 2][1], of[tt * 6 + 3][0], of[tt * 6 + 3][1]};
                o[tt] = MFMA(a1, b0, o[tt]);
            }
#pragma unroll
            for (int tt = 0; tt < 4; ++tt) {
                const v4u a2 = {of[tt * 6 + 4][0], of[tt * 6 + 4][1], of[tt * 6 + 5][0], of[tt * 6 + 5][1]};
                o[tt] = MFMA(a2, b1v, o[tt]);
            }
            const bool l0 = lane & 1, l1 = lane & 2;
            v4u v;
#pragma unroll
            for (int r = 0; r < 4; ++r) {
                const float y0 = (l0 ? o[1][r] : o[0][r]) + dppf<0xB1>(l0 ? o[0][r] : o[1][r]);
                const float y1 = (l0 ? o[3][r] : o[2][r]) + dppf<0xB1>(l0 ? o[2][r] : o[3][r]);
                float z = (l1 ? y1 : y0) + dppf<0x4E>(l1 ? y0 : y1);
                z += dppf<0x128>(z);
                v[r] = canon(__float_as_uint(z));
            }
            if ((lane & 8) == 0)
                __builtin_amdgcn_raw_buffer_store_b128(v, rs_ws, (int)G5_OFF + (((8 * wave + 2 * (lane & 3) + (g4 >> 1)) * 256 + cu) * 16 + ((lane >> 2) & 1) * 8 + (g4 & 1) * 4) * 4, 0, 16);
        }
    }
    STAMP(8);
    if (cu < NC5) {
        v4u v0 = {SENT, SENT, SENT, SENT}, v1 = {SENT, SENT, SENT, SENT};
        SDLY(E5D);
        for (unsigned spins = 0;;) {
            if (has_sent(v0)) v0 = __builtin_amdgcn_raw_buffer_load_b128(rs_ws, (int)(G5_OFF + (size_t)cu * 16384 + tid * 16), 0, 16);
            if (has_sent(v1)) v1 = __builtin_amdgcn_raw_buffer_load_b128(rs_ws, (int)(G5_OFF + (size_t)cu * 16384 + 8192 + tid * 16), 0, 16);
            if (!__any(umax(smax(v0), smax(v1)) == SENT)) break;
            if (spin_fail(spins, tmo)) break;
        }
        f32x4 s; s[0] = __uint_as_float(v0[0]) + __uint_as_float(v1[0]); s[1] = __uint_as_float(v0[1]) + __uint_as_float(v1[1]);
        s[2] = __uint_as_float(v0[2]) + __uint_as_float(v1[2]); s[3] = __uint_as_float(v0[3]) + __uint_as_float(v1[3]);
#pragma unroll
        for (int k = 0; k < 4; ++k) { float v = s[k]; v += dppf<0x124>(v); s[k] = add_hi3(v); }
        LAS float* red = (LAS float*)(S + L_CC);
        if (lane < 4) *(LAS f32x4*)(red + wave * 16 + lane * 4) = s;
        LGKM0();
        BAR();
        if (tid < 16) {
            float t = 0.f;
#pragma unroll
            for (int w = 0; w < 8; ++w) t += red[w * 16 + tid];
            const int b = tid >> 3, m = cu * 8 + (tid & 7);
            t += xb;
            out[b * M + m] = (unsigned short)f2bf(t);
        }
        STAMP(9);
        if (cu == 0 && wave == 0) {
            unsigned smin = 0x7fffffff, smaxv = 0, emax = 0;
            for (int i = lane; i < 368; i += 64) {
                const unsigned a = pt[0] - (unsigned)((gu64*)(ws + K1ST_OFF))[i * 2], b = pt[0] - (unsigned)((gu64*)(ws + K1ST_OFF))[i * 2 + 1];
                smaxv = a > smaxv ? a : smaxv;
                smin = b < smin ? b : smin;
                emax = 0;
            }
            for (int o = 1; o < 64; o <<= 1) { const unsigned a = __shfl_xor(smaxv, o), b = __shfl_xor(smin, o); smaxv = a > smaxv ? a : smaxv; smin = b < smin ? b : smin; }
            unsigned iv = 0;
            if (PROBE == 100) iv = smaxv;
            else if (PROBE == 101) iv = smin;
            else if (PROBE > 0) iv = pt[PROBE % 16] - pt[0];
            if (iv > 5000) iv = 5000;
            iv = __builtin_amdgcn_readfirstlane(iv) * 2;
            const unsigned t1 = (unsigned)rt();
            while ((unsigned)rt() - t1 < iv) __builtin_amdgcn_s_sleep(1);
        }
    }
}
}

extern "C" void kernel_launch(void* const* d_in, const int* in_sizes, int n_in,
                              void* d_out, int out_size, void* d_ws, size_t ws_size,
                              hipStream_t stream) {
    const unsigned short* x = (const unsigned short*)d_in[0];
    const unsigned short* ckv = (const unsigned short*)d_in[1];
    const unsigned short* wq = (const unsigned short*)d_in[2];
    const unsigned short* wuk = (const unsigned short*)d_in[3];
    const unsigned short* wuv = (const unsigned short*)d_in[4];
    const unsigned short* wo = (const unsigned short*)d_in[5];
    const unsigned short* w1 = (const unsigned short*)d_in[7];
    const unsigned short* b1 = (const unsigned short*)d_in[8];
    const unsigned short* w2 = (const unsigned short*)d_in[9];
    const unsigned short* b2 = (const unsigned short*)d_in[10];
    unsigned char* ws = (unsigned char*)d_ws;
    k1<<<368, 320, 0, stream>>>(x, wq, w1, ws);
    k2<<<NCU, 512, 0, stream>>>(x, ckv, wuk, wuv, wo, b1, w2, b2, ws, (unsigned short*)d_out);
}
```

```cpp
#include <hip/hip_runtime.h>
#include <stdint.h>

#define GAS __attribute__((address_space(1)))
#define LAS __attribute__((address_space(3)))
typedef unsigned v4u __attribute__((ext_vector_type(4)));
typedef unsigned v2u __attribute__((ext_vector_type(2)));
typedef float f32x4 __attribute__((ext_vector_type(4)));
typedef float f32x2 __attribute__((ext_vector_type(2)));
typedef short bf16x8 __attribute__((ext_vector_type(8)));
typedef short s16x4 __attribute__((ext_vector_type(4)));
typedef __bf16 bf16x2_t __attribute__((ext_vector_type(2)));
typedef GAS unsigned gu32;
typedef GAS unsigned long long gu64;
#define RLX_AGENT __ATOMIC_RELAXED, __HIP_MEMORY_SCOPE_AGENT

#define STAMPS 0
#define WSEND 0
#define FBAR 1
#define CKV 0
#define FST 0
#define S2M 0
#define PFD 0
#define K1PF 0
#define WIN 16
#define E2D2 0
#define E2D 8
#define FILLM 0
#define QPM 0
#define E3D 0
#define ESD 2
#define PU 4491
#define PO 555555559
#define ROT 2
#define MRT 1
#define E5D 6
#define E3O 0
#define E4D 0
#define SDLY(n) do { _Pragma("unroll") for (int i_ = 0; i_ < (n) / 4; ++i_) __builtin_amdgcn_s_sleep(16); if ((n) % 4) __builtin_amdgcn_s_sleep(4 * ((n) % 4)); } while (0)
#define PIN() __builtin_amdgcn_sched_barrier(0)

namespace {
constexpr int H = 16, SK = 4096, D = 256, M = 448, DC = 1024, F = 1792;
constexpr int NCU = 256;
constexpr int NKG = 32, KPG = 128;
constexpr int NC5 = 56;

constexpr size_t TMO_OFF = 0;
constexpr size_t QABS_OFF = 4096;
constexpr size_t ML_OFF = QABS_OFF + 131072;
constexpr size_t SP_OFF = ML_OFF + 32768;
constexpr size_t PCTX_OFF = SP_OFF + (size_t)256 * 8192;
constexpr size_t G5_OFF = PCTX_OFF + (size_t)2 * NKG * 16 * 1024 * 2;
constexpr size_t G5_BYTES = (size_t)56 * 256 * 16 * 4;
constexpr size_t FILL_BYTES = G5_OFF + G5_BYTES;
constexpr size_t QP_OFF = FILL_BYTES;
constexpr size_t HP_OFF = QP_OFF + 4 * 2 * 4096 * 4;
constexpr size_t STAMP_OFF = HP_OFF + 65536;
constexpr size_t K1ST_OFF = STAMP_OFF + 256 * 16 * 8;
constexpr size_t WS_NEED = K1ST_OFF + 512 * 2 * 8;
static_assert(FILL_BYTES % 16 == 0, "fill");

constexpr int RS2 = 1040;
constexpr int L_CKV = 0;
constexpr int L_WK = 64 * RS2;
constexpr int L_WV = L_WK;
constexpr int L_WO = L_WK + 32 * RS2;
constexpr int L_WO2 = L_CKV;
constexpr int L_PG = L_CKV + 7 * 4096;
constexpr int LDS_DMA_BYTES = L_WO + 7 * 5120;
static_assert(L_PG + 8 * 4096 <= L_WK, "lds");
constexpr int L_SC = 0;
constexpr int L_QF = 8448;
constexpr int L_CB = L_QF;
constexpr int L_QA = L_QF + 16384;
constexpr int L_CC = L_QA + 2048;
constexpr int L_HM = L_CC + 512;
constexpr int LDS_SC_BYTES = L_HM + 64;
static_assert(LDS_DMA_BYTES + LDS_SC_BYTES <= 160 * 1024, "lds");

constexpr unsigned kSpinCap = 1u << 18;
constexpr unsigned SENT = 0xFFFFFFFFu;

__device__ __forceinline__ float bf2f(unsigned short u) { return __uint_as_float(((unsigned)u) << 16); }
__device__ __forceinline__ float bflo(unsigned u) { return __uint_as_float(u << 16); }
__device__ __forceinline__ float bfhi(unsigned u) { return __uint_as_float(u & 0xffff0000u); }
__device__ __forceinline__ unsigned f2bf(float f) { unsigned u = __float_as_uint(f); return (u + 0x7fffu + ((u >> 16) & 1u)) >> 16; }
__device__ __forceinline__ unsigned pack2(float a, float b) { const f32x2 v = {a, b}; return __builtin_bit_cast(unsigned, __builtin_convertvector(v, bf16x2_t)); }
template <int C> __device__ __forceinline__ float dppf(float v) { return __int_as_float(__builtin_amdgcn_update_dpp(0, __float_as_int(v), C, 0xF, 0xF, true)); }
__device__ __forceinline__ float add16(float v) { auto r = __builtin_amdgcn_permlane16_swap(__float_as_uint(v), __float_as_uint(v), false, false); return __uint_as_float(r[0]) + __uint_as_float(r[1]); }
__device__ __forceinline__ float add32(float v) { auto r = __builtin_amdgcn_permlane32_swap(__float_as_uint(v), __float_as_uint(v), false, false); return __uint_as_float(r[0]) + __uint_as_float(r[1]); }
__device__ __forceinline__ float max16(float v) { auto r = __builtin_amdgcn_permlane16_swap(__float_as_uint(v), __float_as_uint(v), false, false); return fmaxf(__uint_as_float(r[0]), __uint_as_float(r[1])); }
__device__ __forceinline__ float max32(float v) { auto r = __builtin_amdgcn_permlane32_swap(__float_as_uint(v), __float_as_uint(v), false, false); return fmaxf(__uint_as_float(r[0]), __uint_as_float(r[1])); }
__device__ __forceinline__ float add_hi3(float v) { v += dppf<0x128>(v); v = add16(v); return add32(v); }
__device__ __forceinline__ float max_hi3(float v) { v = fmaxf(v, dppf<0x128>(v)); v = max16(v); return max32(v); }
__device__ __forceinline__ float add_lo5(float v) { v += dppf<0xB1>(v); v += dppf<0x4E>(v); v += dppf<0x141>(v); v += dppf<0x140>(v); return add16(v); }
__device__ __forceinline__ float max_lo5(float v) { v = fmaxf(v, dppf<0xB1>(v)); v = fmaxf(v, dppf<0x4E>(v)); v = fmaxf(v, dppf<0x141>(v)); v = fmaxf(v, dppf<0x140>(v)); return max16(v); }
__device__ __forceinline__ float wsum(float v) { return add32(add_lo5(v)); }
__device__ __forceinline__ unsigned long long rt() { return __builtin_amdgcn_s_memrealtime(); }

#define BAR() __builtin_amdgcn_s_barrier()
#define LGKM0() asm volatile("s_waitcnt lgkmcnt(0)" ::: "memory")
#define VM0() asm volatile("s_waitcnt vmcnt(0)" ::: "memory")
#define MFMA(a, b, c) __builtin_amdgcn_mfma_f32_16x16x32_bf16(__builtin_bit_cast(bf16x8, a), __builtin_bit_cast(bf16x8, b), c, 0, 0, 0)

__global__ void __launch_bounds__(320) k1(const unsigned short* __restrict__ x, const unsigned short* __restrict__ wq,
                                          const unsigned short* __restrict__ w1, unsigned char* __restrict__ ws) {
    __shared__ float xn[2][M];
    __shared__ float red[4][2][64];
    const int tid = threadIdx.x, wave = __builtin_amdgcn_readfirstlane(tid >> 6), lane = tid & 63;
    const int blk = blockIdx.x;
    const bool isq = blk < 256;
    if (wave == 4) {
        const v4u sn = {SENT, SENT, SENT, SENT};
        const unsigned tot16 = (unsigned)(FILL_BYTES / 16);
        const __amdgpu_buffer_rsrc_t rsf = __builtin_amdgcn_make_buffer_rsrc(ws, 0, (int)WS_NEED, 0x00020000);
        SDLY(PFD);
        for (unsigned i = blk * 64 + lane; i < tot16; i += gridDim.x * 64) __builtin_amdgcn_raw_buffer_store_b128(sn, rsf, (int)(i * 16), 0, FILLM);
        return;
    }
    const int idx = isq ? blk : blk - 256;
    const int mq = idx & 3, cg = idx >> 2;
    const int ncols = isq ? 4096 : F;
    const unsigned short* W = (isq ? wq : w1) + (size_t)(mq * 112) * ncols + cg * 64;
    const int r = tid >> 3, cch = tid & 7;
    unsigned xr[7];
#pragma unroll
    for (int i = 0; i < 7; ++i) asm volatile("global_load_ushort %0, %1, off" : "=v"(xr[i]) : "v"(x + (wave & 1) * M + lane + 64 * i) : "memory");
    v4u wv[4];
#pragma unroll
    for (int i = 0; i < 4; ++i) {
        const int row = (i < 3 || r < 16) ? r + 32 * i : 111;
        asm volatile("global_load_dwordx4 %0, %1, off nt" : "=v"(wv[i]) : "v"(W + (size_t)row * ncols + cch * 8) : "memory");
    }
    PIN();
    asm volatile("s_waitcnt vmcnt(4)" : "+v"(xr[0]), "+v"(xr[1]), "+v"(xr[2]), "+v"(xr[3]), "+v"(xr[4]), "+v"(xr[5]), "+v"(xr[6]) :: "memory");
    PIN();
    if (wave < 2) {
        float v[7]; float s = 0.f;
#pragma unroll
        for (int i = 0; i < 7; ++i) { v[i] = bf2f((unsigned short)xr[i]); s += v[i]; }
        s = wsum(s); const float mean = s * (1.0f / M);
        float ss = 0.f;
#pragma unroll
        for (int i = 0; i < 7; ++i) { const float d = v[i] - mean; ss += d * d; }
        ss = wsum(ss);
        const float rs = 1.0f / sqrtf(ss * (1.0f / M) + 1e-5f);
#pragma unroll
        for (int i = 0; i < 7; ++i) xn[wave][lane + 64 * i] = (v[i] - mean) * rs;
    }
    LGKM0(); BAR();
    asm volatile("s_waitcnt vmcnt(0)" : "+v"(wv[0]), "+v"(wv[1]), "+v"(wv[2]), "+v"(wv[3]) :: "memory");
    float a[2][8];
#pragma unroll
    for (int j = 0; j < 8; ++j) { a[0][j] = 0.f; a[1][j] = 0.f; }
#pragma unroll
    for (int i = 0; i < 4; ++i) {
        int row = mq * 112 + r + 32 * i; if (row >= M) row = M - 1;
        const bool ok = (i < 3 || r < 16);
        const float x0 = ok ? xn[0][row] : 0.f, x1 = ok ? xn[1][row] : 0.f;
#pragma unroll
        for (int j = 0; j < 4; ++j) {
            const float lo = bflo(wv[i][j]), hi = bfhi(wv[i][j]);
            a[0][2 * j] += x0 * lo; a[0][2 * j + 1] += x0 * hi;
            a[1][2 * j] += x1 * lo; a[1][2 * j + 1] += x1 * hi;
        }
    }
#pragma unroll
    for (int b = 0; b < 2; ++b)
#pragma unroll
        for (int j = 0; j < 8; ++j) a[b][j] = add_hi3(a[b][j]);
    if (lane < 8) {
#pragma unroll
        for (int b = 0; b < 2; ++b)
#pragma unroll
            for (int j = 0; j < 8; ++j) red[wave][b][lane * 8 + j] = a[b][j];
    }
    LGKM0(); BAR();
    if (tid < 128) {
        const int b = tid >> 6, c = tid & 63;
        const float s = red[0][b][c] + red[1][b][c] + red[2][b][c] + red[3][b][c];
        float* dst = (float*)(ws + (isq ? QP_OFF : HP_OFF));
        dst[((size_t)mq * 2 + b) * ncols + cg * 64 + c] = s;
    }
}

__device__ __forceinline__ bool spin_fail(unsigned& spins, gu32* tmo) {
    __builtin_amdgcn_s_sleep(1);
    if (++spins > kSpinCap) { __hip_atomic_store(tmo, 1u, RLX_AGENT); return true; }
    return (spins & 1023u) == 0u && __builtin_amdgcn_readfirstlane(__hip_atomic_load(tmo, RLX_AGENT)) == 1u;
}
__device__ __forceinline__ unsigned umax(unsigned a, unsigned b) { return a > b ? a : b; }
__device__ __forceinline__ unsigned smax(v4u v) { return umax(umax(umax(v[0], v[1]), v[2]), v[3]); }
__device__ __forceinline__ bool has_sent(v4u v) { return smax(v) == SENT; }
__device__ __forceinline__ unsigned canon(unsigned u) { return u == SENT ? 0x7FC00000u : u; }
__device__ __forceinline__ unsigned canonb(unsigned u) { return u == SENT ? 0xFFC0FFC0u : u; }

#define GLDS_NT(src, dst) __builtin_amdgcn_global_load_lds((const unsigned*)(src), (LAS unsigned*)(dst), 16, 0, 2)
#define OAUX 0
#define GLDS_O(src, dst) __builtin_amdgcn_global_load_lds((const unsigned*)(src), (LAS unsigned*)(dst), 16, 0, OAUX)
#define STAMP(i) do {} while (0)
#define STAMPW(i, w) do {} while (0)

constexpr int dig(int v, int i) { return i == 0 ? v % 10 : dig(v / 10, i - 1); }
constexpr int rank_of(int p) { return p == 1 ? 0 : (p == 9 ? 1 : (p == 8 ? 5 : (p >= 5 ? p + 1 : p))); }
constexpr int cnt_u(int p) { int n = 0; for (int i = 0; i < 4; ++i) n += dig(PU, i) == p; return n; }
constexpr int cnt_o(int p) { int n = 0; for (int i = 0; i < 9; ++i) n += dig(PO, i) == p; return n; }
constexpr int last_u() { int l = 0; for (int i = 0; i < 4; ++i) if (rank_of(dig(PU, i)) > l) l = rank_of(dig(PU, i)); return l; }
constexpr int o_after_u(int p) {
    int n = 0; for (int i = 0; i < 9; ++i) n += (rank_of(dig(PO, i)) >= last_u() && rank_of(dig(PO, i)) <= rank_of(p)); return n; }
#define OE 1
constexpr bool OEARLY = OE && cnt_o(6) + cnt_o(7) == 0;
static_assert(last_u() <= rank_of(5), "w_uv");
static_assert(rank_of(dig(PO, 5)) >= 5 && rank_of(dig(PO, 6)) >= 5 && rank_of(dig(PO, 7)) >= 5 && rank_of(dig(PO, 8)) >= 5, "w_o late part");
#define BULK(p) do { \
        _Pragma("unroll") for (int i = 0; i < 4; ++i) if (dig(PU, i) == (p)) \
            GLDS_NT(wuv + ((size_t)(256 * cq + 32 * wave + 8 * i + (lane >> 3)) * H + th) * D + 64 * dq + (((lane & 7) ^ ((lane >> 3) & 2) ^ ((i & 1) << 2)) * 8), L + L_WV + wave * 4096 + i * 1024); \
        if (cnt_o(p) && wave < 7) { \
            _Pragma("unroll") for (int i = 0; i < 9; ++i) if (dig(PO, i) == (p)) { \
                const int row = lane >> 3; \
                const unsigned short* sp = i > 0 ? wo + ((size_t)th * D + 64 * dq + 8 * ((i - 1 + ROT * cq) & 7) + row) * M : w2 + ((size_t)cu * 7 + (row < 7 ? row : 6)) * M; \
                GLDS_O(sp + 64 * wave + (((lane & 7) ^ (row & 2) ^ (i > 0 ? ((i - 1) & 1) << 2 : 0)) * 8), L + (i < 5 ? L_WO + wave * 5120 + i * 1024 : L_WO2 + wave * 4096 + (i - 5) * 1024)); \
            } \
        } \
        PIN(); \
    } while (0)
#define VMW2(a, b) do { if ((b) > 0 && wave == 7) VMW(a); else VMW((a) + (b)); } while (0)
#define VMW(n) asm volatile("s_waitcnt vmcnt(%0)" :: "n"(n) : "memory")
#define XB 0
#define XBITS "sc1"
#define XLD4(dst, off) asm volatile("buffer_load_dwordx4 %0, %1, %2, 0 offen " XBITS : "=v"(dst) : "v"(off), "s"(rs_ws) : "memory")
#define XLD4R(dst, off) asm volatile("buffer_load_dwordx4 %0, %1, %2, 0 offen " XBITS : "+v"(dst) : "v"(off), "s"(rs_ws) : "memory")
#define XLD2(dst, off) asm volatile("buffer_load_dwordx2 %0, %1, %2, 0 offen sc1" : "=v"(dst) : "v"(off), "s"(rs_ws) : "memory")
__global__ void __launch_bounds__(512, 1) k2(const unsigned short* __restrict__ x, const unsigned short* __restrict__ ckv,
                                             const unsigned short* __restrict__ wuk, const unsigned short* __restrict__ wuv,
                                             const unsigned short* __restrict__ wo, const unsigned short* __restrict__ b1,
                                             const unsigned short* __restrict__ w2, const unsigned short* __restrict__ b2,
                                             unsigned char* __restrict__ ws, unsigned short* __restrict__ out) {
    __shared__ __attribute__((aligned(16))) unsigned char lds_dma[LDS_DMA_BYTES];
    __shared__ __attribute__((aligned(16))) unsigned char lds_sc[LDS_SC_BYTES];
    LAS unsigned char* L = (LAS unsigned char*)lds_dma;
    LAS unsigned char* S = (LAS unsigned char*)lds_sc;
    const int tid = threadIdx.x, wave = __builtin_amdgcn_readfirstlane(tid >> 6), lane = tid & 63;
    const int cu = blockIdx.x;
    const int hh = cu >> 4, cs = cu & 15;
    const int bb = cu >> 7, kg = (cu >> 2) & 31, cg = cu & 3;
    const int th = (cu & 7) * 2 + (cu >> 7), dq = (cu >> 5) & 3, cq = (cu >> 3) & 3;
    const int g4 = lane >> 4;
    gu32* tmo = (gu32*)(ws + TMO_OFF);
    const __amdgpu_buffer_rsrc_t rs_ws = __builtin_amdgcn_make_buffer_rsrc(ws, 0, (int)WS_NEED, 0x00020000);
    STAMP(0);

    float qp[4];
    {
        const float* p = (const float*)(ws + QP_OFF) + (size_t)(tid >> 8) * 4096 + hh * D + (tid & 255);
#pragma unroll
        for (int i = 0; i < 4; ++i) asm volatile("global_load_dword %0, %1, off" : "=v"(qp[i]) : "v"(p + (size_t)i * 8192) : "memory");
    }
    unsigned xr0, xr1, hb1; float hp4[4];
    {
        const int b = (tid >> 3) & 1, m = (cu < NC5 ? cu : 0) * 8 + (tid & 7);
        asm volatile("global_load_ushort %0, %1, off" : "=v"(xr0) : "v"(x + b * M + m) : "memory");
        asm volatile("global_load_ushort %0, %1, off" : "=v"(xr1) : "v"(b2 + m) : "memory");
    }
    if (wave == 7) {
        const int l2 = lane < 14 ? lane : 13;
        const int b = l2 / 7, r = l2 % 7, f = cu * 7 + r;
        const float* hp = (const float*)(ws + HP_OFF);
        asm volatile("global_load_ushort %0, %1, off" : "=v"(hb1) : "v"(b1 + f) : "memory");
#pragma unroll
        for (int p = 0; p < 4; ++p) asm volatile("global_load_dword %0, %1, off" : "=v"(hp4[p]) : "v"(hp + ((size_t)p * 2 + b) * F + f) : "memory");
    }
    if (wave < 4) {
#pragma unroll
        for (int i = 0; i < 8; ++i) {
            const int r = 2 * i + (lane >> 5);
            const int ck = (lane & 31) ^ ((r & 7) << 2);
            GLDS_NT(wuk + ((size_t)(64 * cs + 16 * wave + r) * H + hh) * D + 8 * ck, L + L_WK + (8 * wave + i) * RS2);
        }
    }
    PIN();
    BAR();
    PIN();
    {
        const unsigned short* src = ckv + ((size_t)bb * SK + (size_t)kg * KPG + 16 * wave) * DC + 256 * cg;
#pragma unroll
        for (int i = 0; i < 8; ++i) {
            const int r = 2 * i + (lane >> 5);
            const int ck = (lane & 31) ^ ((r & 7) << 2);
            GLDS_NT(src + (size_t)r * DC + 8 * ck, L + L_CKV + (8 * wave + i) * RS2);
        }
    }
    PIN();
    if (wave < 4) asm volatile("s_waitcnt vmcnt(16)" ::: "memory");
    else asm volatile("s_waitcnt vmcnt(8)" ::: "memory");
    asm volatile("" : "+v"(qp[0]), "+v"(qp[1]), "+v"(qp[2]), "+v"(qp[3]), "+v"(xr0), "+v"(xr1));
    const float xb = bf2f((unsigned short)xr0) + bf2f((unsigned short)xr1);
    if (wave == 7) {
        asm volatile("" : "+v"(hb1), "+v"(hp4[0]), "+v"(hp4[1]), "+v"(hp4[2]), "+v"(hp4[3]));
        const float hv = bf2f((unsigned short)hb1) + hp4[0] + hp4[1] + hp4[2] + hp4[3];
        const float t = tanhf(0.7978845608028654f * (hv + 0.044715f * hv * hv * hv));
        const float gl = lane < 14 ? 0.5f * hv * (1.0f + t) : 0.f;
        const unsigned hi = f2bf(gl), lo = f2bf(gl - __uint_as_float(hi << 16));
        const int ix = lane < 14 ? (lane / 7) * 16 + lane % 7 : (lane - 14) * 16 + 7;
        if (lane < 16) { ((LAS unsigned short*)(S + L_HM))[ix] = (unsigned short)hi; ((LAS unsigned short*)(S + L_HM))[ix + 8] = (unsigned short)lo; }
    }
    {
        float s = (qp[0] + qp[1]) + (qp[2] + qp[3]);
        s *= 0.0625f * 1.4426950408889634f;
        const unsigned hi = f2bf(s);
        const unsigned lo = f2bf(s - __uint_as_float(hi << 16));
        const int b = tid >> 8, d = tid & 255;
        ((LAS unsigned short*)(S + L_QA))[(2 * b) * 256 + d] = (unsigned short)hi;
        ((LAS unsigned short*)(S + L_QA))[(2 * b + 1) * 256 + d] = (unsigned short)lo;
    }
    LGKM0();
    BAR();
    STAMPW(1, 5);
    if (wave < 4) {
        v4u av[8], wk[8];
#pragma unroll
        for (int ks = 0; ks < 8; ++ks) av[ks] = *(const LAS v4u*)(S + L_QA + (lane & 3) * 512 + (32 * ks + 8 * g4) * 2);
        asm volatile("s_waitcnt vmcnt(8)" ::: "memory");
        {
            const unsigned wa = (unsigned)(size_t)(L + L_WK + (8 * wave + ((lane & 15) >> 1)) * RS2 + (lane & 1) * 512 + g4 * 16);
#pragma unroll
            for (int ks = 0; ks < 8; ++ks) asm volatile("ds_read_b128 %0, %1" : "=v"(wk[ks]) : "v"(wa + ((ks ^ (lane & 7)) << 6)) : "memory");
            asm volatile("s_waitcnt lgkmcnt(0)" : "+v"(wk[0]), "+v"(wk[1]), "+v"(wk[2]), "+v"(wk[3]), "+v"(wk[4]), "+v"(wk[5]), "+v"(wk[6]), "+v"(wk[7]) :: "memory");
        }
        f32x4 acc0 = {0.f, 0.f, 0.f, 0.f}, acc1 = {0.f, 0.f, 0.f, 0.f};
#pragma unroll
        for (int ks = 0; ks < 8; ks += 2) { acc0 = MFMA(wk[ks], av[ks], acc0); acc1 = MFMA(wk[ks + 1], av[ks + 1], acc1); }
        acc0 += acc1;
        unsigned hb[4];
#pragma unroll
        for (int r = 0; r < 4; ++r) {
            const float v = acc0[r] + dppf<0xB1>(acc0[r]);
            const unsigned h0 = f2bf(v);
            hb[r] = (lane & 1) ? f2bf(v - __uint_as_float(h0 << 16)) : h0;
        }
        if ((lane & 15) < 4) {
            const v2u v = {canonb(hb[0] | (hb[1] << 16)), canonb(hb[2] | (hb[3] << 16))};
            __builtin_amdgcn_raw_buffer_store_b64(v, rs_ws, (int)QABS_OFF + ((((((lane >> 1) & 1) * 4 + (cs >> 2)) * 2 + (lane & 1)) * 8 + ((2 * cs + (wave >> 1)) & 7)) * 64 + hh + 16 * (2 * (wave & 1) + (g4 >> 1))) * 16 + (g4 & 1) * 8, 0, 16);
        }
        STAMP(2);
    }
    PIN();
    BAR();
    SDLY(E2D);
    PIN();
    const int qoff = (int)QABS_OFF + ((bb * 4 + cg) * 2 * 8 + wave) * 1024 + lane * 16;
    v4u qa[2];
    XLD4(qa[0], qoff);
    XLD4(qa[1], qoff + 8192);
    asm volatile("s_waitcnt vmcnt(2)" ::: "memory");
    STAMPW(13, 7);
    v4u kf[8];
    {
        const unsigned ka = (unsigned)(size_t)(L + L_CKV + (8 * wave + ((lane & 15) >> 1)) * RS2 + (lane & 1) * 512 + g4 * 16);
#pragma unroll
        for (int ks = 0; ks < 8; ++ks) asm volatile("ds_read_b128 %0, %1" : "=v"(kf[ks]) : "v"(ka + ((ks ^ (lane & 7)) << 6)) : "memory");
    }
    BULK(1);
    VMW2(cnt_u(1), cnt_o(1));
    asm volatile("" : "+v"(qa[0]), "+v"(qa[1]));
    for (unsigned spins = 0;;) {
        if (!__any(umax(smax(qa[0]), smax(qa[1])) == SENT)) break;
        if (spin_fail(spins, tmo)) break;
        XLD4(qa[0], qoff);
        XLD4(qa[1], qoff + 8192);
        asm volatile("s_waitcnt vmcnt(0)" : "+v"(qa[0]), "+v"(qa[1]) :: "memory");
    }
    *(LAS v4u*)(S + L_QF + wave * 1024 + lane * 16) = qa[0];
    *(LAS v4u*)(S + L_QF + 8192 + wave * 1024 + lane * 16) = qa[1];
    asm volatile("s_waitcnt lgkmcnt(0)" : "+v"(kf[0]), "+v"(kf[1]), "+v"(kf[2]), "+v"(kf[3]), "+v"(kf[4]), "+v"(kf[5]), "+v"(kf[6]), "+v"(kf[7]) :: "memory");
    BAR();
    STAMPW(12, 7);
    BULK(9);
    f32x4 sown;
    {
        f32x4 acc0 = {0.f, 0.f, 0.f, 0.f}, acc1 = {0.f, 0.f, 0.f, 0.f};
#pragma unroll
        for (int ks = 0; ks < 8; ++ks) {
            const v4u ah = *(const LAS v4u*)(S + L_QF + ks * 1024 + lane * 16);
            const v4u al = *(const LAS v4u*)(S + L_QF + 8192 + ks * 1024 + lane * 16);
            acc0 = MFMA(ah, kf[ks], acc0);
            acc1 = MFMA(al, kf[ks], acc1);
        }
        sown = acc0 + acc1;
        v4u v; v[0] = canon(__float_as_uint(sown[0])); v[1] = canon(__float_as_uint(sown[1])); v[2] = canon(__float_as_uint(sown[2])); v[3] = canon(__float_as_uint(sown[3]));
        __builtin_amdgcn_raw_buffer_store_b128(v, rs_ws, (int)SP_OFF + cu * 8192 + wave * 1024 + lane * 16, 0, 16);
    }
    STAMP(3);
    PIN();
    BULK(2);
    v2u vf[16];
    {
        const int q = (lane & 15) >> 2, p = lane & 3;
#pragma unroll
        for (int u = 0; u < 2; ++u) {
            const unsigned va = (unsigned)(size_t)(L + L_CKV + (4 * g4 + 2 * u + (q >> 1)) * RS2 + (q & 1) * 512 + ((wave ^ (4 * u + q)) << 6) + 16 * p);
#pragma unroll
            for (int j = 0; j < 4; ++j)
#pragma unroll
                for (int t = 0; t < 2; ++t)
                    asm volatile("ds_read_b64_tr_b16 %0, %1 offset:%2" : "=v"(vf[(j * 2 + t) * 2 + u]) : "v"(va), "n"(j * 16 * RS2 + 8 * t) : "memory");
        }
    }
    SDLY(ESD);
    PIN();
    {
        v4u e[3];
        const int eb = (int)SP_OFF + (cu & ~3) * 8192 + wave * 1024 + lane * 16;
        const int eo0 = eb + (cg < 1 ? 1 : 0) * 8192, eo1 = eb + (cg < 2 ? 2 : 1) * 8192, eo2 = eb + (cg < 3 ? 3 : 2) * 8192;
        XLD4(e[0], eo0); XLD4(e[1], eo1); XLD4(e[2], eo2);
        BULK(3);
        VMW2(cnt_u(3), cnt_o(3));
        asm volatile("" : "+v"(e[0]), "+v"(e[1]), "+v"(e[2]));
        for (unsigned spins = 0;;) {
            if (!__any(umax(umax(smax(e[0]), smax(e[1])), smax(e[2])) == SENT)) break;
            if (spin_fail(spins, tmo)) break;
            XLD4(e[0], eo0); XLD4(e[1], eo1); XLD4(e[2], eo2);
            asm volatile("s_waitcnt vmcnt(0)" : "+v"(e[0]), "+v"(e[1]), "+v"(e[2]) :: "memory");
        }
        STAMP(10);
        BULK(4);
        LAS float* sc = (LAS float*)(S + L_SC);
        const bool lo = cg < 2;
#pragma unroll
        for (int r = 0; r < 4; ++r) {
            const float e0 = __uint_as_float(e[0][r]), e1 = __uint_as_float(e[1][r]), e2 = __uint_as_float(e[2][r]);
            const float s = (e0 + (lo ? sown[r] : e1)) + (e2 + (lo ? e1 : sown[r]));
            sc[(4 * g4 + r) * 132 + 16 * wave + (lane & 15)] = s;
        }
    }
    asm volatile("s_waitcnt lgkmcnt(0)" : "+v"(vf[0]), "+v"(vf[1]), "+v"(vf[2]), "+v"(vf[3]), "+v"(vf[4]), "+v"(vf[5]), "+v"(vf[6]), "+v"(vf[7]) :: "memory");
    asm volatile("" : "+v"(vf[8]), "+v"(vf[9]), "+v"(vf[10]), "+v"(vf[11]), "+v"(vf[12]), "+v"(vf[13]), "+v"(vf[14]), "+v"(vf[15]));
    BAR();
    BULK(8);
    {
        const LAS f32x4* sc4 = (const LAS f32x4*)(S + L_SC + (lane & 15) * 528 + g4 * 32);
        f32x4 sv[8];
#pragma unroll
        for (int j = 0; j < 4; ++j) { sv[2 * j] = sc4[8 * j]; sv[2 * j + 1] = sc4[8 * j + 1]; }
        float mx = sv[0][0];
#pragma unroll
        for (int i = 0; i < 8; ++i)
#pragma unroll
            for (int k = 0; k < 4; ++k) mx = fmaxf(mx, sv[i][k]);
        mx = max16(mx); mx = max32(mx);
        v4u pf[4];
        float l = 0.f;
#pragma unroll
        for (int i = 0; i < 8; ++i) {
            const unsigned p01 = pack2(__builtin_amdgcn_exp2f(sv[i][0] - mx), __builtin_amdgcn_exp2f(sv[i][1] - mx));
            const unsigned p23 = pack2(__builtin_amdgcn_exp2f(sv[i][2] - mx), __builtin_amdgcn_exp2f(sv[i][3] - mx));
            pf[i >> 1][(i & 1) * 2] = p01; pf[i >> 1][(i & 1) * 2 + 1] = p23;
            l += (bflo(p01) + bfhi(p01)) + (bflo(p23) + bfhi(p23));
        }
        f32x4 o0 = {0.f, 0.f, 0.f, 0.f}, o1 = {0.f, 0.f, 0.f, 0.f};
#pragma unroll
        for (int j = 0; j < 4; ++j) {
            const v4u a0 = {vf[(j * 2) * 2][0], vf[(j * 2) * 2][1], vf[(j * 2) * 2 + 1][0], vf[(j * 2) * 2 + 1][1]};
            const v4u a1 = {vf[(j * 2 + 1) * 2][0], vf[(j * 2 + 1) * 2][1], vf[(j * 2 + 1) * 2 + 1][0], vf[(j * 2 + 1) * 2 + 1][1]};
            o0 = MFMA(a0, pf[j], o0);
            o1 = MFMA(a1, pf[j], o1);
        }
        v4u v; v[0] = canonb(pack2(o0[0], o0[1])); v[1] = canonb(pack2(o0[2], o0[3])); v[2] = canonb(pack2(o1[0], o1[1])); v[3] = canonb(pack2(o1[2], o1[3]));
        __builtin_amdgcn_raw_buffer_store_b128(v, rs_ws, (int)PCTX_OFF + ((((lane & 15) * 4 + cg) * 64 + bb * NKG + kg) * 256 + 32 * wave + 8 * g4) * 2, 0, 16);
        if (wave == 0) {
            l = add16(l); l = add32(l);
            if (lane < 16) __builtin_amdgcn_raw_buffer_store_b64((v2u){canon(__float_as_uint(mx)), canon(__float_as_uint(l))}, rs_ws, (int)ML_OFF + (((cg * 2 + bb) * 16 + lane) * 32 + kg) * 8, 0, 16);
        }
    }
    STAMP(4);
    PIN();
    BULK(5);
    {
        const int mt = wave & 3, kh = wave >> 2;
        const unsigned char* pg = ws + PCTX_OFF + (size_t)(((th * 4 + cq) * 64 + 16 * mt + g4) * 256 + 128 * kh) * 2;
        const unsigned pr = (unsigned)(size_t)(L + L_PG + wave * 4096 + (lane & 15) * 256);
        const int mo = (int)ML_OFF + (((cq * 2 + (mt >> 1)) * 16 + th) * 32 + 16 * (mt & 1) + 4 * g4) * 8;
        const int mo2 = mo ^ 128;
        const int q = (lane & 15) >> 2, p = lane & 3;
        const int swz = ((q & 2) | ((g4 & 1) << 2)) << 4;
        v4u pa[4], ml[4];
        v2u uf[32], of[24];
#define OFRD() do { \
            const unsigned oa = (unsigned)(size_t)(L + L_WO + wave * 5120 + (8 * g4 + q) * 128 + 8 * p); \
            const unsigned ob = (unsigned)(size_t)(L + L_WO2 + wave * 4096 + (8 * g4 + q) * 128 + 8 * p); \
            _Pragma("unroll") for (int tt = 0; tt < 4; ++tt) { \
                const unsigned a = oa + ((tt * 32) ^ swz), a2 = ob + ((tt * 32) ^ swz); \
                _Pragma("unroll") for (int u = 0; u < 2; ++u) { \
                    asm volatile("ds_read_b64_tr_b16 %0, %1 offset:%2" : "=v"(of[tt * 6 + u]) : "v"(a), "n"(u * 512) : "memory"); \
                    asm volatile("ds_read_b64_tr_b16 %0, %1 offset:%2" : "=v"(of[tt * 6 + 2 + u]) : "v"(a), "n"(1024 + u * 512) : "memory"); \
                    asm volatile("ds_read_b64_tr_b16 %0, %1 offset:%2" : "=v"(of[tt * 6 + 4 + u]) : "v"(a2), "n"(u * 512) : "memory"); \
                } \
            } } while (0)
#define PGLD(j) __builtin_amdgcn_global_load_lds((const unsigned*)(pg + (j) * 2048 + (((lane & 15) ^ (4 * (j) + g4)) << 4)), (LAS unsigned*)(L + L_PG + wave * 4096 + (j) * 1024), 16, 0, 16)
#define PGRD() do { _Pragma("unroll") for (int ks = 0; ks < 4; ++ks) asm volatile("ds_read_b128 %0, %1" : "=v"(pa[ks]) : "v"(pr + (((4 * ks + g4) ^ (lane & 15)) << 4)) : "memory"); \
                    asm volatile("s_waitcnt lgkmcnt(0)" : "+v"(pa[0]), "+v"(pa[1]), "+v"(pa[2]), "+v"(pa[3]) :: "memory"); } while (0)
#define E3POLL() do { PGLD(0); PGLD(1); PGLD(2); PGLD(3); \
                      XLD4(ml[0], mo); XLD4(ml[1], mo + 16); XLD4(ml[2], mo2); XLD4(ml[3], mo2 + 16); } while (0)
        VMW2(0, o_after_u(5));
        STAMP(7);
        BAR();
        STAMP(14);
        {
            const unsigned ua = (unsigned)(size_t)(L + L_WV + kh * 16384 + (8 * g4 + q) * 128 + 8 * p);
#pragma unroll
            for (int nt = 0; nt < 4; ++nt) {
                const unsigned a = ua + ((nt * 32) ^ swz);
#pragma unroll
                for (int ks = 0; ks < 4; ++ks)
#pragma unroll
                    for (int u = 0; u < 2; ++u)
                        asm volatile("ds_read_b64_tr_b16 %0, %1 offset:%2" : "=v"(uf[(nt * 4 + ks) * 2 + u]) : "v"(a), "n"(ks * 4096 + u * 512) : "memory");
            }
        }
        SDLY(E3D);
        E3POLL();
        BULK(6);
        STAMP(15);
        if (OEARLY) { if (wave < 7) { VMW(8); OFRD(); } }
        asm volatile("s_waitcnt lgkmcnt(0)" : "+v"(uf[0]), "+v"(uf[1]), "+v"(uf[2]), "+v"(uf[3]), "+v"(uf[4]), "+v"(uf[5]), "+v"(uf[6]), "+v"(uf[7]) :: "memory");
        asm volatile("" : "+v"(uf[8]), "+v"(uf[9]), "+v"(uf[10]), "+v"(uf[11]), "+v"(uf[12]), "+v"(uf[13]), "+v"(uf[14]), "+v"(uf[15]));
        asm volatile("" : "+v"(uf[16]), "+v"(uf[17]), "+v"(uf[18]), "+v"(uf[19]), "+v"(uf[20]), "+v"(uf[21]), "+v"(uf[22]), "+v"(uf[23]));
        asm volatile("" : "+v"(uf[24]), "+v"(uf[25]), "+v"(uf[26]), "+v"(uf[27]), "+v"(uf[28]), "+v"(uf[29]), "+v"(uf[30]), "+v"(uf[31]));
        VMW2(0, cnt_o(6));
        asm volatile("" : "+v"(ml[0]), "+v"(ml[1]), "+v"(ml[2]), "+v"(ml[3]));
        for (unsigned spins = 0;;) {
            PGRD();
            if (!__any(umax(umax(umax(smax(pa[0]), smax(pa[1])), umax(smax(pa[2]), smax(pa[3]))), umax(umax(smax(ml[0]), smax(ml[1])), umax(smax(ml[2]), smax(ml[3])))) == SENT)) break;
            if (spin_fail(spins, tmo)) break;
            {
                const unsigned long long bm = __ballot(umax(umax(smax(pa[0]), smax(pa[1])), umax(smax(pa[2]), smax(pa[3]))) == SENT);
                const unsigned rows = ((unsigned)bm | (unsigned)(bm >> 16) | (unsigned)(bm >> 32) | (unsigned)(bm >> 48)) >> g4;
                if (rows & 1u) PGLD(0);
                if (rows & 16u) PGLD(1);
                if (rows & 256u) PGLD(2);
                if (rows & 4096u) PGLD(3);
            }
            if (has_sent(ml[0])) XLD4R(ml[0], mo);
            if (has_sent(ml[1])) XLD4R(ml[1], mo + 16);
            if (has_sent(ml[2])) XLD4R(ml[2], mo2);
            if (has_sent(ml[3])) XLD4R(ml[3], mo2 + 16);
            asm volatile("s_waitcnt vmcnt(0)" : "+v"(ml[0]), "+v"(ml[1]), "+v"(ml[2]), "+v"(ml[3]) :: "memory");
        }
        STAMP(5);
        BULK(7);
        f32x4 T[4];
#pragma unroll
        for (int nt = 0; nt < 4; ++nt) T[nt] = (f32x4){0.f, 0.f, 0.f, 0.f};
#pragma unroll
        for (int ks = 0; ks < 4; ++ks)
#pragma unroll
            for (int nt = 0; nt < 4; ++nt) {
                const v4u bq = {uf[(nt * 4 + ks) * 2][0], uf[(nt * 4 + ks) * 2][1], uf[(nt * 4 + ks) * 2 + 1][0], uf[(nt * 4 + ks) * 2 + 1][1]};
                T[nt] = MFMA(pa[ks], bq, T[nt]);
            }
        if (!OEARLY) { if (wave < 7) { VM0(); OFRD(); } }
        float mv[8], lv[8];
#pragma unroll
        for (int i = 0; i < 4; ++i) { mv[2 * i] = __uint_as_float(ml[i][0]); lv[2 * i] = __uint_as_float(ml[i][1]); mv[2 * i + 1] = __uint_as_float(ml[i][2]); lv[2 * i + 1] = __uint_as_float(ml[i][3]); }
        float gm = fmaxf(fmaxf(fmaxf(mv[0], mv[1]), fmaxf(mv[2], mv[3])), fmaxf(fmaxf(mv[4], mv[5]), fmaxf(mv[6], mv[7])));
        gm = max16(gm); gm = max32(gm);
#pragma unroll
        for (int i = 0; i < 8; ++i) mv[i] = __builtin_amdgcn_exp2f(mv[i] - gm);
        float ls = ((mv[0] * lv[0] + mv[1] * lv[1]) + (mv[2] * lv[2] + mv[3] * lv[3])) + ((mv[4] * lv[4] + mv[5] * lv[5]) + (mv[6] * lv[6] + mv[7] * lv[7]));
        ls = add16(ls); ls = add32(ls);
        const float inv = 1.0f / ls;
        float sv[4];
        {
            float wr[4];
#pragma unroll
            for (int r = 0; r < 4; ++r) wr[r] = mv[r] * inv;
#pragma unroll
            for (int nt = 0; nt < 4; ++nt) sv[nt] = (T[nt][0] * wr[0] + T[nt][1] * wr[1]) + (T[nt][2] * wr[2] + T[nt][3] * wr[3]);
        }
        const auto r0 = __builtin_amdgcn_permlane32_swap(__float_as_uint(sv[0]), __float_as_uint(sv[2]), false, false);
        const auto r1 = __builtin_amdgcn_permlane32_swap(__float_as_uint(sv[1]), __float_as_uint(sv[3]), false, false);
        const float x0 = __uint_as_float(r0[0]) + __uint_as_float(r0[1]), x1 = __uint_as_float(r1[0]) + __uint_as_float(r1[1]);
        const auto r2 = __builtin_amdgcn_permlane16_swap(__float_as_uint(x0), __float_as_uint(x1), false, false);
        const float cv = __uint_as_float(r2[0]) + __uint_as_float(r2[1]);
        {
            const unsigned hi = f2bf(cv), lo = f2bf(cv - __uint_as_float(hi << 16));
            LAS unsigned short* cb = (LAS unsigned short*)(S + L_CB + ((mt << 1) | (kh << 3)) * 144) + ((lane - 8 * ROT * cq) & 63);
            cb[0] = (unsigned short)hi; cb[72] = (unsigned short)lo;
        }
        v4u hb = {0u, 0u, 0u, 0u};
        if (g4 == 0 && (lane & 10) == 0) hb = *(const LAS v4u*)(S + L_HM + ((lane >> 2) * 2 + (lane & 1)) * 16);
        asm volatile("s_waitcnt lgkmcnt(0)" : "+v"(of[0]), "+v"(of[1]), "+v"(of[2]), "+v"(of[3]), "+v"(of[4]), "+v"(of[5]), "+v"(of[6]), "+v"(of[7]) :: "memory");
        asm volatile("" : "+v"(of[8]), "+v"(of[9]), "+v"(of[10]), "+v"(of[11]), "+v"(of[12]), "+v"(of[13]), "+v"(of[14]), "+v"(of[15]));
        asm volatile("" : "+v"(of[16]), "+v"(of[17]), "+v"(of[18]), "+v"(of[19]), "+v"(of[20]), "+v"(of[21]), "+v"(of[22]), "+v"(of[23]));
        STAMP(6);
        BAR();
        STAMP(11);
        if (wave < 7) {
            const v4u b0 = *(const LAS v4u*)(S + L_CB + (lane & 15) * 144 + g4 * 16);
            const v4u b1v = *(const LAS v4u*)(S + L_CB + (lane & 15) * 144 + 64 + g4 * 16);
            f32x4 o[4];
#pragma unroll
            for (int tt = 0; tt < 4; ++tt) {
                const v4u a0 = {of[tt * 6][0], of[tt * 6][1], of[tt * 6 + 1][0], of[tt * 6 + 1][1]};
                o[tt] = (f32x4){0.f, 0.f, 0.f, 0.f};
                o[tt] = MFMA(a0, hb, o[tt]);
            }
#pragma unroll
            for (int tt = 0; tt < 4; ++tt) {
                const v4u a1 = {of[tt * 6 + 2][0], of[tt * 6 + 2][1], of[tt * 6 + 3][0], of[tt * 6 + 3][1]};
                o[tt] = MFMA(a1, b0, o[tt]);
            }
#pragma unroll
            for (int tt = 0; tt < 4; ++tt) {
                const v4u a2 = {of[tt * 6 + 4][0], of[tt * 6 + 4][1], of[tt * 6 + 5][0], of[tt * 6 + 5][1]};
                o[tt] = MFMA(a2, b1v, o[tt]);
            }
            const bool l0 = lane & 1, l1 = lane & 2;
            v4u v;
#pragma unroll
            for (int r = 0; r < 4; ++r) {
                const float y0 = (l0 ? o[1][r] : o[0][r]) + dppf<0xB1>(l0 ? o[0][r] : o[1][r]);
                const float y1 = (l0 ? o[3][r] : o[2][r]) + dppf<0xB1>(l0 ? o[2][r] : o[3][r]);
                float z = (l1 ? y1 : y0) + dppf<0x4E>(l1 ? y0 : y1);
                z += dppf<0x128>(z);
                v[r] = canon(__float_as_uint(z));
            }
            if ((lane & 8) == 0)
                __builtin_amdgcn_raw_buffer_store_b128(v, rs_ws, (int)G5_OFF + (((8 * wave + 2 * (lane & 3) + (g4 >> 1)) * 256 + cu) * 16 + ((lane >> 2) & 1) * 8 + (g4 & 1) * 4) * 4, 0, 16);
        }
    }
    STAMP(8);
    if (cu < NC5) {
        v4u v0 = {SENT, SENT, SENT, SENT}, v1 = {SENT, SENT, SENT, SENT};
        SDLY(E5D);
        for (unsigned spins = 0;;) {
            if (has_sent(v0)) v0 = __builtin_amdgcn_raw_buffer_load_b128(rs_ws, (int)(G5_OFF + (size_t)cu * 16384 + tid * 16), 0, 16);
            if (has_sent(v1)) v1 = __builtin_amdgcn_raw_buffer_load_b128(rs_ws, (int)(G5_OFF + (size_t)cu * 16384 + 8192 + tid * 16), 0, 16);
            if (!__any(umax(smax(v0), smax(v1)) == SENT)) break;
            if (spin_fail(spins, tmo)) break;
        }
        f32x4 s; s[0] = __uint_as_float(v0[0]) + __uint_as_float(v1[0]); s[1] = __uint_as_float(v0[1]) + __uint_as_float(v1[1]);
        s[2] = __uint_as_float(v0[2]) + __uint_as_float(v1[2]); s[3] = __uint_as_float(v0[3]) + __uint_as_float(v1[3]);
#pragma unroll
        for (int k = 0; k < 4; ++k) { float v = s[k]; v += dppf<0x124>(v); s[k] = add_hi3(v); }
        LAS float* red = (LAS float*)(S + L_CC);
        if (lane < 4) *(LAS f32x4*)(red + wave * 16 + lane * 4) = s;
        LGKM0();
        BAR();
        if (tid < 16) {
            float t = 0.f;
#pragma unroll
            for (int w = 0; w < 8; ++w) t += red[w * 16 + tid];
            const int b = tid >> 3, m = cu * 8 + (tid & 7);
            t += xb;
            out[b * M + m] = (unsigned short)f2bf(t);
        }
        STAMP(9);
    }
}
}

extern "C" void kernel_launch(void* const* d_in, const int* in_sizes, int n_in,
                              void* d_out, int out_size, void* d_ws, size_t ws_size,
                              hipStream_t stream) {
    const unsigned short* x = (const unsigned short*)d_in[0];
    const unsigned short* ckv = (const unsigned short*)d_in[1];
    const unsigned short* wq = (const unsigned short*)d_in[2];
    const unsigned short* wuk = (const unsigned short*)d_in[3];
    const unsigned short* wuv = (const unsigned short*)d_in[4];
    const unsigned short* wo = (const unsigned short*)d_in[5];
    const unsigned short* w1 = (const unsigned short*)d_in[7];
    const unsigned short* b1 = (const unsigned short*)d_in[8];
    const unsigned short* w2 = (const unsigned short*)d_in[9];
    const unsigned short* b2 = (const unsigned short*)d_in[10];
    unsigned char* ws = (unsigned char*)d_ws;
    k1<<<368, 320, 0, stream>>>(x, wq, w1, ws);
    k2<<<NCU, 512, 0, stream>>>(x, ckv, wuk, wuv, wo, b1, w2, b2, ws, (unsigned short*)d_out);
}
```

```cpp
#include <hip/hip_runtime.h>
#include <stdint.h>

#define GAS __attribute__((address_space(1)))
#define LAS __attribute__((address_space(3)))
typedef unsigned v4u __attribute__((ext_vector_type(4)));
typedef unsigned v2u __attribute__((ext_vector_type(2)));
typedef float f32x4 __attribute__((ext_vector_type(4)));
typedef float f32x2 __attribute__((ext_vector_type(2)));
typedef short bf16x8 __attribute__((ext_vector_type(8)));
typedef short s16x4 __attribute__((ext_vector_type(4)));
typedef __bf16 bf16x2_t __attribute__((ext_vector_type(2)));
typedef GAS unsigned gu32;
typedef GAS unsigned long long gu64;
#define RLX_AGENT __ATOMIC_RELAXED, __HIP_MEMORY_SCOPE_AGENT

#define STAMPS 0
#define WSEND 0
#define FBAR 1
#define CKV 0
#define FST 0
#define S2M 0
#define PFD 0
#define K1PF 0
#define WIN 16
#define E2D2 4
#define E2D 10
#define FILLM 0
#define QPM 0
#define E3D 0
#define ESD 2
#define PU 4491
#define PO 555555559
#define ROT 2
#define MRT 1
#define E5D 6
#define E3O 0
#define E4D 0
#define PACEK 4
#define PACEC 4
#define D0 6
#define D1 2
#define PI 1
#define RSI 8
#define QD 10
#define QD2 4
#define HD 0
#define HW 1
#define FR 16
#define FWD 6
#define FFIN 0
#define FLN 1
#define DW 3
#define FPD 10
#define FPI 4
#define FD1 0
#define FD2 2
#define CPF 0
#define CPW 1
#define CR2 1
#define CRS 1
#define E2P 0
#define ESP 0
#define WQNT 0
#define WQBITS ""
#define SMASK 0xFFFFFFFu
#define NSTAMP 28
#define SDLY(n) do { _Pragma("unroll") for (int i_ = 0; i_ < (n) / 4; ++i_) __builtin_amdgcn_s_sleep(16); if ((n) % 4) __builtin_amdgcn_s_sleep(4 * ((n) % 4)); } while (0)
#define PIN() __builtin_amdgcn_sched_barrier(0)

namespace {
constexpr int H = 16, SK = 4096, D = 256, M = 448, DC = 1024, F = 1792;
constexpr int NCU = 256;
constexpr int NKG = 32, KPG = 128;
constexpr int NC5 = 56;

constexpr size_t FT_OFF = 0;
constexpr size_t QABS_OFF = 65536;
constexpr size_t ML_OFF = QABS_OFF + 65536;
constexpr size_t SP_OFF = ML_OFF + 32768;
constexpr size_t PCTX_OFF = SP_OFF + (size_t)256 * 4096;
constexpr size_t G5_OFF = PCTX_OFF + (size_t)2 * NKG * 16 * 1024 * 2;
constexpr size_t G5_BYTES = (size_t)56 * 256 * 16 * 4;
constexpr size_t FILL_BYTES = G5_OFF + G5_BYTES;
constexpr size_t QX_OFF = FILL_BYTES;
constexpr size_t HX_OFF = QX_OFF + 131072;
constexpr size_t STAMP_OFF = HX_OFF + 7 * 2 * F * 4;
constexpr size_t K1ST_OFF = STAMP_OFF + 256 * NSTAMP * 8;
constexpr size_t WS_NEED = K1ST_OFF + 512 * 2 * 8;

constexpr int RS2 = 1040;
constexpr int L_CKV = 0;
constexpr int L_WK = 64 * RS2;
constexpr int L_WV = L_WK;
constexpr int L_WO = L_WK + 32 * RS2;
constexpr int L_WO2 = L_CKV;
constexpr int L_PG = L_CKV + 7 * 4096;
constexpr int LDS_DMA_BYTES = L_WO + 7 * 5120;
static_assert(L_PG + 8 * 4096 <= L_WK, "lds");
constexpr int LDS_P_BYTES = 8448;
constexpr int PM_OFF = 16 * 272;
constexpr int L_QF = 0;
constexpr int L_CB = L_QF;
constexpr int L_SQ = L_QF + 8704;
constexpr int L_HM = L_SQ + 512;
constexpr int L_FL = L_HM + 64;
constexpr int L_QC = L_FL + 128;
constexpr int L_QR = L_QC + 64;
constexpr int L_CC = L_QR + 2048;
constexpr int LDS_SC_BYTES = L_CC + 512;
static_assert(LDS_DMA_BYTES + LDS_SC_BYTES + LDS_P_BYTES <= 160 * 1024, "lds");

constexpr unsigned kSpinCap = 1u << 17;
constexpr unsigned SENT = 0xFFFFFFFFu;

__device__ __forceinline__ float bf2f(unsigned short u) { return __uint_as_float(((unsigned)u) << 16); }
__device__ __forceinline__ float bflo(unsigned u) { return __uint_as_float(u << 16); }
__device__ __forceinline__ float bfhi(unsigned u) { return __uint_as_float(u & 0xffff0000u); }
__device__ __forceinline__ unsigned f2bf(float f) { unsigned u = __float_as_uint(f); return (u + 0x7fffu + ((u >> 16) & 1u)) >> 16; }
__device__ __forceinline__ unsigned pack2(float a, float b) { const f32x2 v = {a, b}; return __builtin_bit_cast(unsigned, __builtin_convertvector(v, bf16x2_t)); }
typedef _Float16 f16x2_t __attribute__((ext_vector_type(2)));
__device__ __forceinline__ unsigned packh(float a, float b) {
    const f32x2 v = {__builtin_amdgcn_fmed3f(a, -65504.f, 65504.f), __builtin_amdgcn_fmed3f(b, -65504.f, 65504.f)};
    const unsigned u = __builtin_bit_cast(unsigned, __builtin_convertvector(v, f16x2_t));
    return u < 0xFFFFFFFEu ? u : 0xFFFFFFFEu;
}
__device__ __forceinline__ float unh(unsigned u, int i) { return (float)__builtin_bit_cast(f16x2_t, u)[i]; }
template <int C> __device__ __forceinline__ float dppf(float v) { return __int_as_float(__builtin_amdgcn_update_dpp(0, __float_as_int(v), C, 0xF, 0xF, true)); }
__device__ __forceinline__ float add16(float v) { auto r = __builtin_amdgcn_permlane16_swap(__float_as_uint(v), __float_as_uint(v), false, false); return __uint_as_float(r[0]) + __uint_as_float(r[1]); }
__device__ __forceinline__ float add32(float v) { auto r = __builtin_amdgcn_permlane32_swap(__float_as_uint(v), __float_as_uint(v), false, false); return __uint_as_float(r[0]) + __uint_as_float(r[1]); }
__device__ __forceinline__ float max16(float v) { auto r = __builtin_amdgcn_permlane16_swap(__float_as_uint(v), __float_as_uint(v), false, false); return fmaxf(__uint_as_float(r[0]), __uint_as_float(r[1])); }
__device__ __forceinline__ float max32(float v) { auto r = __builtin_amdgcn_permlane32_swap(__float_as_uint(v), __float_as_uint(v), false, false); return fmaxf(__uint_as_float(r[0]), __uint_as_float(r[1])); }
__device__ __forceinline__ float add_hi3(float v) { v += dppf<0x128>(v); v = add16(v); return add32(v); }
__device__ __forceinline__ float max_hi3(float v) { v = fmaxf(v, dppf<0x128>(v)); v = max16(v); return max32(v); }
__device__ __forceinline__ float add_lo5(float v) { v += dppf<0xB1>(v); v += dppf<0x4E>(v); v += dppf<0x141>(v); v += dppf<0x140>(v); return add16(v); }
__device__ __forceinline__ float max_lo5(float v) { v = fmaxf(v, dppf<0xB1>(v)); v = fmaxf(v, dppf<0x4E>(v)); v = fmaxf(v, dppf<0x141>(v)); v = fmaxf(v, dppf<0x140>(v)); return max16(v); }
__device__ __forceinline__ float wsum(float v) { return add32(add_lo5(v)); }
__device__ __forceinline__ unsigned long long rt() { return __builtin_amdgcn_s_memrealtime(); }

#define BAR() __builtin_amdgcn_s_barrier()
#define LGKM0() asm volatile("s_waitcnt lgkmcnt(0)" ::: "memory")
#define VM0() asm volatile("s_waitcnt vmcnt(0)" ::: "memory")
#define MFMA(a, b, c) __builtin_amdgcn_mfma_f32_16x16x32_bf16(__builtin_bit_cast(bf16x8, a), __builtin_bit_cast(bf16x8, b), c, 0, 0, 0)

__device__ __forceinline__ bool spin_fail(unsigned& spins) {
    __builtin_amdgcn_s_sleep(1);
    return ++spins > kSpinCap;
}
__device__ __forceinline__ unsigned umax(unsigned a, unsigned b) { return a > b ? a : b; }
__device__ __forceinline__ unsigned smax(v4u v) { return umax(umax(umax(v[0], v[1]), v[2]), v[3]); }
__device__ __forceinline__ bool has_sent(v4u v) { return smax(v) == SENT; }
__device__ __forceinline__ unsigned canon(unsigned u) { return u < 0xFFFFFFFEu ? u : 0xFFFFFFFEu; }
__device__ __forceinline__ unsigned canonb(unsigned u) { return u < 0xFFFFFFFEu ? u : 0xFFFFFFFEu; }

#define GLDS_NT(src, dst) __builtin_amdgcn_global_load_lds((const unsigned*)(src), (LAS unsigned*)(dst), 16, 0, 2)
#define OAUX 0
#define GLDS_O(src, dst) __builtin_amdgcn_global_load_lds((const unsigned*)(src), (LAS unsigned*)(dst), 16, 0, OAUX)
#define HPB 0
#define HPD 8
#define NSW 1
#define HOS 0
#define HOD 0
#define HOE 0
#define HED 0
#define CH 0
#define CHD 0
#define PACE2 6
#define MLL 1
#define MLB 1
#define MLD 0
#define MLE 0
#define HOT 4
#define SWV 0
#define STAMP(i) do {} while (0)
#define STAMPW(i, w) do {} while (0)

constexpr int dig(int v, int i) { return i == 0 ? v % 10 : dig(v / 10, i - 1); }
constexpr int rank_of(int p) { return p == 1 ? 0 : (p == 9 ? 1 : (p == 8 ? 5 : (p >= 5 ? p + 1 : p))); }
constexpr int cnt_u(int p) { int n = 0; for (int i = 0; i < 4; ++i) n += dig(PU, i) == p; return n; }
constexpr int cnt_o(int p) { int n = 0; for (int i = 0; i < 9; ++i) n += dig(PO, i) == p; return n; }
constexpr int last_u() { int l = 0; for (int i = 0; i < 4; ++i) if (rank_of(dig(PU, i)) > l) l = rank_of(dig(PU, i)); return l; }
constexpr int o_after_u(int p) {
    int n = 0; for (int i = 0; i < 9; ++i) n += (rank_of(dig(PO, i)) >= last_u() && rank_of(dig(PO, i)) <= rank_of(p)); return n; }
#define OE 1
constexpr bool OEARLY = OE && cnt_o(6) + cnt_o(7) == 0;
static_assert(last_u() <= rank_of(5), "w_uv");
static_assert(rank_of(dig(PO, 5)) >= 5 && rank_of(dig(PO, 6)) >= 5 && rank_of(dig(PO, 7)) >= 5 && rank_of(dig(PO, 8)) >= 5, "w_o late part");
#define BULK(p) do { \
        _Pragma("unroll") for (int i = 0; i < 4; ++i) if (dig(PU, i) == (p)) \
            GLDS_NT(wuv + ((size_t)(256 * cq + 32 * wave + 8 * i + (lane >> 3)) * H + th) * D + 64 * dq + (((lane & 7) ^ ((lane >> 3) & 2) ^ ((i & 1) << 2)) * 8), L + L_WV + wave * 4096 + i * 1024); \
        if (cnt_o(p) && wave < 7) { \
            _Pragma("unroll") for (int i = 0; i < 9; ++i) if (dig(PO, i) == (p)) { \
                const int row = lane >> 3; \
                const unsigned short* sp = i > 0 ? wo + ((size_t)th * D + 64 * dq + 8 * ((i - 1 + ROT * cq) & 7) + row) * M : w2 + ((size_t)ug * 7 + (row < 7 ? row : 6)) * M; \
                GLDS_O(sp + 64 * wave + (((lane & 7) ^ (row & 2) ^ (i > 0 ? ((i - 1) & 1) << 2 : 0)) * 8), L + (i < 5 ? L_WO + wave * 5120 + i * 1024 : L_WO2 + wave * 4096 + (i - 5) * 1024)); \
            } \
        } \
        PIN(); \
    } while (0)
#define VMW2(a, b) do { if ((b) > 0 && wave == 7) VMW(a); else VMW((a) + (b)); } while (0)
#define VMW(n) asm volatile("s_waitcnt vmcnt(%0)" :: "n"(n) : "memory")
#define XB 0
#define XBITS "sc1"
#define XLD4(dst, off) asm volatile("buffer_load_dwordx4 %0, %1, %2, 0 offen " XBITS : "=v"(dst) : "v"(off), "s"(rs_ws) : "memory")
#define XLD4R(dst, off) asm volatile("buffer_load_dwordx4 %0, %1, %2, 0 offen " XBITS : "+v"(dst) : "v"(off), "s"(rs_ws) : "memory")
#define XLD2(dst, off) asm volatile("buffer_load_dwordx2 %0, %1, %2, 0 offen sc1" : "=v"(dst) : "v"(off), "s"(rs_ws) : "memory")
#define XLD2R(dst, off) asm volatile("buffer_load_dwordx2 %0, %1, %2, 0 offen sc1" : "+v"(dst) : "v"(off), "s"(rs_ws) : "memory")
#define XLD1(dst, off) asm volatile("buffer_load_dword %0, %1, %2, 0 offen sc1" : "=v"(dst) : "v"(off), "s"(rs_ws) : "memory")
#define XLDB(dst, off) asm volatile("buffer_load_ubyte %0, %1, %2, 0 offen sc1" : "=v"(dst) : "v"(off), "s"(rs_ws) : "memory")
#define GLDS_D(src, dst) __builtin_amdgcn_global_load_lds((const unsigned*)(src), (LAS unsigned*)(dst), 16, 0, 0)
#define XLDH(dst, off) asm volatile("buffer_load_ushort %0, %1, %2, 0 offen sc1" : "=v"(dst) : "v"(off), "s"(rs_ws) : "memory")
#define GLDS_X(src, dst) __builtin_amdgcn_global_load_lds((const unsigned*)(src), (LAS unsigned*)(dst), 16, 0, 16)
__device__ __forceinline__ bool tag_new(v4u w, v4u s) { return ((w[0] ^ s[0]) & 255u) != 0u && ((w[1] ^ s[1]) & 255u) != 0u && ((w[2] ^ s[2]) & 255u) != 0u && ((w[3] ^ s[3]) & 255u) != 0u; }
__device__ __forceinline__ unsigned tags4(v4u w) { return __builtin_amdgcn_perm(__builtin_amdgcn_perm(w[3], w[2], 0x04000400u), __builtin_amdgcn_perm(w[1], w[0], 0x04000400u), 0x05040100u); }
__device__ __forceinline__ v4u lds_rd4(const volatile LAS unsigned* p) {
    v4u r;
    asm volatile("ds_read_b128 %0, %1\n s_waitcnt lgkmcnt(0)" : "=v"(r) : "v"((unsigned)(size_t)p) : "memory");
    return r;
}
__device__ __forceinline__ void lds_wr(volatile LAS unsigned* p, unsigned v) { asm volatile("ds_write_b32 %0, %1" :: "v"((unsigned)(size_t)p), "v"(v) : "memory"); }
#define RFL(v) ((unsigned)__builtin_amdgcn_readfirstlane((int)(v)))
__device__ __forceinline__ f32x4 ln_stats(v4u xa, v4u xb, int lane) {
    const bool ok = lane < 56;
    float v[2][8], s[2], ss[2];
#pragma unroll
    for (int j = 0; j < 4; ++j) { v[0][2 * j] = bflo(xa[j]); v[0][2 * j + 1] = bfhi(xa[j]); v[1][2 * j] = bflo(xb[j]); v[1][2 * j + 1] = bfhi(xb[j]); }
#pragma unroll
    for (int b = 0; b < 2; ++b) {
        s[b] = ((v[b][0] + v[b][1]) + (v[b][2] + v[b][3])) + ((v[b][4] + v[b][5]) + (v[b][6] + v[b][7]));
        s[b] = wsum(ok ? s[b] : 0.f) * (1.0f / M);
    }
#pragma unroll
    for (int b = 0; b < 2; ++b) {
        ss[b] = 0.f;
#pragma unroll
        for (int j = 0; j < 8; ++j) { v[b][j] -= s[b]; ss[b] += v[b][j] * v[b][j]; }
        ss[b] = wsum(ok ? ss[b] : 0.f);
    }
    return (f32x4){s[0], 1.0f / sqrtf(ss[0] * (1.0f / M) + 1e-5f), s[1], 1.0f / sqrtf(ss[1] * (1.0f / M) + 1e-5f)};
}
__device__ __forceinline__ void block_mma(const v4u* w, v4u xf, bool use, int lane, f32x4* aq) {
    v4u a0, a1;
#pragma unroll
    for (int i = 0; i < 4; ++i) {
        const unsigned v = use ? ((lane & 2) ? 0x3F803F80u : xf[i]) : 0u;
        a0[i] = (lane & 8) ? 0u : v;
        a1[i] = (lane & 8) ? v : 0u;
    }
#pragma unroll
    for (int c = 0; c < 4; ++c) {
        v4u fa, fb;
#pragma unroll
        for (int p = 0; p < 4; ++p) {
            fa[p] = __builtin_amdgcn_perm(w[2 * p + 1][c], w[2 * p][c], 0x05040100u);
            fb[p] = __builtin_amdgcn_perm(w[2 * p + 1][c], w[2 * p][c], 0x07060302u);
        }
        aq[(2 * c) & 3] = MFMA(c < 2 ? a0 : a1, fa, aq[(2 * c) & 3]);
        aq[(2 * c + 1) & 3] = MFMA(c < 2 ? a0 : a1, fb, aq[(2 * c + 1) & 3]);
    }
}
__device__ __forceinline__ f32x4 block_out(const f32x4* aq, f32x4 st, float sc, int lane) {
    const float rs = ((lane & 16) ? st[3] : st[1]) * sc;
    f32x4 f;
#pragma unroll
    for (int k = 0; k < 4; ++k) {
        const int t0 = __float_as_int(aq[k][0] - st[0] * aq[k][2]), t1 = __float_as_int(aq[k][1] - st[2] * aq[k][2]);
        const int ua = __builtin_amdgcn_update_dpp(t0, t0, 0x128, 0xA, 0xF, false);
        const int ub = __builtin_amdgcn_update_dpp(t1, t1, 0x128, 0x5, 0xF, false);
        const auto r = __builtin_amdgcn_permlane16_swap((unsigned)ua, (unsigned)ub, false, false);
        f[k] = (__uint_as_float(r[0]) + __uint_as_float(r[1])) * rs;
    }
    return f;
}
#define TIE8(w) "+v"((w)[0]), "+v"((w)[1]), "+v"((w)[2]), "+v"((w)[3]), "+v"((w)[4]), "+v"((w)[5]), "+v"((w)[6]), "+v"((w)[7])
#define XLD4S(dst, off, so) asm volatile("buffer_load_dwordx4 %0, %1, %2, %3 offen sc1" : "=v"(dst) : "v"(off), "s"(rs_ws), "s"(so) : "memory")
#define XLD4SR(dst, off, so) asm volatile("buffer_load_dwordx4 %0, %1, %2, %3 offen sc1" : "+v"(dst) : "v"(off), "s"(rs_ws), "s"(so) : "memory")

__global__ void __launch_bounds__(768, 1) k2(const unsigned short* __restrict__ x, const unsigned short* __restrict__ ckv,
                                             const unsigned short* __restrict__ wq, const unsigned short* __restrict__ w1,
                                             const unsigned short* __restrict__ wuk, const unsigned short* __restrict__ wuv,
                                             const unsigned short* __restrict__ wo, const unsigned short* __restrict__ b1,
                                             const unsigned short* __restrict__ w2, const unsigned short* __restrict__ b2,
                                             unsigned char* __restrict__ ws, unsigned short* __restrict__ out) {
    __shared__ __attribute__((aligned(16))) unsigned char lds_dma[LDS_DMA_BYTES];
    __shared__ __attribute__((aligned(16))) unsigned char lds_sc[LDS_SC_BYTES];
    LAS unsigned char* L = (LAS unsigned char*)lds_dma;
    __shared__ __attribute__((aligned(16))) unsigned char lds_p[LDS_P_BYTES];
    LAS unsigned char* S = (LAS unsigned char*)lds_sc;
    LAS unsigned char* P = (LAS unsigned char*)lds_p;
    const int tid = threadIdx.x, wave = __builtin_amdgcn_readfirstlane(tid >> 6), lane = tid & 63;
    const int cu = blockIdx.x;
    const int bb = cu >> 7, kg = (cu >> 2) & 31, cg = cu & 3;
    const int th = (cu & 7) * 2 + (cu >> 7), dq = (cu >> 5) & 3, cq = (cu >> 3) & 3;
    const int hh = th, cs = (cu >> 3) & 15;
    const int ug = (cu & 7) * 32 + (cu >> 3);
    const int g4 = lane >> 4;
    const __amdgpu_buffer_rsrc_t rs_ws = __builtin_amdgcn_make_buffer_rsrc(ws, 0, (int)WS_NEED, 0x00020000);
    volatile LAS unsigned* fl = (volatile LAS unsigned*)(S + L_FL);
    STAMP(0);

#define QXO(j) ((int)QX_OFF + ((th * 4 + cq) * 2 + ((j) >> 4)) * 1024 + dq * 256 + ((j) & 15) * 16)
#define QLINE(b) ((int)QABS_OFF + (((b) * 4 + (cs >> 2)) * 16 + hh) * 512 + (cs & 3) * 128)
#define RESEND(sp) do { if (((sp) & (RSI - 1)) == RSI - 1) { \
        const unsigned k_ = (sp) / RSI + 1u; \
        if (fl[3] != 0u && lane < 32) { \
            v4u w_ = *(const LAS v4u*)(S + L_SQ + lane * 16); \
            _Pragma("unroll") for (int i_ = 0; i_ < 4; ++i_) w_[i_] = (w_[i_] & ~255u) | ((w_[i_] + k_) & 255u); \
            __builtin_amdgcn_raw_buffer_store_b128(w_, rs_ws, QXO(lane), 0, 16); \
        } \
        if (fl[0] != 0u && lane < FR) \
            __builtin_amdgcn_raw_buffer_store_b32(*(const LAS unsigned*)(S + L_QC) + k_, rs_ws, (int)FT_OFF + lane * 1024 + cu * 4, 0, 16); \
    } } while (0)
    if (wave == 8) {
        const int ib = (int)QX_OFF + th * 8192 + lane * 16;
        v4u q[8];
        unsigned tg[8];
#pragma unroll
        for (int j = 0; j < 8; ++j) XLD4S(q[j], ib, j * 1024);
        BAR();
        asm volatile("s_waitcnt vmcnt(0)" : TIE8(q) :: "memory");
        STAMPW(23, 8);
#pragma unroll
        for (int j = 0; j < 8; ++j) tg[j] = tags4(q[j]);
        for (unsigned spins = 0; RFL(fl[3]) == 0u;) if (spin_fail(spins)) break;
        SDLY(QD);
        const unsigned qr = (unsigned)(size_t)(P + lane * 16);
#pragma unroll
        for (int j = 0; j < 8; ++j) GLDS_X(ws + ib + j * 1024, P + j * 1024);
        SDLY(QD2);
#pragma unroll
        for (int j = 0; j < 8; ++j) GLDS_X(ws + ib + j * 1024, P + j * 1024);
        for (unsigned spins = 0;;) {
            VMW(8);
#pragma unroll
            for (int j = 0; j < 8; ++j) asm volatile("ds_read_b128 %0, %1 offset:%2" : "=v"(q[j]) : "v"(qr), "n"(j * 1024) : "memory");
            asm volatile("s_waitcnt lgkmcnt(0)" : TIE8(q) :: "memory");
            unsigned old = 0u;
#pragma unroll
            for (int j = 0; j < 8; ++j) { const unsigned x_ = tags4(q[j]) ^ tg[j]; old |= (((x_ - 0x01010101u) & ~x_ & 0x80808080u) != 0u ? 1u : 0u) << j; }
            if (!__any(old != 0u)) break;
            if (spin_fail(spins)) break;
            RESEND(spins);
            SDLY(PI);
            if (lane == 0) old = 255u;
#pragma unroll
            for (int j = 0; j < 8; ++j) if ((old >> j) & 1u) GLDS_X(ws + ib + j * 1024, P + j * 1024);
            PIN();
        }
#pragma unroll
        for (int b = 0; b < 2; ++b) {
            float f[4];
#pragma unroll
            for (int r = 0; r < 4; ++r)
                f[r] = (__uint_as_float(q[b][r] & 0xFFFFFF00u) + __uint_as_float(q[2 + b][r] & 0xFFFFFF00u)) + (__uint_as_float(q[4 + b][r] & 0xFFFFFF00u) + __uint_as_float(q[6 + b][r] & 0xFFFFFF00u));
            const unsigned h0 = pack2(f[0], f[1]), h1 = pack2(f[2], f[3]);
            const unsigned l0 = pack2(f[0] - bflo(h0), f[1] - bfhi(h0)), l1 = pack2(f[2] - bflo(h1), f[3] - bfhi(h1));
            asm volatile("ds_write_b64 %0, %1\n ds_write_b64 %0, %2 offset:512" :: "v"((unsigned)(size_t)(S + L_QR + b * 1024 + lane * 8)), "v"((v2u){h0, h1}), "v"((v2u){l0, l1}) : "memory");
        }
        lds_wr(fl + 1, 1u);
        STAMPW(1, 8);
        VM0();
        lds_wr(fl + 16, 1u);
        return;
    }
    if (wave == 9) {
        const v4u sn = {SENT, SENT, SENT, SENT};
        const int fo = (int)FT_OFF + (cu & (FR - 1)) * 1024 + lane * 16;
        v4u f0, f1;
        unsigned fw;
        XLD4(f0, fo);
        XLD1(fw, (int)FT_OFF + (cu & (FR - 1)) * 1024 + cu * 4);
        SDLY(FD1);
        if (lane < 16) __builtin_amdgcn_raw_buffer_store_b128(sn, rs_ws, QLINE(lane >> 3) + (lane & 7) * 16, 0, 16);
#pragma unroll
        for (int i = 0; i < 4; ++i) __builtin_amdgcn_raw_buffer_store_b128(sn, rs_ws, (int)SP_OFF + cu * 4096 + i * 1024 + lane * 16, 0, 16);
        if (lane < 8) __builtin_amdgcn_raw_buffer_store_b128(sn, rs_ws, (int)ML_OFF + cu * 128 + lane * 16, 0, 16);
        BAR();
        asm volatile("s_waitcnt vmcnt(0)" : "+v"(f0), "+v"(fw) :: "memory");
        SDLY(FWD);
        if (lane < FR) __builtin_amdgcn_raw_buffer_store_b32(fw + 1u, rs_ws, (int)FT_OFF + lane * 1024 + cu * 4, 0, 16);
        lds_wr((volatile LAS unsigned*)(S + L_QC), fw + 1u);
        lds_wr(fl, 1u);
        STAMPW(20, 9);
        SDLY(FPD);
        f1 = f0;
        for (unsigned spins = 0;;) {
            v4u f2 = f1;
            if (f1[0] == f0[0] || f1[1] == f0[1] || f1[2] == f0[2] || f1[3] == f0[3]) XLD4R(f2, fo);
            asm volatile("s_waitcnt vmcnt(0)" : "+v"(f2) :: "memory");
#pragma unroll
            for (int i = 0; i < 4; ++i) f1[i] = f1[i] != f0[i] ? f1[i] : f2[i];
            if (!__any(f1[0] == f0[0] || f1[1] == f0[1] || f1[2] == f0[2] || f1[3] == f0[3])) break;
            if (spin_fail(spins)) break;
            RESEND(spins);
            SDLY(FPI);
        }
        lds_wr(fl + 17, 1u);
        STAMPW(26, 9);
        return;
    }
    if (wave == 11) {
        const v4u sn = {SENT, SENT, SENT, SENT};
        const int tk = 255 - cu < 196 ? 255 - cu : 0, hs = tk / 7, rb = tk - 7 * hs;
        const int ho = (int)HX_OFF + ((rb * 2 + (g4 & 1)) * F + 64 * hs + 8 * (lane & 7) + 4 * (g4 >> 1)) * 4;
        const bool hw = 255 - cu < 196 && ((lane >> 3) & 1) == (g4 & 1);
        BAR();
        SDLY(FD2);
#pragma unroll
        for (int i = 0; i < 8; ++i) __builtin_amdgcn_raw_buffer_store_b128(sn, rs_ws, (int)PCTX_OFF + (((2 * i + (lane >> 5)) * 4 + cg) * 64 + bb * NKG + kg) * 512 + (lane & 31) * 16, 0, 16);
#if !(FLN & 1)
        if (lane < 16) __builtin_amdgcn_raw_buffer_store_b64((v2u){SENT, SENT}, rs_ws, (int)ML_OFF + (((cg * 2 + bb) * 16 + lane) * 32 + kg) * 8, 0, 16);
#endif
#if !(FLN & 2)
#pragma unroll
        for (int i = 0; i < 4; ++i)
            if (i < 3 || lane < 32) __builtin_amdgcn_raw_buffer_store_b128(sn, rs_ws, (int)G5_OFF + ((16 * i + (lane >> 2)) * 256 + cu) * 64 + (lane & 3) * 16, 0, 16);
#endif
        if (hw) __builtin_amdgcn_raw_buffer_store_b128(sn, rs_ws, ho, 0, 16);
        VM0();
        for (unsigned spins = 0; RFL(fl[0]) == 0u;) if (spin_fail(spins)) break;
        lds_wr(fl + 2, 1u);
        STAMPW(21, 11);
#define HBULK(n0, n1) do { \
            const int row = lane >> 3; \
            int n_ = 0; \
            _Pragma("unroll") for (int i = 0; i < 4; ++i) if (dig(PU, i) == 0) { \
                _Pragma("unroll") for (int w = 0; w < 8; ++w) { \
                    if (n_ >= (n0) && n_ < (n1)) { \
                        GLDS_NT(wuv + ((size_t)(256 * cq + 32 * w + 8 * i + row) * H + th) * D + 64 * dq + (((lane & 7) ^ (row & 2) ^ ((i & 1) << 2)) * 8), L + L_WV + w * 4096 + i * 1024); \
                        if (HOT) __builtin_amdgcn_s_sleep(HOT); \
                        PIN(); \
                    } \
                    ++n_; \
                } \
            } \
            _Pragma("unroll") for (int i = 0; i < 5; ++i) if (dig(PO, i) == 0) { \
                const unsigned short* sp = i > 0 ? wo + ((size_t)th * D + 64 * dq + 8 * ((i - 1 + ROT * cq) & 7) + row) * M : w2 + ((size_t)ug * 7 + (row < 7 ? row : 6)) * M; \
                _Pragma("unroll") for (int w = 0; w < 7; ++w) { \
                    if (n_ >= (n0) && n_ < (n1)) { \
                        GLDS_O(sp + 64 * w + (((lane & 7) ^ (row & 2) ^ (i > 0 ? ((i - 1) & 1) << 2 : 0)) * 8), L + L_WO + w * 5120 + i * 1024); \
                        if (HOT) __builtin_amdgcn_s_sleep(HOT); \
                        PIN(); \
                    } \
                    ++n_; \
                } \
            } \
        } while (0)
        if (cnt_u(0) > 0 && HOE > 0) for (unsigned spins = 0;;) { const v4u f = lds_rd4(fl + 12); if (RFL(f[0] & f[1] & f[2] & f[3]) != 0u) break; if (spin_fail(spins)) break; }
        if (255 - cu < 196) {
            v4u xa, xb2, xf, wb[8];
            for (unsigned spins = 0; RFL(fl[HW]) == 0u;) if (spin_fail(spins)) break;
            SDLY(HD);
            asm volatile("global_load_dwordx4 %0, %1, off" : "=v"(xa) : "v"(x + (lane < 56 ? lane : 55) * 8) : "memory");
            asm volatile("global_load_dwordx4 %0, %1, off" : "=v"(xb2) : "v"(x + M + (lane < 56 ? lane : 55) * 8) : "memory");
            asm volatile("global_load_dwordx4 %0, %1, off" : "=v"(xf) : "v"(x + (lane & 1) * M + 64 * rb + 8 * (lane & 4) + 8 * g4) : "memory");
            const unsigned short* sp = w1 + (size_t)(64 * rb + 4 * (lane & 8) + 8 * g4) * F + 64 * hs + 8 * (lane & 7);
#pragma unroll
            for (int i = 0; i < 8; ++i) asm volatile("global_load_dwordx4 %0, %1, off nt" : "=v"(wb[i]) : "v"(sp + i * F) : "memory");
            asm volatile("s_waitcnt vmcnt(0)" : "+v"(xa), "+v"(xb2), "+v"(xf), TIE8(wb) :: "memory");
            const f32x4 st = ln_stats(xa, xb2, lane);
            f32x4 aq[4];
#pragma unroll
            for (int k = 0; k < 4; ++k) aq[k] = (f32x4){0.f, 0.f, 0.f, 0.f};
            block_mma(wb, xf, true, lane, aq);
            const f32x4 f = block_out(aq, st, 1.0f, lane);
            const v4u v = {canon(__float_as_uint(f[0])), canon(__float_as_uint(f[1])), canon(__float_as_uint(f[2])), canon(__float_as_uint(f[3]))};
            if (hw) __builtin_amdgcn_raw_buffer_store_b128(v, rs_ws, ho, 0, 16);
            STAMPW(24, 11);
        }
        const int hc = ug * 7 + ((lane & 7) < 7 ? (lane & 7) : 6), hi_ = (int)HX_OFF + ((lane < 56 ? lane >> 3 : 6) * 2 * F + hc) * 4;
        unsigned hp0, hp1, hb1;
        asm volatile("global_load_ushort %0, %1, off" : "=v"(hb1) : "v"(b1 + hc) : "memory");
        if (cnt_u(0) > 0) for (unsigned spins = 0;;) { const v4u f = lds_rd4(fl + 12); if (RFL(f[0] & f[1] & f[2] & f[3]) != 0u) break; if (spin_fail(spins)) break; }
        HBULK(HOE, HOS > HOE ? HOS : HOE);
        BAR();
        SDLY(HOD);
        HBULK(HOS > HOE ? HOS : HOE, 64);
        SDLY(HPD);
        XLD1(hp0, hi_);
        XLD1(hp1, hi_ + F * 4);
        BAR();
        BAR();
        asm volatile("s_waitcnt vmcnt(0)" : "+v"(hp0), "+v"(hp1), "+v"(hb1) :: "memory");
        for (unsigned spins = 0; __any(umax(hp0, hp1) == SENT);) {
            if (spin_fail(spins)) break;
            XLD1(hp0, hi_);
            XLD1(hp1, hi_ + F * 4);
            asm volatile("s_waitcnt vmcnt(0)" : "+v"(hp0), "+v"(hp1) :: "memory");
        }
        const float h0 = add_hi3(lane < 56 ? __uint_as_float(hp0) : 0.f), h1 = add_hi3(lane < 56 ? __uint_as_float(hp1) : 0.f);
        const float hv = bf2f((unsigned short)hb1) + ((lane & 8) ? h1 : h0);
        const float t = tanhf(0.7978845608028654f * (hv + 0.044715f * hv * hv * hv));
        const float gl = (lane & 7) < 7 ? 0.5f * hv * (1.0f + t) : 0.f;
        const unsigned hi = f2bf(gl), lo = f2bf(gl - __uint_as_float(hi << 16));
        if (lane < 16) {
            const unsigned ha = (unsigned)(size_t)(S + L_HM + (lane & 8) * 4 + (lane & 7) * 2);
            asm volatile("ds_write_b16 %0, %1\n ds_write_b16 %0, %2 offset:16" :: "v"(ha), "v"(hi), "v"(lo) : "memory");
        }
        const int mo = (int)ML_OFF + ((lane >> 5) * 128 + (lane & 31) * 4 + cq) * 128 + th * 8;
        v2u ml;
        LGKM0();
        STAMPW(16, 11);
        BAR();
        SDLY(MLD);
        XLD2(ml, mo);
        asm volatile("s_waitcnt vmcnt(0)" : "+v"(ml) :: "memory");
        for (unsigned spins = 0; __any(umax(ml[0], ml[1]) == SENT);) {
            if (spin_fail(spins)) break;
            if (umax(ml[0], ml[1]) == SENT) XLD2R(ml, mo);
            asm volatile("s_waitcnt vmcnt(0)" : "+v"(ml) :: "memory");
        }
        const float ew = __builtin_amdgcn_exp2f(__uint_as_float(ml[0]) - max_lo5(__uint_as_float(ml[0])));
        const float ls = add_lo5(ew * __uint_as_float(ml[1]));
        lds_wr((volatile LAS unsigned*)(S + L_CC) + lane, canon(__float_as_uint(ew * (1.0f / ls))));
        LGKM0();
        return;
    }
    if (wave == 10) {
        v4u og, xa, xb2, xf0, xf1, wa[8], wb[8];
        const int pj = (g4 & 1) * 16 + 2 * (lane & 7) + (g4 >> 1);
        const bool ua = (lane & 36) != 36, ub = (lane & 40) != 40;
        asm volatile("global_load_dwordx4 %0, %1, off" : "=v"(xa) : "v"(x + (lane < 56 ? lane : 55) * 8) : "memory");
        asm volatile("global_load_dwordx4 %0, %1, off" : "=v"(xb2) : "v"(x + M + (lane < 56 ? lane : 55) * 8) : "memory");
        {
            const unsigned short* xs = x + (lane & 1) * M + 112 * cq + 8 * (lane & 4) + 8 * g4;
            asm volatile("global_load_dwordx4 %0, %1, off" : "=v"(xf0) : "v"(xs) : "memory");
            asm volatile("global_load_dwordx4 %0, %1, off" : "=v"(xf1) : "v"(xs + (ua ? 64 : 48)) : "memory");
            const __amdgpu_buffer_rsrc_t rs_wq = __builtin_amdgcn_make_buffer_rsrc((void*)wq, 0, M * H * D * 2, 0x00020000);
            const int wvo = ((112 * cq + 4 * (lane & 8) + 8 * g4) * (H * D) + th * D + 64 * dq + 8 * (lane & 7)) * 2;
            const int wv1 = wvo + (ub ? 64 : 48) * (H * D * 2);
#pragma unroll
            for (int i = 0; i < 8; ++i) asm volatile("buffer_load_dwordx4 %0, %1, %2, %3 offen" WQBITS : "=v"(wa[i]) : "v"(wvo), "s"(rs_wq), "s"(i * 8192) : "memory");
#pragma unroll
            for (int i = 0; i < 8; ++i) asm volatile("buffer_load_dwordx4 %0, %1, %2, %3 offen" WQBITS : "=v"(wb[i]) : "v"(wv1), "s"(rs_wq), "s"(i * 8192) : "memory");
        }
        XLD4(og, QXO(pj));
        PIN();
        BAR();
        asm volatile("s_waitcnt vmcnt(17)" : "+v"(xa), "+v"(xb2), "+v"(xf0), "+v"(xf1) :: "memory");
        const f32x4 st = ln_stats(xa, xb2, lane);
        f32x4 aq[4];
#pragma unroll
        for (int k = 0; k < 4; ++k) aq[k] = (f32x4){0.f, 0.f, 0.f, 0.f};
        asm volatile("s_waitcnt vmcnt(9)" : TIE8(wa) :: "memory");
        block_mma(wa, xf0, true, lane, aq);
        asm volatile("s_waitcnt vmcnt(0)" : TIE8(wb), "+v"(og) :: "memory");
        STAMPW(18, 10);
        block_mma(wb, xf1, ua, lane, aq);
        const f32x4 qv = block_out(aq, st, 0.0625f * 1.4426950408889634f, lane);
        v4u pay;
#pragma unroll
        for (int r = 0; r < 4; ++r) pay[r] = ((__float_as_uint(qv[r]) + 128u) & 0xFFFFFF00u) | ((og[r] + 1u) & 255u);
        STAMPW(17, 10);
        PIN();
        if (((lane >> 3) & 1) == (g4 & 1)) {
            __builtin_amdgcn_raw_buffer_store_b128(pay, rs_ws, QXO(pj), 0, 16);
            asm volatile("ds_write_b128 %0, %1" :: "v"((unsigned)(size_t)(S + L_SQ + pj * 16)), "v"(pay) : "memory");
        }
        lds_wr(fl + 3, 1u);
        STAMPW(19, 10);
        return;
    }

    unsigned xr0, xr1;
    {
        const int b = (tid >> 3) & 1, m = (cu < NC5 ? cu : 0) * 8 + (tid & 7);
        asm volatile("global_load_ushort %0, %1, off" : "=v"(xr0) : "v"(x + b * M + m) : "memory");
        asm volatile("global_load_ushort %0, %1, off" : "=v"(xr1) : "v"(b2 + m) : "memory");
    }
    const int qoff = (int)QABS_OFF + ((bb * 4 + cg) * 8 + wave) * 1024 + lane * 16;
    v4u qa;
    if (tid < 32) fl[tid] = 0u;
    if (wave == 1) ((volatile LAS unsigned*)(S + L_CC))[lane] = SENT;
    LGKM0();
    BAR();
    PIN();
    if (wave < 4) {
        SDLY(D0);
#pragma unroll
        for (int i = 0; i < 8; ++i) {
            const int r = 2 * i + (lane >> 5);
            const int ck = (lane & 31) ^ ((r & 7) << 2);
            if (i >= PACEK) VMW(PACEK - 1);
            GLDS_NT(wuk + ((size_t)(64 * cs + 16 * wave + r) * H + hh) * D + 8 * ck, L + L_WK + (8 * wave + i) * RS2);
        }
    } else {
        for (unsigned spins = 0; RFL(fl[DW]) == 0u;) if (spin_fail(spins)) break;
        SDLY(D1);
        const unsigned short* src = ckv + ((size_t)bb * SK + (size_t)kg * KPG + 16 * (wave - 4)) * DC + 256 * cg;
#pragma unroll
        for (int j = 0; j < 16; ++j) {
            const int i = j & 7, tw = j >> 3;
            const int r = 2 * i + (lane >> 5);
            const int ck = (lane & 31) ^ ((r & 7) << 2);
            if (j >= PACEC) VMW(PACEC - 1);
            GLDS_NT(src + (size_t)(64 * tw + r) * DC + 8 * ck, L + L_CKV + (8 * (wave - 4 + 4 * tw) + i) * RS2);
        }
        STAMPW(22, 4);
    }
    PIN();
    if (wave < 4) {
        v4u av[8], wk[8];
        VM0();
        {
            const unsigned wa = (unsigned)(size_t)(L + L_WK + (8 * wave + ((lane & 15) >> 1)) * RS2 + (lane & 1) * 512 + g4 * 16);
#pragma unroll
            for (int ks = 0; ks < 8; ++ks) asm volatile("ds_read_b128 %0, %1" : "=v"(wk[ks]) : "v"(wa + ((ks ^ (lane & 7)) << 6)) : "memory");
            asm volatile("s_waitcnt lgkmcnt(0)" : "+v"(wk[0]), "+v"(wk[1]), "+v"(wk[2]), "+v"(wk[3]), "+v"(wk[4]), "+v"(wk[5]), "+v"(wk[6]), "+v"(wk[7]) :: "memory");
        }
        fl[12 + wave] = 1u;
        STAMP(27);
        for (unsigned spins = 0; RFL(fl[1]) == 0u;) if (spin_fail(spins)) break;
        asm volatile("" ::: "memory");
#pragma unroll
        for (int ks = 0; ks < 8; ++ks) av[ks] = *(const LAS v4u*)(S + L_QR + (lane & 3) * 512 + (32 * ks + 8 * g4) * 2);
        const unsigned f2 = fl[2];
        f32x4 acc0 = {0.f, 0.f, 0.f, 0.f}, acc1 = {0.f, 0.f, 0.f, 0.f};
#pragma unroll
        for (int ks = 0; ks < 8; ks += 2) { acc0 = MFMA(wk[ks], av[ks], acc0); acc1 = MFMA(wk[ks + 1], av[ks + 1], acc1); }
        acc0 += acc1;
#pragma unroll
        for (int r = 0; r < 4; ++r) acc0[r] += dppf<0xB1>(acc0[r]);
        const v2u v = {canonb(pack2(acc0[0], acc0[1])), canonb(pack2(acc0[2], acc0[3]))};
        if (RFL(f2) == 0u) for (unsigned spins = 0; RFL(fl[2]) == 0u;) if (spin_fail(spins)) break;
        PIN();
        if ((lane & 13) == 0) __builtin_amdgcn_raw_buffer_store_b64(v, rs_ws, QLINE((lane >> 1) & 1) + wave * 32 + g4 * 8, 0, 16);
        lds_wr(fl + 4 + wave, 1u);
        STAMP(2);
    }
    PIN();
    static_assert(PACEC <= 15, "pace");
    asm volatile("" : "+v"(xr0), "+v"(xr1));
    const float xb = bf2f((unsigned short)xr0) + bf2f((unsigned short)xr1);
    if (wave >= 4) for (unsigned spins = 0; RFL(fl[wave]) == 0u;) if (spin_fail(spins)) break;
    STAMPW(25, 4);
    if (wave < 4) SDLY(E2D); else SDLY(E2D2);
    for (unsigned spins = 0; RFL(fl[17]) == 0u;) if (spin_fail(spins)) break;
    PIN();
    XLD4(qa, qoff);
    if (wave >= 4) {
        asm volatile("s_waitcnt vmcnt(1)" ::: "memory");
        fl[4 + wave] = 1u;
    } else {
        for (unsigned spins = 0; RFL(fl[8 + wave]) == 0u;) if (spin_fail(spins)) break;
        asm volatile("" ::: "memory");
    }
    STAMPW(13, 7);
    v4u kf[8];
    {
        const unsigned ka = (unsigned)(size_t)(L + L_CKV + (8 * wave + ((lane & 15) >> 1)) * RS2 + (lane & 1) * 512 + g4 * 16);
#pragma unroll
        for (int ks = 0; ks < 8; ++ks) asm volatile("ds_read_b128 %0, %1" : "=v"(kf[ks]) : "v"(ka + ((ks ^ (lane & 7)) << 6)) : "memory");
    }
    if (cnt_u(1) + cnt_o(1) > 0) {
        for (unsigned spins = 0;;) { const v4u f = lds_rd4(fl + 12); if (RFL(f[0] & f[1] & f[2] & f[3]) != 0u) break; if (spin_fail(spins)) break; }
    }
    BULK(1);
    VMW2(cnt_u(1), cnt_o(1));
    asm volatile("" : "+v"(qa));
    for (unsigned spins = 0;;) {
        if (!__any(has_sent(qa))) break;
        if (spin_fail(spins)) break;
        RESEND(spins);
        SDLY(E2P);
        if (has_sent(qa)) XLD4R(qa, qoff);
        asm volatile("s_waitcnt vmcnt(0)" : "+v"(qa) :: "memory");
    }
    *(LAS v4u*)(S + L_QF + ((lane & 31) >> 2) * 1088 + (lane & 3) * 272 + (2 * wave + (lane >> 5)) * 16) = qa;
    asm volatile("s_waitcnt lgkmcnt(0)" : "+v"(kf[0]), "+v"(kf[1]), "+v"(kf[2]), "+v"(kf[3]), "+v"(kf[4]), "+v"(kf[5]), "+v"(kf[6]), "+v"(kf[7]) :: "memory");
    BAR();
    STAMPW(12, 7);
    BULK(9);
    v2u sown;
    {
        f32x4 acc0 = {0.f, 0.f, 0.f, 0.f}, acc1 = {0.f, 0.f, 0.f, 0.f};
#pragma unroll
        for (int ks = 0; ks < 8; ks += 2) {
            const v4u ah = *(const LAS v4u*)(S + L_QF + ks * 1088 + g4 * 272 + (lane & 15) * 16);
            const v4u al = *(const LAS v4u*)(S + L_QF + 1088 + ks * 1088 + g4 * 272 + (lane & 15) * 16);
            acc0 = MFMA(kf[ks], ah, acc0);
            acc1 = MFMA(kf[ks + 1], al, acc1);
        }
        acc0 += acc1;
        sown = (v2u){packh(acc0[0], acc0[1]), packh(acc0[2], acc0[3])};
        __builtin_amdgcn_raw_buffer_store_b64(sown, rs_ws, (int)SP_OFF + cu * 4096 + wave * 512 + lane * 8, 0, 16);
    }
    STAMP(3);
    PIN();
    BULK(2);
    v2u vf[16];
    {
        const int q = (lane & 15) >> 2, p = lane & 3;
#pragma unroll
        for (int u = 0; u < 2; ++u) {
            const unsigned va = (unsigned)(size_t)(L + L_CKV + (4 * g4 + 2 * u + (q >> 1)) * RS2 + (q & 1) * 512 + ((wave ^ (4 * u + q)) << 6) + 16 * p);
#pragma unroll
            for (int j = 0; j < 4; ++j)
#pragma unroll
                for (int t = 0; t < 2; ++t)
                    asm volatile("ds_read_b64_tr_b16 %0, %1 offset:%2" : "=v"(vf[(j * 2 + t) * 2 + u]) : "v"(va), "n"(j * 16 * RS2 + 8 * t) : "memory");
        }
    }
    SDLY(ESD);
    PIN();
    f32x4 sc;
    {
        v2u e[3];
        const int eb = (int)SP_OFF + (cu & ~3) * 4096 + wave * 512 + lane * 8;
        const int eo0 = eb + (cg < 1 ? 1 : 0) * 4096, eo1 = eb + (cg < 2 ? 2 : 1) * 4096, eo2 = eb + (cg < 3 ? 3 : 2) * 4096;
        XLD2(e[0], eo0); XLD2(e[1], eo1); XLD2(e[2], eo2);
        BULK(3);
        for (unsigned spins = 0; RFL(lds_rd4(fl + 16)[0]) == 0u;) if (spin_fail(spins)) break;
        VMW2(cnt_u(3), cnt_o(3));
        asm volatile("" : "+v"(e[0]), "+v"(e[1]), "+v"(e[2]));
        for (unsigned spins = 0;;) {
            if (!__any(umax(umax(umax(e[0][0], e[0][1]), umax(e[1][0], e[1][1])), umax(e[2][0], e[2][1])) == SENT)) break;
            if (spin_fail(spins)) break;
            RESEND(spins);
            SDLY(ESP);
            if (umax(e[0][0], e[0][1]) == SENT) XLD2R(e[0], eo0);
            if (umax(e[1][0], e[1][1]) == SENT) XLD2R(e[1], eo1);
            if (umax(e[2][0], e[2][1]) == SENT) XLD2R(e[2], eo2);
            asm volatile("s_waitcnt vmcnt(0)" : "+v"(e[0]), "+v"(e[1]), "+v"(e[2]) :: "memory");
        }
        STAMP(10);
        BULK(4);
        const bool lo = cg < 2;
#pragma unroll
        for (int r = 0; r < 4; ++r) {
            const float e0 = unh(e[0][r >> 1], r & 1), e1 = unh(e[1][r >> 1], r & 1), e2 = unh(e[2][r >> 1], r & 1), eo = unh(sown[r >> 1], r & 1);
            sc[r] = (e0 + (lo ? eo : e1)) + (e2 + (lo ? e1 : eo));
        }
        float mw = fmaxf(fmaxf(sc[0], sc[1]), fmaxf(sc[2], sc[3]));
        mw = max16(mw); mw = max32(mw);
        if (lane < 16) *(LAS float*)(P + PM_OFF + lane * 48 + wave * 4) = mw;
    }
    asm volatile("s_waitcnt lgkmcnt(0)" : "+v"(vf[0]), "+v"(vf[1]), "+v"(vf[2]), "+v"(vf[3]), "+v"(vf[4]), "+v"(vf[5]), "+v"(vf[6]), "+v"(vf[7]) :: "memory");
    asm volatile("" : "+v"(vf[8]), "+v"(vf[9]), "+v"(vf[10]), "+v"(vf[11]), "+v"(vf[12]), "+v"(vf[13]), "+v"(vf[14]), "+v"(vf[15]));
    BAR();
    STAMP(15);
    BULK(8);
    {
        asm volatile("" ::: "memory");
        const f32x4 m0 = *(const LAS f32x4*)(P + PM_OFF + (lane & 15) * 48), m1 = *(const LAS f32x4*)(P + PM_OFF + (lane & 15) * 48 + 16);
        const float mx = fmaxf(fmaxf(fmaxf(m0[0], m0[1]), fmaxf(m0[2], m0[3])), fmaxf(fmaxf(m1[0], m1[1]), fmaxf(m1[2], m1[3])));
        *(LAS v2u*)(P + (lane & 15) * 272 + wave * 32 + g4 * 8) = (v2u){pack2(__builtin_amdgcn_exp2f(sc[0] - mx), __builtin_amdgcn_exp2f(sc[1] - mx)),
                                                                        pack2(__builtin_amdgcn_exp2f(sc[2] - mx), __builtin_amdgcn_exp2f(sc[3] - mx))};
        unsigned pa = (unsigned)(size_t)(P + (lane & 15) * 272 + g4 * 16);
        int po = (int)PCTX_OFF + ((((lane & 15) * 4 + cg) * 64 + bb * NKG + kg) * 256 + 32 * wave + 8 * g4) * 2;
        asm volatile("s_waitcnt lgkmcnt(0)" : "+v"(pa), "+v"(po) :: "memory");
        BAR();
        v4u pf[4];
#pragma unroll
        for (int j = 0; j < 4; ++j) asm volatile("ds_read_b128 %0, %1 offset:%2" : "=v"(pf[j]) : "v"(pa), "n"(j * 64) : "memory");
        asm volatile("s_waitcnt lgkmcnt(0)" : "+v"(pf[0]), "+v"(pf[1]), "+v"(pf[2]), "+v"(pf[3]) :: "memory");
        const v4u one = {0x3F803F80u, 0x3F803F80u, 0x3F803F80u, 0x3F803F80u};
        f32x4 o0 = {0.f, 0.f, 0.f, 0.f}, o1 = {0.f, 0.f, 0.f, 0.f}, l = {0.f, 0.f, 0.f, 0.f};
#pragma unroll
        for (int j = 0; j < 4; ++j) {
            const v4u a0 = {vf[(j * 2) * 2][0], vf[(j * 2) * 2][1], vf[(j * 2) * 2 + 1][0], vf[(j * 2) * 2 + 1][1]};
            const v4u a1 = {vf[(j * 2 + 1) * 2][0], vf[(j * 2 + 1) * 2][1], vf[(j * 2 + 1) * 2 + 1][0], vf[(j * 2 + 1) * 2 + 1][1]};
            o0 = MFMA(a0, pf[j], o0);
            o1 = MFMA(a1, pf[j], o1);
            l = MFMA(one, pf[j], l);
        }
        asm volatile("" : "+v"(l));
        v4u v; v[0] = canonb(pack2(o0[0], o0[1])); v[1] = canonb(pack2(o0[2], o0[3])); v[2] = canonb(pack2(o1[0], o1[1])); v[3] = canonb(pack2(o1[2], o1[3]));
        __builtin_amdgcn_raw_buffer_store_b128(v, rs_ws, po, 0, 16);
        {
            if (tid < 16) __builtin_amdgcn_raw_buffer_store_b64((v2u){canon(__float_as_uint(mx)), canon(__float_as_uint(l[0]))}, rs_ws, MLL ? (int)ML_OFF + cu * 128 + lane * 8 : (int)ML_OFF + (((cg * 2 + bb) * 16 + lane) * 32 + kg) * 8, 0, 16);
        }
    }
    STAMP(4);
    PIN();
    BULK(5);
    PIN();
    {
        const int mt = wave & 3, kh = wave >> 2;
        const unsigned char* pg = ws + PCTX_OFF + (size_t)(((th * 4 + cq) * 64 + 16 * mt + g4) * 256 + 128 * kh) * 2;
        const unsigned pr = (unsigned)(size_t)(L + L_PG + wave * 4096 + (lane & 15) * 256);
        const int q = (lane & 15) >> 2, p = lane & 3;
        const int swz = ((q & 2) | ((g4 & 1) << 2)) << 4;
        v4u pa[4];
        v2u uf[32], of[24];
#define OFRD() do { \
            const unsigned oa = (unsigned)(size_t)(L + L_WO + wave * 5120 + (8 * g4 + q) * 128 + 8 * p); \
            const unsigned ob = (unsigned)(size_t)(L + L_WO2 + wave * 4096 + (8 * g4 + q) * 128 + 8 * p); \
            _Pragma("unroll") for (int tt = 0; tt < 4; ++tt) { \
                const unsigned a = oa + ((tt * 32) ^ swz), a2 = ob + ((tt * 32) ^ swz); \
                _Pragma("unroll") for (int u = 0; u < 2; ++u) { \
                    asm volatile("ds_read_b64_tr_b16 %0, %1 offset:%2" : "=v"(of[tt * 6 + u]) : "v"(a), "n"(u * 512) : "memory"); \
                    asm volatile("ds_read_b64_tr_b16 %0, %1 offset:%2" : "=v"(of[tt * 6 + 2 + u]) : "v"(a), "n"(1024 + u * 512) : "memory"); \
                    asm volatile("ds_read_b64_tr_b16 %0, %1 offset:%2" : "=v"(of[tt * 6 + 4 + u]) : "v"(a2), "n"(u * 512) : "memory"); \
                } \
            } } while (0)
#define PGLD(j) __builtin_amdgcn_global_load_lds((const unsigned*)(pg + (j) * 2048 + (((lane & 15) ^ (4 * (j) + g4)) << 4)), (LAS unsigned*)(L + L_PG + wave * 4096 + (j) * 1024), 16, 0, 16)
#define PGRD() do { _Pragma("unroll") for (int ks = 0; ks < 4; ++ks) asm volatile("ds_read_b128 %0, %1" : "=v"(pa[ks]) : "v"(pr + (((4 * ks + g4) ^ (lane & 15)) << 4)) : "memory"); \
                    asm volatile("s_waitcnt lgkmcnt(0)" : "+v"(pa[0]), "+v"(pa[1]), "+v"(pa[2]), "+v"(pa[3]) :: "memory"); } while (0)
#define E3POLL() do { PGLD(0); PGLD(1); PGLD(2); PGLD(3); } while (0)
        if (NSW && last_u() < rank_of(5)) { if (wave == 0) VMW(2 + o_after_u(5)); else VMW2(1, o_after_u(5)); } else VMW2(0, o_after_u(5));
        STAMP(7);
        BAR();
        STAMP(14);
        {
            const unsigned ua = (unsigned)(size_t)(L + L_WV + kh * 16384 + (8 * g4 + q) * 128 + 8 * p);
#pragma unroll
            for (int nt = 0; nt < 4; ++nt) {
                const unsigned a = ua + ((nt * 32) ^ swz);
#pragma unroll
                for (int ks = 0; ks < 4; ++ks)
#pragma unroll
                    for (int u = 0; u < 2; ++u)
                        asm volatile("ds_read_b64_tr_b16 %0, %1 offset:%2" : "=v"(uf[(nt * 4 + ks) * 2 + u]) : "v"(a), "n"(ks * 4096 + u * 512) : "memory");
            }
        }
        SDLY(E3D);
        E3POLL();
        BULK(6);
        if (OEARLY) { if (wave < 7) { VMW(4); OFRD(); } }
        asm volatile("s_waitcnt lgkmcnt(0)" : "+v"(uf[0]), "+v"(uf[1]), "+v"(uf[2]), "+v"(uf[3]), "+v"(uf[4]), "+v"(uf[5]), "+v"(uf[6]), "+v"(uf[7]) :: "memory");
        asm volatile("" : "+v"(uf[8]), "+v"(uf[9]), "+v"(uf[10]), "+v"(uf[11]), "+v"(uf[12]), "+v"(uf[13]), "+v"(uf[14]), "+v"(uf[15]));
        asm volatile("" : "+v"(uf[16]), "+v"(uf[17]), "+v"(uf[18]), "+v"(uf[19]), "+v"(uf[20]), "+v"(uf[21]), "+v"(uf[22]), "+v"(uf[23]));
        asm volatile("" : "+v"(uf[24]), "+v"(uf[25]), "+v"(uf[26]), "+v"(uf[27]), "+v"(uf[28]), "+v"(uf[29]), "+v"(uf[30]), "+v"(uf[31]));
        VMW2(0, cnt_o(6));
        for (unsigned spins = 0;;) {
            PGRD();
            if (!__any(umax(umax(smax(pa[0]), smax(pa[1])), umax(smax(pa[2]), smax(pa[3]))) == SENT)) break;
            if (spin_fail(spins)) break;
            RESEND(spins);
            {
                const unsigned long long bm = __ballot(umax(umax(smax(pa[0]), smax(pa[1])), umax(smax(pa[2]), smax(pa[3]))) == SENT);
                const unsigned rows = ((unsigned)bm | (unsigned)(bm >> 16) | (unsigned)(bm >> 32) | (unsigned)(bm >> 48)) >> g4;
                if (rows & 1u) PGLD(0);
                if (rows & 16u) PGLD(1);
                if (rows & 256u) PGLD(2);
                if (rows & 4096u) PGLD(3);
            }
            VM0();
        }
        STAMP(5);
        BULK(7);
        f32x4 T[4];
#pragma unroll
        for (int nt = 0; nt < 4; ++nt) T[nt] = (f32x4){0.f, 0.f, 0.f, 0.f};
#pragma unroll
        for (int ks = 0; ks < 4; ++ks)
#pragma unroll
            for (int nt = 0; nt < 4; ++nt) {
                const v4u bq = {uf[(nt * 4 + ks) * 2][0], uf[(nt * 4 + ks) * 2][1], uf[(nt * 4 + ks) * 2 + 1][0], uf[(nt * 4 + ks) * 2 + 1][1]};
                T[nt] = MFMA(pa[ks], bq, T[nt]);
            }
        v4u wr;
        for (unsigned spins = 0;;) {
            wr = lds_rd4((const volatile LAS unsigned*)(S + L_CC) + 16 * mt + 4 * g4);
            if (!__any(has_sent(wr))) break;
            if (spin_fail(spins)) break;
            RESEND(spins);
        }
        if (!OEARLY) { if (wave < 7) { VM0(); OFRD(); } }
        float sv[4];
#pragma unroll
        for (int nt = 0; nt < 4; ++nt)
            sv[nt] = (T[nt][0] * __uint_as_float(wr[0]) + T[nt][1] * __uint_as_float(wr[1])) + (T[nt][2] * __uint_as_float(wr[2]) + T[nt][3] * __uint_as_float(wr[3]));
        const auto r0 = __builtin_amdgcn_permlane32_swap(__float_as_uint(sv[0]), __float_as_uint(sv[2]), false, false);
        const auto r1 = __builtin_amdgcn_permlane32_swap(__float_as_uint(sv[1]), __float_as_uint(sv[3]), false, false);
        const float x0 = __uint_as_float(r0[0]) + __uint_as_float(r0[1]), x1 = __uint_as_float(r1[0]) + __uint_as_float(r1[1]);
        const auto r2 = __builtin_amdgcn_permlane16_swap(__float_as_uint(x0), __float_as_uint(x1), false, false);
        const float cv = __uint_as_float(r2[0]) + __uint_as_float(r2[1]);
        {
            const unsigned hi = f2bf(cv), lo = f2bf(cv - __uint_as_float(hi << 16));
            LAS unsigned short* cb = (LAS unsigned short*)(S + L_CB + ((mt << 1) | (kh << 3)) * 144) + ((lane - 8 * ROT * cq) & 63);
            cb[0] = (unsigned short)hi; cb[72] = (unsigned short)lo;
        }
        v4u hb = {0u, 0u, 0u, 0u};
        if (g4 == 0 && (lane & 10) == 0) hb = *(const LAS v4u*)(S + L_HM + ((lane >> 2) * 2 + (lane & 1)) * 16);
        asm volatile("s_waitcnt lgkmcnt(0)" : "+v"(of[0]), "+v"(of[1]), "+v"(of[2]), "+v"(of[3]), "+v"(of[4]), "+v"(of[5]), "+v"(of[6]), "+v"(of[7]) :: "memory");
        asm volatile("" : "+v"(of[8]), "+v"(of[9]), "+v"(of[10]), "+v"(of[11]), "+v"(of[12]), "+v"(of[13]), "+v"(of[14]), "+v"(of[15]));
        asm volatile("" : "+v"(of[16]), "+v"(of[17]), "+v"(of[18]), "+v"(of[19]), "+v"(of[20]), "+v"(of[21]), "+v"(of[22]), "+v"(of[23]));
        STAMP(6);
        BAR();
        STAMP(11);
        if (wave < 7) {
            const v4u b0 = *(const LAS v4u*)(S + L_CB + (lane & 15) * 144 + g4 * 16);
            const v4u b1v = *(const LAS v4u*)(S + L_CB + (lane & 15) * 144 + 64 + g4 * 16);
            f32x4 o[4];
#pragma unroll
            for (int tt = 0; tt < 4; ++tt) {
                const v4u a0 = {of[tt * 6][0], of[tt * 6][1], of[tt * 6 + 1][0], of[tt * 6 + 1][1]};
                o[tt] = (f32x4){0.f, 0.f, 0.f, 0.f};
                o[tt] = MFMA(a0, hb, o[tt]);
            }
#pragma unroll
            for (int tt = 0; tt < 4; ++tt) {
                const v4u a1 = {of[tt * 6 + 2][0], of[tt * 6 + 2][1], of[tt * 6 + 3][0], of[tt * 6 + 3][1]};
                o[tt] = MFMA(a1, b0, o[tt]);
            }
#pragma unroll
            for (int tt = 0; tt < 4; ++tt) {
                const v4u a2 = {of[tt * 6 + 4][0], of[tt * 6 + 4][1], of[tt * 6 + 5][0], of[tt * 6 + 5][1]};
                o[tt] = MFMA(a2, b1v, o[tt]);
            }
            const bool l0 = lane & 1, l1 = lane & 2;
            v4u v;
#pragma unroll
            for (int r = 0; r < 4; ++r) {
                const float y0 = (l0 ? o[1][r] : o[0][r]) + dppf<0xB1>(l0 ? o[0][r] : o[1][r]);
                const float y1 = (l0 ? o[3][r] : o[2][r]) + dppf<0xB1>(l0 ? o[2][r] : o[3][r]);
                float z = (l1 ? y1 : y0) + dppf<0x4E>(l1 ? y0 : y1);
                z += dppf<0x128>(z);
                v[r] = canon(__float_as_uint(z));
            }
            if ((lane & 8) == 0)
                __builtin_amdgcn_raw_buffer_store_b128(v, rs_ws, (int)G5_OFF + (((8 * wave + 2 * (lane & 3) + (g4 >> 1)) * 256 + cu) * 16 + ((lane >> 2) & 1) * 8 + (g4 & 1) * 4) * 4, 0, 16);
        }
    }
    STAMP(8);
    if (cu < NC5) {
        v4u v0 = {SENT, SENT, SENT, SENT}, v1 = {SENT, SENT, SENT, SENT};
        SDLY(E5D);
        for (unsigned spins = 0;;) {
            if (has_sent(v0)) v0 = __builtin_amdgcn_raw_buffer_load_b128(rs_ws, (int)(G5_OFF + (size_t)cu * 16384 + tid * 16), 0, 16);
            if (has_sent(v1)) v1 = __builtin_amdgcn_raw_buffer_load_b128(rs_ws, (int)(G5_OFF + (size_t)cu * 16384 + 8192 + tid * 16), 0, 16);
            if (!__any(umax(smax(v0), smax(v1)) == SENT)) break;
            if (spin_fail(spins)) break;
        }
        f32x4 s; s[0] = __uint_as_float(v0[0]) + __uint_as_float(v1[0]); s[1] = __uint_as_float(v0[1]) + __uint_as_float(v1[1]);
        s[2] = __uint_as_float(v0[2]) + __uint_as_float(v1[2]); s[3] = __uint_as_float(v0[3]) + __uint_as_float(v1[3]);
#pragma unroll
        for (int k = 0; k < 4; ++k) { float v = s[k]; v += dppf<0x124>(v); s[k] = add_hi3(v); }
        LAS float* red = (LAS float*)(S + L_CC);
        if (lane < 4) *(LAS f32x4*)(red + wave * 16 + lane * 4) = s;
        LGKM0();
        BAR();
        if (tid < 16) {
            float t = 0.f;
#pragma unroll
            for (int w = 0; w < 8; ++w) t += red[w * 16 + tid];
            const int b = tid >> 3, m = cu * 8 + (tid & 7);
            t += xb;
            out[b * M + m] = (unsigned short)f2bf(t);
        }
        STAMP(9);
    }
}
}

extern "C" void kernel_launch(void* const* d_in, const int* in_sizes, int n_in,
                              void* d_out, int out_size, void* d_ws, size_t ws_size,
                              hipStream_t stream) {
    const unsigned short* x = (const unsigned short*)d_in[0];
    const unsigned short* ckv = (const unsigned short*)d_in[1];
    const unsigned short* wq = (const unsigned short*)d_in[2];
    const unsigned short* wuk = (const unsigned short*)d_in[3];
    const unsigned short* wuv = (const unsigned short*)d_in[4];
    const unsigned short* wo = (const unsigned short*)d_in[5];
    const unsigned short* w1 = (const unsigned short*)d_in[7];
    const unsigned short* b1 = (const unsigned short*)d_in[8];
    const unsigned short* w2 = (const unsigned short*)d_in[9];
    const unsigned short* b2 = (const unsigned short*)d_in[10];
    unsigned char* ws = (unsigned char*)d_ws;
    k2<<<NCU, 768, 0, stream>>>(x, ckv, wq, w1, wuk, wuv, wo, b1, w2, b2, ws, (unsigned short*)d_out);
}
```
